```python
import torch

def _softmax(x, axis=-1):
    return torch.softmax(x, dim=axis)
def _gelu(x):
    return torch.nn.functional.gelu(x, approximate="tanh")
def _silu(x):
    return torch.nn.functional.silu(x)
def _sigmoid(x):
    return torch.sigmoid(x)
def _layernorm(x, axis=-1, eps=1e-5):
    m = x.mean(dim=axis, keepdim=True)
    v = x.var(dim=axis, keepdim=True, unbiased=False)
    return (x - m) / torch.sqrt(v + eps)
def _scatter_add(tgt, idx, src, axis=0):
    return tgt.clone().index_add_(axis, idx, src)
def _segment_sum(src, seg, n):
    out = torch.zeros((n,) + src.shape[1:], dtype=src.dtype)
    return out.index_add_(0, seg, src)
def _pad(x, pw):
    flat = tuple(v for pair in reversed(pw) for v in pair)
    return torch.nn.functional.pad(x, flat)

kernel_name = 'parallel_sum__mha_block__glu_gated_glu__prompt'

def setup_inputs():

    _w = (lambda a: (lambda t: t.to(torch.bfloat16) if t.is_floating_point() else t)(torch.as_tensor(a)))
    def _A_setup():
        B, S, D, H = 1, 2048, 1024, 32
        return {
            "x": _w((lambda *s: torch.randn(*s, dtype=torch.bfloat16) * 0.05)(B, S, D)),
            "wqkv": _w((lambda *s: torch.randn(*s, dtype=torch.bfloat16) * 0.05)(D, 3 * D)),
            "wo": _w((lambda *s: torch.randn(*s, dtype=torch.bfloat16) * 0.05)(D, D)),
        }
    _A_in = _A_setup()

    def _B_setup():
        B, S, D, F = 1, 2048, 1024, 3584
        return {
            "x": _w((lambda *s: torch.randn(*s, dtype=torch.bfloat16) * 0.05)(B, S, D)),
            "w_gate": _w((lambda *s: torch.randn(*s, dtype=torch.bfloat16) * 0.05)(D, F)),
            "w_up": _w((lambda *s: torch.randn(*s, dtype=torch.bfloat16) * 0.05)(D, F)),
            "w_down": _w((lambda *s: torch.randn(*s, dtype=torch.bfloat16) * 0.05)(F, D)),
        }
    _B_in = _B_setup()

    return {
        "A_x": _A_in["x"],
        "A_wqkv": _A_in["wqkv"],
        "A_wo": _A_in["wo"],
        "B_x": _B_in["x"],
        "B_w_gate": _B_in["w_gate"],
        "B_w_up": _B_in["w_up"],
        "B_w_down": _B_in["w_down"],
    }

def reference(A_x, A_wqkv, A_wo, B_x, B_w_gate, B_w_up, B_w_down):
    def _A_ref(x, wqkv, wo):
        B, S, D = x.shape
        H = 32
        Dh = D // H
        qkv = (lambda x, *d: x.permute(*d))((x @ wqkv).reshape(B, S, 3, H, Dh), 2, 0, 3, 1, 4)
        q, k, v = qkv[0], qkv[1], qkv[2]
        scores = torch.einsum("bhqd,bhkd->bhqk", q, k) * (Dh ** -0.5)
        p = _softmax(scores, axis=-1)
        o = torch.einsum("bhqk,bhkd->bhqd", p, v)
        return (lambda x, *d: x.permute(*d))(o, 0, 2, 1, 3).reshape(B, S, D) @ wo

    def _B_ref(x, w_gate, w_up, w_down):
        g = torch.einsum("bsd,df->bsf", x, w_gate)
        u = torch.einsum("bsd,df->bsf", x, w_up)
        h = _sigmoid(g) * u
        return torch.einsum("bsf,fd->bsd", h, w_down)

    x = A_x
    xn = _layernorm(x, axis=-1)
    return x + _A_ref(xn, A_wqkv, A_wo) + _B_ref(xn, B_w_gate, B_w_up, B_w_down)
```

```cpp
#include <hip/hip_runtime.h>
#include <hip/hip_bf16.h>
typedef unsigned short u16;
__device__ __forceinline__ float bf2f(u16 b){ return __uint_as_float(((unsigned)b)<<16); }
__device__ __forceinline__ u16 f2bf(float f){ unsigned u=__float_as_uint(f); u += 0x7FFFu + ((u>>16)&1u); return (u16)(u>>16); }
__device__ __forceinline__ float rbf(float f){ return bf2f(f2bf(f)); }
constexpr int S=2048,D=1024,H=32,DH=32,F=3584;

__global__ void ln_k(const u16* x, u16* xn){
  int r=blockIdx.x; __shared__ float red[256];
  float s=0; for(int i=threadIdx.x;i<D;i+=256) s+=bf2f(x[r*D+i]);
  red[threadIdx.x]=s; __syncthreads();
  for(int o=128;o>0;o>>=1){ if(threadIdx.x<o) red[threadIdx.x]+=red[threadIdx.x+o]; __syncthreads(); }
  float mean=red[0]/D; __syncthreads();
  float q=0; for(int i=threadIdx.x;i<D;i+=256){ float d=bf2f(x[r*D+i])-mean; q+=d*d; }
  red[threadIdx.x]=q; __syncthreads();
  for(int o=128;o>0;o>>=1){ if(threadIdx.x<o) red[threadIdx.x]+=red[threadIdx.x+o]; __syncthreads(); }
  float var=red[0]/D;
  float mb=rbf(mean), vb=rbf(var); float sd=rbf(sqrtf(rbf(vb+1e-5f)));
  for(int i=threadIdx.x;i<D;i+=256){ float d=rbf(bf2f(x[r*D+i])-mb); xn[r*D+i]=f2bf(d/sd); }
}
__global__ void gemm_k(const u16* A,const u16* B,u16* C,int M,int N,int K){
  __shared__ float As[16][65], Bs[16][65];
  int tx=threadIdx.x%16, ty=threadIdx.x/16; int m0=blockIdx.y*64,n0=blockIdx.x*64;
  float acc[4][4]={};
  for(int k0=0;k0<K;k0+=16){
    for(int i=threadIdx.x;i<64*16;i+=256){ int m=i/16,k=i%16; As[k][m]=bf2f(A[(size_t)(m0+m)*K+k0+k]); }
    for(int i=threadIdx.x;i<64*16;i+=256){ int k=i/64,n=i%64; Bs[k][n]=bf2f(B[(size_t)(k0+k)*N+n0+n]); }
    __syncthreads();
    for(int k=0;k<16;++k){ float a[4],b[4]; for(int i=0;i<4;++i){a[i]=As[k][ty*4+i]; b[i]=Bs[k][tx*4+i];}
      for(int i=0;i<4;++i)for(int j=0;j<4;++j) acc[i][j]+=a[i]*b[j]; }
    __syncthreads();
  }
  for(int i=0;i<4;++i)for(int j=0;j<4;++j) C[(size_t)(m0+ty*4+i)*N+n0+tx*4+j]=f2bf(acc[i][j]);
}
__global__ void attn_k(const u16* qkv,u16* o){
  int h=blockIdx.y, q=blockIdx.x*256+threadIdx.x;
  float qv[DH]; for(int d=0;d<DH;++d) qv[d]=bf2f(qkv[(size_t)q*3*D+h*DH+d]);
  float m=-1e30f,l=0,acc[DH]; for(int d=0;d<DH;++d)acc[d]=0;
  for(int k=0;k<S;++k){ float s=0; for(int d=0;d<DH;++d) s+=qv[d]*bf2f(qkv[(size_t)k*3*D+D+h*DH+d]);
    s=rbf(rbf(s)*0.17677669529663687f);
    float mn=fmaxf(m,s); float a=__expf(m-mn), p=__expf(s-mn); l=l*a+p; for(int d=0;d<DH;++d) acc[d]=acc[d]*a+p*bf2f(qkv[(size_t)k*3*D+2*D+h*DH+d]); m=mn; }
  for(int d=0;d<DH;++d) o[(size_t)q*D+h*DH+d]=f2bf(acc[d]/l);
}
__global__ void glu_k(const u16* g,const u16* u,u16* h,int n){ int i=blockIdx.x*256+threadIdx.x; if(i<n){ float s=rbf(1.f/(1.f+__expf(-bf2f(g[i])))); h[i]=f2bf(s*bf2f(u[i])); } }
__global__ void fin_k(const u16* x,const u16* a,const u16* b,u16* o,int n){ int i=blockIdx.x*256+threadIdx.x; if(i<n){ o[i]=f2bf(rbf(bf2f(x[i])+bf2f(a[i]))+bf2f(b[i])); } }

extern "C" void kernel_launch(void* const* d_in, const int* in_sizes, int n_in, void* d_out, int out_size, void* d_ws, size_t ws_size, hipStream_t stream){
  const u16* x=(const u16*)d_in[0]; const u16* wqkv=(const u16*)d_in[1]; const u16* wo=(const u16*)d_in[2];
  const u16* wg=(const u16*)d_in[4]; const u16* wu=(const u16*)d_in[5]; const u16* wd=(const u16*)d_in[6];
  u16* ws=(u16*)d_ws; u16* xn=ws; u16* qkv=xn+S*D; u16* o=qkv+S*3*D; u16* ao=o+S*D; u16* g=ao+S*D; u16* u=g+S*F; u16* h=u+S*F; u16* fo=h+S*F;
  ln_k<<<S,256,0,stream>>>(x,xn);
  gemm_k<<<dim3(3*D/64,S/64),256,0,stream>>>(xn,wqkv,qkv,S,3*D,D);
  attn_k<<<dim3(S/256,H),256,0,stream>>>(qkv,o);
  gemm_k<<<dim3(D/64,S/64),256,0,stream>>>(o,wo,ao,S,D,D);
  gemm_k<<<dim3(F/64,S/64),256,0,stream>>>(xn,wg,g,S,F,D);
  gemm_k<<<dim3(F/64,S/64),256,0,stream>>>(xn,wu,u,S,F,D);
  glu_k<<<S*F/256,256,0,stream>>>(g,u,h,S*F);
  gemm_k<<<dim3(D/64,S/64),256,0,stream>>>(h,wd,fo,S,D,F);
  fin_k<<<S*D/256,256,0,stream>>>(x,ao,fo,(u16*)d_out,S*D);
}
```

```cpp
#include <hip/hip_runtime.h>
#include <hip/hip_bf16.h>
#include <stdint.h>

typedef unsigned short u16;
typedef short bf16x8 __attribute__((ext_vector_type(8)));
typedef short s16x4 __attribute__((ext_vector_type(4)));
typedef float f32x16 __attribute__((ext_vector_type(16)));
typedef float f32x4 __attribute__((ext_vector_type(4)));
typedef float f32x2 __attribute__((ext_vector_type(2)));
typedef unsigned u32x4 __attribute__((ext_vector_type(4)));
typedef __bf16 bf16x2_t __attribute__((ext_vector_type(2)));
typedef __amdgpu_buffer_rsrc_t srd_t;
#define LAS __attribute__((address_space(3)))
#define DI __device__ __forceinline__
#define SBAR() __builtin_amdgcn_sched_barrier(0)
#define MFMA32(a, b, c) __builtin_amdgcn_mfma_f32_32x32x16_bf16((a), (b), (c), 0, 0, 0)
#define MFMA16(a, b, c) __builtin_amdgcn_mfma_f32_16x16x32_bf16((a), (b), (c), 0, 0, 0)

constexpr int S = 2048, D = 1024, NH = 32, F = 3584;
constexpr int NKB = (F + D) / 32;
constexpr int NSUB = (F + D) / 16;
constexpr float QSCALE = 0.17677669529663687f * 1.4426950408889634f;

constexpr size_t WS_XN = 0;
constexpr size_t WS_Q = WS_XN + (size_t)S * D * 2;
constexpr size_t WS_KV = WS_Q + (size_t)S * D * 2;
constexpr size_t WS_X2 = WS_KV + (size_t)S * D * 4;
constexpr size_t X2_BYTES = (size_t)S * (F + D) * 2;
constexpr size_t WS_CNT = WS_X2 + X2_BYTES;

DI unsigned rfl(unsigned x) { return __builtin_amdgcn_readfirstlane(x); }
DI unsigned cvtpk(float lo, float hi) { f32x2 v = {lo, hi}; bf16x2_t b = __builtin_convertvector(v, bf16x2_t); return __builtin_bit_cast(unsigned, b); }
DI float bf2f(u16 b) { return __uint_as_float(((unsigned)b) << 16); }
DI float rbf(float f) { return __uint_as_float(cvtpk(f, f) << 16); }
DI srd_t mksrd(const void* p, unsigned bytes) { return __builtin_amdgcn_make_buffer_rsrc((void*)p, 0, bytes, 0x00020000); }
DI void dma16(srd_t srd, unsigned voff, unsigned soff, unsigned m0v) {
  asm volatile("s_mov_b32 m0, %0\n\ts_nop 0\n\tbuffer_load_dwordx4 %1, %2, %3 offen lds" ::"s"(m0v), "v"(voff), "s"(srd), "s"(soff) : "memory");
}
typedef short v4i16_t __attribute__((ext_vector_type(4)));
DI s16x4 ldstr(LAS const char* p) { return __builtin_bit_cast(s16x4, __builtin_amdgcn_ds_read_tr16_b64_v4i16((LAS v4i16_t*)p)); }
DI bf16x8 lds128(LAS const char* p) { return *(LAS const bf16x8*)p; }
DI bf16x8 cat8(s16x4 lo, s16x4 hi) { return __builtin_shufflevector(lo, hi, 0, 1, 2, 3, 4, 5, 6, 7); }
DI u32x4 pack8(const f32x16& v, int s, float sc) {
  u32x4 w;
  w[0] = cvtpk(v[8 * s + 0] * sc, v[8 * s + 1] * sc); w[1] = cvtpk(v[8 * s + 2] * sc, v[8 * s + 3] * sc);
  w[2] = cvtpk(v[8 * s + 4] * sc, v[8 * s + 5] * sc); w[3] = cvtpk(v[8 * s + 6] * sc, v[8 * s + 7] * sc);
  return w;
}

#define STAMP_DECL() do {} while (0)
#define STAMP(i) do {} while (0)
#define STAMP_OUT(ws, region) do {} while (0)
__global__ __launch_bounds__(512) void ln_kernel(const u16* __restrict__ x, u16* __restrict__ xn, unsigned* cnt) {
  if (blockIdx.x == 0 && threadIdx.x < 32) cnt[threadIdx.x * 64] = 0u;
  const int lane = threadIdx.x & 63, row = blockIdx.x * 8 + (threadIdx.x >> 6);
  const u32x4* xr = (const u32x4*)(x + (size_t)row * D);
  u32x4 a = xr[lane], b = xr[lane + 64];
  float v[16];
#pragma unroll
  for (int i = 0; i < 4; ++i) { v[2 * i] = __uint_as_float(a[i] << 16); v[2 * i + 1] = __uint_as_float(a[i] & 0xffff0000u); v[8 + 2 * i] = __uint_as_float(b[i] << 16); v[9 + 2 * i] = __uint_as_float(b[i] & 0xffff0000u); }
  float s = 0.f;
#pragma unroll
  for (int i = 0; i < 16; ++i) s += v[i];
#pragma unroll
  for (int o = 1; o < 64; o <<= 1) s += __shfl_xor(s, o);
  const float mean = s * (1.f / D);
  float q = 0.f;
#pragma unroll
  for (int i = 0; i < 16; ++i) { const float d = v[i] - mean; q += d * d; }
#pragma unroll
  for (int o = 1; o < 64; o <<= 1) q += __shfl_xor(q, o);
  const float mb = rbf(mean), vb = rbf(q * (1.f / D));
  const float sd = rbf(sqrtf(rbf(vb + 1e-5f)));
  const float inv = 1.f / sd;
  u32x4 oa, ob;
#pragma unroll
  for (int i = 0; i < 4; ++i) { oa[i] = cvtpk(rbf(v[2 * i] - mb) * inv, rbf(v[2 * i + 1] - mb) * inv); ob[i] = cvtpk(rbf(v[8 + 2 * i] - mb) * inv, rbf(v[9 + 2 * i] - mb) * inv); }
  u32x4* o = (u32x4*)(xn + (size_t)row * D);
  o[lane] = oa; o[lane + 64] = ob;
}

constexpr int G1_XS = 32768, G1_WB = 65536, G1_WS = 40960, G1_LDS = G1_WB + 2 * G1_WS;
DI void mfma16a(f32x4& c, const bf16x8& a, const bf16x8& b) { asm volatile("v_mfma_f32_16x16x32_bf16 %0, %1, %2, %0" : "+v"(c) : "v"(a), "v"(b)); }
DI u32x4 pack44(const f32x4& a, const f32x4& b, float sc) {
  u32x4 w; w[0] = cvtpk(a[0] * sc, a[1] * sc); w[1] = cvtpk(a[2] * sc, a[3] * sc); w[2] = cvtpk(b[0] * sc, b[1] * sc); w[3] = cvtpk(b[2] * sc, b[3] * sc); return w;
}
template <bool SWAP0>
DI void g1_body(const u16* xn, const u16* wqkv, const u16* wg, const u16* wu, char* ws, LAS char* lds, int mt, int j) {
  const int tid = threadIdx.x, lane = tid & 63, wid = rfl(tid >> 6), wr = wid & 3, wc = wid >> 2;
  const bool isY = j >= 28;
  const int a0 = j < 16 ? 32 + j : (j < 28 ? j - 16 : 12 + 5 * (j - 28));
  const srd_t sx = mksrd(xn, S * D * 2), sq = mksrd(wqkv, D * 3 * D * 2);
  const srd_t sa = isY ? sq : mksrd(wg, D * F * 2), sb = isY ? sq : mksrd(wu, D * F * 2);
  const unsigned ld1 = isY ? 6144u : 7168u;
  const unsigned c0 = a0 * 128u, c1 = isY ? c0 + 128u : j * 256u, c2 = isY ? c0 + 256u : j * 256u, c3 = isY ? c0 + 384u : j * 256u + 128u, c4 = isY ? c0 + 512u : j * 256u + 128u;
  const unsigned l3 = lane >> 3, l7 = lane & 7, l4 = lane >> 4;
  const unsigned vx0 = l3 * 2048u + ((l7 ^ l4) << 4), vx1 = vx0 ^ 64u;
  const unsigned wsw = (l7 ^ (((l4 & 1) | ((wid & 1) << 1)) << 1)) << 4;
  const unsigned vw0 = l3 * 6144u + wsw, vw1 = l3 * ld1 + wsw;
  const unsigned ldsb = (unsigned)(uintptr_t)lds;
  unsigned sox = (unsigned)(mt * 256 + wid * 32) * 2048u;
  unsigned so0 = wid * 8 * 6144u + c0, so1 = wid * 8 * ld1;
  const unsigned m0x = ldsb + wid * 4096u, m0w = ldsb + G1_WB + wid * 1024u;
#define G1_DMA(i, st)                                                                   \
  do {                                                                                  \
    if ((i) == 0) dma16(sx, vx0, sox, m0x + (st) * G1_XS);                               \
    if ((i) == 1) dma16(sq, vw0, so0, m0w + (st) * G1_WS);                               \
    if ((i) == 2) dma16(sx, vx1, sox + 16384u, m0x + (st) * G1_XS + 1024u);              \
    if ((i) == 3) dma16(sa, vw1, so1 + c1, m0w + (st) * G1_WS + 8192u);                  \
    if ((i) == 4) dma16(sx, vx0, sox + 32768u, m0x + (st) * G1_XS + 2048u);              \
    if ((i) == 5) dma16(sb, vw1, so1 + c2, m0w + (st) * G1_WS + 16384u);                 \
    if ((i) == 6) dma16(sx, vx1, sox + 49152u, m0x + (st) * G1_XS + 3072u);              \
    if ((i) == 7) dma16(sa, vw1, so1 + c3, m0w + (st) * G1_WS + 24576u);                 \
    if ((i) == 8) dma16(sb, vw1, so1 + c4, m0w + (st) * G1_WS + 32768u);                 \
  } while (0)
#define G1_ADV() do { sox += 128u; so0 += 64u * 6144u; so1 += 64u * ld1; } while (0)
  const int t16 = lane & 15, g = lane >> 4, q4 = t16 >> 2, p4 = lane & 3;
  unsigned xa0 = ldsb + (wr * 64 + t16) * 128 + ((g ^ ((t16 >> 1) & 7)) << 4), xa1 = xa0 ^ 64u;
  unsigned wa00 = ldsb + G1_WB + (8 * g + q4) * 128 + (((2 * wc) ^ (((q4 >> 1) & 1) | ((g & 1) << 1))) << 5) + p4 * 8;
  unsigned wa01 = wa00 ^ 32u, wa10 = wa00 + G1_WS, wa11 = wa01 + G1_WS;
  asm volatile("" : "+v"(xa0), "+v"(xa1), "+v"(wa00), "+v"(wa01), "+v"(wa10), "+v"(wa11));

  f32x4 acc[4][10];
#pragma unroll
  for (int m = 0; m < 4; ++m)
#pragma unroll
    for (int n = 0; n < 10; ++n) acc[m][n] = f32x4{};
  bf16x8 xf[2][4];
  s16x4 wl[10], wh[10];
#define LP(a) ((LAS const char*)(uintptr_t)(a))
#define G1_RX(st, s, m) xf[s][m] = lds128(LP((s) ? xa1 : xa0) + (st) * G1_XS + (m) * 2048)
#define G1_RW(st, s, n)                                                                                      \
  do {                                                                                                       \
    LAS const char* p_ = LP((st) ? (((n) & 1) ? wa11 : wa10) : (((n) & 1) ? wa01 : wa00)) + ((n) >> 1) * 8192 + (s) * 4096; \
    wl[n] = ldstr(p_); wh[n] = ldstr(p_ + 512);                                                              \
  } while (0)
#define G1_MM(s, n, m)                                                                                       \
  do {                                                                                                       \
    if (SWAP0 && (n) < 2) mfma16a(acc[m][n], xf[s][m], cat8(wl[n], wh[n]));                                   \
    else mfma16a(acc[m][n], cat8(wl[n], wh[n]), xf[s][m]);                                                    \
  } while (0)
#define G1_GRP(s, n, A, B, C) do { G1_MM(s, n, 0); SBAR(); A; SBAR(); G1_MM(s, n, 1); SBAR(); B; SBAR(); G1_MM(s, n, 2); SBAR(); C; SBAR(); G1_MM(s, n, 3); SBAR(); } while (0)
#define NOP_ ((void)0)
#define G1_WAITBAR() asm volatile("s_waitcnt vmcnt(0) lgkmcnt(0)\n\ts_barrier" ::: "memory")
#define G1_TILE(st, NX, DM)                                                                                  \
  do {                                                                                                       \
    G1_GRP(0, 0, G1_RW(st, 0, 9), NOP_, NOP_);                                                               \
    G1_GRP(0, 1, G1_RW(st, 1, 0), G1_RX(st, 1, 0), NOP_);                                                    \
    G1_GRP(0, 2, G1_RW(st, 1, 1), G1_RX(st, 1, 1), NOP_);                                                    \
    G1_GRP(0, 3, G1_RW(st, 1, 2), G1_RX(st, 1, 2), NOP_);                                                    \
    G1_GRP(0, 4, G1_RW(st, 1, 3), G1_RX(st, 1, 3), NOP_);                                                    \
    G1_GRP(0, 5, G1_RW(st, 1, 4), NOP_, NOP_);                                                               \
    G1_GRP(0, 6, G1_RW(st, 1, 5), NOP_, NOP_);                                                               \
    G1_GRP(0, 7, G1_RW(st, 1, 6), NOP_, NOP_);                                                               \
    G1_GRP(0, 8, G1_RW(st, 1, 7), NOP_, NOP_);                                                               \
    G1_GRP(0, 9, G1_RW(st, 1, 8), NOP_, NOP_);                                                               \
    G1_GRP(1, 0, G1_RW(st, 1, 9), NOP_, NOP_);                                                               \
    G1_GRP(1, 1, NOP_, NOP_, NOP_);                                                                          \
    G1_GRP(1, 2, NOP_, NOP_, NOP_);                                                                          \
    if (NX) G1_WAITBAR();                                                                                    \
    G1_GRP(1, 3, if (NX) G1_RX(st ^ 1, 0, 0), if (NX) G1_RW(st ^ 1, 0, 0), if (DM) G1_DMA(0, st));            \
    G1_GRP(1, 4, if (NX) G1_RX(st ^ 1, 0, 1), if (NX) G1_RW(st ^ 1, 0, 1), if (DM) G1_DMA(1, st));            \
    G1_GRP(1, 5, if (NX) G1_RX(st ^ 1, 0, 2), if (NX) G1_RW(st ^ 1, 0, 2), if (DM) G1_DMA(2, st));            \
    G1_GRP(1, 6, if (NX) G1_RX(st ^ 1, 0, 3), if (NX) G1_RW(st ^ 1, 0, 3), if (DM) G1_DMA(3, st));            \
    G1_GRP(1, 7, if (NX) G1_RW(st ^ 1, 0, 4), if (NX) G1_RW(st ^ 1, 0, 5), if (DM) { G1_DMA(4, st); G1_DMA(5, st); }); \
    G1_GRP(1, 8, if (NX) G1_RW(st ^ 1, 0, 6), if (NX) G1_RW(st ^ 1, 0, 7), if (DM) { G1_DMA(6, st); G1_DMA(7, st); }); \
    G1_GRP(1, 9, if (NX) G1_RW(st ^ 1, 0, 8), NOP_, if (DM) { G1_DMA(8, st); G1_ADV(); });                    \
  } while (0)

  STAMP_DECL(); STAMP(0);
  G1_DMA(0, 0); G1_DMA(1, 0); G1_DMA(2, 0); G1_DMA(3, 0); G1_DMA(4, 0); G1_DMA(5, 0); G1_DMA(6, 0); G1_DMA(7, 0); G1_DMA(8, 0);
  G1_ADV();
  G1_DMA(0, 1); G1_DMA(1, 1); G1_DMA(2, 1); G1_DMA(3, 1); G1_DMA(4, 1); G1_DMA(5, 1); G1_DMA(6, 1); G1_DMA(7, 1); G1_DMA(8, 1);
  G1_ADV();
  asm volatile("s_waitcnt vmcnt(9)\n\ts_barrier" ::: "memory");
  G1_RX(0, 0, 0); G1_RW(0, 0, 0); G1_RX(0, 0, 1); G1_RX(0, 0, 2); G1_RX(0, 0, 3);
  G1_RW(0, 0, 1); G1_RW(0, 0, 2); G1_RW(0, 0, 3); G1_RW(0, 0, 4); G1_RW(0, 0, 5); G1_RW(0, 0, 6); G1_RW(0, 0, 7); G1_RW(0, 0, 8);
  SBAR();
  STAMP(1);
  for (int t = 0; t < 14; t += 2) { G1_TILE(0, true, true); G1_TILE(1, true, true); }
  G1_TILE(0, true, false);
  G1_TILE(1, false, false);
  asm volatile("s_nop 15" ::: "memory"); SBAR();
  STAMP(2);
  {
  int tz = threadIdx.x; asm volatile("" : "+v"(tz));
  const int lane = tz & 63, t16 = lane & 15, g = lane >> 4, q4 = t16 >> 2, p4 = lane & 3;
  const int T32 = mt * 8 + wr * 2;
  const int rho = 16 * (q4 >> 1) + 4 * (q4 & 1) + p4;
  auto store_qk = [&](int a, const f32x4& v0, const f32x4& v1, int m) {
    const int head = 2 * (a & 15) + wc; const bool isq = a < 16;
    char* dst = isq ? ws + WS_Q + (size_t)(head * 64 + T32 + (m >> 1)) * 2048 + (16 * (m & 1) + t16) * 16 : ws + WS_KV + (size_t)(head * 64 + T32 + (m >> 1)) * 4096 + (rho + 8 * (m & 1)) * 16;
    *(u32x4*)(dst + g * 512) = pack44(v0, v1, isq ? QSCALE : 1.f);
  };
  if (SWAP0) {
    const int head = 2 * (a0 & 15) + wc;
#pragma unroll
    for (int mm = 0; mm < 2; ++mm)
#pragma unroll
      for (int c = 0; c < 2; ++c)
        *(u32x4*)(ws + WS_KV + (size_t)(head * 64 + T32 + mm) * 4096 + 2048 + g * 512 + (rho + 8 * c) * 16) = pack44(acc[2 * mm][c], acc[2 * mm + 1][c], 1.f);
  } else {
#pragma unroll
    for (int m = 0; m < 4; ++m) store_qk(a0, acc[m][0], acc[m][1], m);
  }
  if (!SWAP0 && isY) {
#pragma unroll
    for (int u = 1; u < 5; ++u)
#pragma unroll
      for (int m = 0; m < 4; ++m) store_qk(a0 + u, acc[m][2 * u], acc[m][2 * u + 1], m);
  } else {
#pragma unroll
    for (int pr = 0; pr < 2; ++pr)
#pragma unroll
      for (int m = 0; m < 4; ++m) {
        const int kb = 2 * (2 * j + pr) + wc, tok = mt * 256 + wr * 64 + m * 16;
        f32x4 hv[2];
#pragma unroll
        for (int c = 0; c < 2; ++c)
#pragma unroll
          for (int r = 0; r < 4; ++r) {
            const float e = __builtin_amdgcn_exp2f(acc[m][2 + 4 * pr + c][r] * -1.4426950408889634f);
            hv[c][r] = acc[m][4 + 4 * pr + c][r] * __builtin_amdgcn_rcpf(1.f + e);
          }
        *(u32x4*)(ws + WS_X2 + ((size_t)(tok >> 7) * NKB + kb) * 8192 + g * 2048 + ((tok & 127) + t16) * 16) = pack44(hv[0], hv[1], 1.f);
      }
  }
  }
  STAMP(3);
  STAMP(4); STAMP_OUT(ws, 0);
}

__global__ __launch_bounds__(512, 2) void g1_kernel(const u16* xn, const u16* wqkv, const u16* wg, const u16* wu, char* ws) {
  extern __shared__ __attribute__((aligned(16))) char smem[];
  const int b = blockIdx.x, xcd = b & 7, r = b >> 3, mt = r & 7, j = xcd + 8 * (r >> 3);
  if (j < 16) g1_body<true>(xn, wqkv, wg, wu, ws, (LAS char*)smem, mt, j);
  else g1_body<false>(xn, wqkv, wg, wu, ws, (LAS char*)smem, mt, j);
}

constexpr int AT_LDS = 65536;
#define PIN(x) asm volatile("" : "+v"(x))
#define EX2(v) __builtin_amdgcn_exp2f(v)
DI void att_slow(const char* kv, const bf16x8 (&qf)[2][2], f32x16 (&o)[2], float (&linv)[2], int lane) {
#pragma unroll
  for (int m = 0; m < 2; ++m) {
    f32x16 oo = f32x16{}; float mref = -INFINITY, l = 0.f;
    for (int kt = 0; kt < 64; ++kt) {
      const char* p = kv + kt * 4096 + lane * 16;
      const bf16x8 k0 = *(const bf16x8*)p, k1 = *(const bf16x8*)(p + 1024), v0 = *(const bf16x8*)(p + 2048), v1 = *(const bf16x8*)(p + 3072);
      f32x16 s = MFMA32(k0, qf[m][0], f32x16{});
      s = MFMA32(k1, qf[m][1], s);
      float mx = s[0];
#pragma unroll
      for (int r = 1; r < 16; ++r) mx = fmaxf(mx, s[r]);
      mx = fmaxf(fmaxf(mx, __shfl_xor(mx, 32)), mref);
      const float f = EX2(mref - mx);
      mref = mx;
      float sum = 0.f;
#pragma unroll
      for (int r = 0; r < 16; ++r) { s[r] = EX2(s[r] - mref); sum += s[r]; oo[r] *= f; }
      l = l * f + sum;
      oo = MFMA32(v0, __builtin_bit_cast(bf16x8, pack8(s, 0, 1.f)), oo);
      oo = MFMA32(v1, __builtin_bit_cast(bf16x8, pack8(s, 1, 1.f)), oo);
    }
    o[m] = oo; linv[m] = 1.f / (l + __shfl_xor(l, 32));
  }
}
typedef __attribute__((address_space(1))) unsigned gu32;
#define RLX_AGENT __ATOMIC_RELAXED, __HIP_MEMORY_SCOPE_AGENT
constexpr int P2_G2RING = 65536, P2_CTL = 131072, P2_LDS = P2_CTL + 64;
constexpr unsigned SPIN_CAP = 1u << 22;
#define G2_NB 12
DI void st16_sc1(srd_t srd, unsigned voff, u32x4 v) { __builtin_amdgcn_raw_buffer_store_b128(v, srd, voff, 0, 16); }

DI void att_role(char* ws, LAS char* lds, int head, int qb, int tg, int wid, int lane) {
  const char* kvg = ws + WS_KV + (size_t)head * 262144;
  const srd_t skv = mksrd(kvg, 262144);
  const unsigned ldsb = (unsigned)(uintptr_t)lds;
  const unsigned voff = lane * 16u;
#define AT_DMA1(c, i) dma16(skv, voff, (unsigned)(c) * 16384u + wid * 4096u + (i) * 1024u, ldsb + ((c) & 3) * 16384u + wid * 4096u + (i) * 1024u)
  STAMP_DECL(); STAMP(0);
  AT_DMA1(0, 0); AT_DMA1(0, 1); AT_DMA1(0, 2); AT_DMA1(0, 3);
  bf16x8 qf[2][2];
  const char* qp = ws + WS_Q + (size_t)(head * 64 + qb * 8 + wid * 2) * 2048 + lane * 16;
#pragma unroll
  for (int m = 0; m < 2; ++m)
#pragma unroll
    for (int s = 0; s < 2; ++s) qf[m][s] = *(const bf16x8*)(qp + m * 2048 + s * 1024);
  AT_DMA1(1, 0); AT_DMA1(1, 1); AT_DMA1(1, 2); AT_DMA1(1, 3);
  PIN(qf[0][0]); PIN(qf[0][1]); PIN(qf[1][0]); PIN(qf[1][1]);
  AT_DMA1(2, 0); AT_DMA1(2, 1); AT_DMA1(2, 2); AT_DMA1(2, 3);
  f32x16 o[2] = {f32x16{}, f32x16{}};
  f32x4 ls[2] = {f32x4{}, f32x4{}};
  bf16x8 sel;
  { const short one = ((lane & 1) == ((lane >> 4) & 1) && (lane & 2) == 0) ? (short)0x3f80 : (short)0;
#pragma unroll
    for (int e = 0; e < 8; ++e) sel[e] = one; }
  PIN(sel);
  asm volatile("s_waitcnt vmcnt(4)\n\ts_barrier" ::: "memory");
  bf16x8 kf[2][2], vf[2][2];
  f32x16 sa, sb, negm[2];
  u32x4 pa0, pa1, pb0, pb1;
  unsigned kp = ldsb + lane * 16u;
  kf[0][0] = lds128(LP(kp)); kf[0][1] = lds128(LP(kp) + 1024);
  {
    sa = MFMA32(kf[0][0], qf[0][0], f32x16{}); sa = MFMA32(kf[0][1], qf[0][1], sa);
    sb = MFMA32(kf[0][0], qf[1][0], f32x16{}); sb = MFMA32(kf[0][1], qf[1][1], sb);
    float ma = sa[0], mb = sb[0];
#pragma unroll
    for (int r = 1; r < 16; ++r) { ma = fmaxf(ma, sa[r]); mb = fmaxf(mb, sb[r]); }
    ma = fmaxf(ma, __shfl_xor(ma, 32)); mb = fmaxf(mb, __shfl_xor(mb, 32));
#pragma unroll
    for (int r = 0; r < 16; ++r) { negm[0][r] = -ma; negm[1][r] = -mb; sa[r] -= ma; }
  }
  PIN(negm[0]); PIN(negm[1]);
  pb0 = u32x4{}; pb1 = u32x4{}; vf[1][0] = bf16x8{}; vf[1][1] = bf16x8{};
  PIN(pb0); PIN(pb1); PIN(vf[1][0]); PIN(vf[1][1]);
#define BF(x) __builtin_bit_cast(bf16x8, x)
#define E1(S_, i) S_[i] = EX2(S_[i])
#define CV(P_, i, S_, r) P_[i] = cvtpk(S_[r], S_[(r) + 1])
#define AT_UNIT(SC, PC0, PC1, SN, kz, mn, PP0, PP1, vz, X0, X1, X2, X3)                                       \
  do {                                                                                                       \
    SN = MFMA32(kf[kz][0], qf[mn][0], negm[mn]); SBAR(); X0;                                                  \
    E1(SC, 0); E1(SC, 1); E1(SC, 2); E1(SC, 3); PIN(SC); SBAR();                                               \
    SN = MFMA32(kf[kz][1], qf[mn][1], SN); SBAR(); X1;                                                        \
    CV(PC0, 0, SC, 0); CV(PC0, 1, SC, 2); E1(SC, 4); E1(SC, 5); E1(SC, 6); PIN(SC); PIN(PC0); SBAR();           \
    o[mn] = MFMA32(vf[vz][0], BF(PP0), o[mn]); SBAR(); X2;                                                    \
    E1(SC, 7); CV(PC0, 2, SC, 4); E1(SC, 8); CV(PC0, 3, SC, 6); E1(SC, 9); PIN(SC); PIN(PC0); SBAR();           \
    ls[mn] = MFMA16(sel, BF(PP0), ls[mn]); SBAR();                                                            \
    E1(SC, 10); E1(SC, 11); PIN(SC); SBAR();                                                                  \
    o[mn] = MFMA32(vf[vz][1], BF(PP1), o[mn]); SBAR(); X3;                                                    \
    CV(PC1, 0, SC, 8); E1(SC, 12); CV(PC1, 1, SC, 10); E1(SC, 13); E1(SC, 14); E1(SC, 15); PIN(SC); PIN(PC1); SBAR(); \
    ls[mn] = MFMA16(sel, BF(PP1), ls[mn]); SBAR();                                                            \
    CV(PC1, 2, SC, 12); CV(PC1, 3, SC, 14); PIN(PC1); SBAR();                                                  \
  } while (0)
#define AT_KT(z, KADDR, VADDR, D0)                                                                            \
  do {                                                                                                       \
    AT_UNIT(sa, pa0, pa1, sb, z, 1, pb0, pb1, z ^ 1, kf[z ^ 1][0] = lds128(KADDR), kf[z ^ 1][1] = lds128((KADDR) + 1024), vf[z][0] = lds128(VADDR), vf[z][1] = lds128((VADDR) + 1024)); \
    AT_UNIT(sb, pb0, pb1, sa, z ^ 1, 0, pa0, pa1, z, D0, NOP_, NOP_, NOP_);                                    \
  } while (0)
  STAMP(1);
  for (int c = 0; c < 16; ++c) {
    const unsigned cp = kp + (c & 3) * 16384u, cn = kp + ((c + 1) & 3) * 16384u;
    const int cd = c + 3 + (c + 3 < 16 ? 0 : 64);
    AT_KT(0, LP(cp) + 4096, LP(cp) + 2048, AT_DMA1(cd, 0));
    AT_KT(1, LP(cp) + 8192, LP(cp) + 4096 + 2048, AT_DMA1(cd, 1));
    AT_KT(0, LP(cp) + 12288, LP(cp) + 8192 + 2048, AT_DMA1(cd, 2));
    AT_KT(1, LP(cn), LP(cp) + 12288 + 2048, AT_DMA1(cd, 3));
    asm volatile("s_waitcnt vmcnt(4) lgkmcnt(0)\n\ts_barrier" ::: "memory");
  }
  STAMP(2);
  o[1] = MFMA32(vf[1][0], BF(pb0), o[1]); ls[1] = MFMA16(sel, BF(pb0), ls[1]);
  o[1] = MFMA32(vf[1][1], BF(pb1), o[1]); ls[1] = MFMA16(sel, BF(pb1), ls[1]);
  float linv[2];
  bool bad = false;
#pragma unroll
  for (int m = 0; m < 2; ++m) {
    const float lt = (lane & 16) ? ls[m][1] : ls[m][0];
    float chk = lt, nanp = lt;
#pragma unroll
    for (int r = 0; r < 16; ++r) { chk = fmaxf(chk, fabsf(o[m][r])); nanp += o[m][r]; }
    bad = bad || !(chk < 1e30f) || (nanp != nanp);
    linv[m] = 1.f / lt;
  }
  if (__builtin_expect(__any(bad), 0)) att_slow(kvg, qf, o, linv, lane);
  const srd_t sx2 = mksrd(ws + WS_X2, (unsigned)X2_BYTES);
#pragma unroll
  for (int m = 0; m < 2; ++m) {
    const int T = qb * 8 + wid * 2 + m;
    const unsigned off = ((T >> 2) * NKB + 112 + head) * 8192u + (lane >> 5) * 2048u + ((T & 3) * 32 + (lane & 31)) * 16u;
    st16_sc1(sx2, off, pack8(o[m], 0, linv[m]));
    st16_sc1(sx2, off + 4096u, pack8(o[m], 1, linv[m]));
  }
  asm volatile("s_waitcnt vmcnt(0)" ::: "memory");
  STAMP(3);
  LAS unsigned* ctl = (LAS unsigned*)(lds + P2_CTL);
  gu32* cnt = (gu32*)(ws + WS_CNT);
  if (lane == 0) {
    const unsigned prev = __hip_atomic_fetch_add(ctl, 1u, __ATOMIC_RELAXED, __HIP_MEMORY_SCOPE_WORKGROUP);
    if (prev == 3u) __hip_atomic_fetch_add(cnt + (qb * 4 + (head >> 3)) * 64, 1u, RLX_AGENT);
  }
  gu32* mine = cnt + ((tg >> 1) * 4 + wid) * 64;
  for (unsigned spins = 0; spins < SPIN_CAP; ++spins) {
    if (rfl(__hip_atomic_load(mine, RLX_AGENT)) >= 8u) break;
    __builtin_amdgcn_s_sleep(2);
  }
  if (lane == 0) __hip_atomic_store(ctl + 4 + wid, 1u, __ATOMIC_RELAXED, __HIP_MEMORY_SCOPE_WORKGROUP);
  STAMP(4); STAMP_OUT(ws, 1);
}

template <bool SC1> DI void ldx(bf16x8& d, srd_t srd, unsigned voff, unsigned soff, int imm) {
  if (SC1) asm volatile("buffer_load_dwordx4 %0, %1, %2, %3 offen offset:%4 sc1" : "=v"(d) : "v"(voff), "s"(srd), "s"(soff), "i"(imm) : "memory");
  else asm volatile("buffer_load_dwordx4 %0, %1, %2, %3 offen offset:%4" : "=v"(d) : "v"(voff), "s"(srd), "s"(soff), "i"(imm) : "memory");
}
DI void g2_role(const u16* wo, const u16* wd, char* ws, LAS char* lds, int tg, int cg, int kq, int lane) {
  const srd_t sx2 = mksrd(ws + WS_X2 + (size_t)tg * NKB * 8192, NKB * 8192);
  const srd_t swd = mksrd(wd, F * D * 2), swo = mksrd(wo, D * D * 2);
  const unsigned ldsw = (unsigned)(uintptr_t)lds + P2_G2RING + kq * 16384u;
  const unsigned l3 = lane >> 3, l7 = lane & 7;
  const unsigned vw = l3 * 2048u + ((l7 ^ (((l3 >> 1) & 3) << 1)) << 4) + cg * 128u;
  const int t16 = lane & 15, g = lane >> 4, q4 = t16 >> 2, p4 = lane & 3;
  const unsigned vx = g * 2048u + t16 * 16u;
  unsigned wa[4];
#pragma unroll
  for (int c = 0; c < 4; ++c) { wa[c] = ldsw + (4 * g + q4) * 128 + ((c ^ ((2 * g + (q4 >> 1)) & 3)) << 5) + p4 * 8; PIN(wa[c]); }
  f32x4 acc[8][4];
#pragma unroll
  for (int m = 0; m < 8; ++m)
#pragma unroll
    for (int c = 0; c < 4; ++c) acc[m][c] = f32x4{};
  bf16x8 xf[2][8];
  s16x4 wl[2][4], wh[2][4];
  constexpr unsigned OOB = 0x7ff00000u;
#define G2_WDMA(srd, sl, so, p) dma16(srd, vw, (so) + (p) * 16384u, ldsw + (sl) * 4096u + (p) * 1024u)
#define G2_RW(z, sl, c) do { LAS const char* p_ = LP(wa[c]) + (sl) * 4096; wl[z][c] = ldstr(p_); wh[z][c] = ldstr(p_ + 2048); } while (0)
  STAMP_DECL(); STAMP(0);
  unsigned xo = (unsigned)(28 * kq) * 8192u, wo_ = (unsigned)(28 * kq) * 65536u;
#pragma unroll
  for (int i = 0; i < 2; ++i) {
#pragma unroll
    for (int p = 0; p < 4; ++p) G2_WDMA(swd, i, wo_, p);
    wo_ += 65536u; }
#pragma unroll
  for (int z = 0; z < 2; ++z) {
#pragma unroll
    for (int m = 0; m < 8; ++m) { ldx<false>(xf[z][m], sx2, vx, xo, m * 256); if (!(m & 1)) G2_WDMA(swd, z + 2, wo_, m >> 1); }
    xo += 8192u; wo_ += 65536u; }
  asm volatile("s_barrier\n\ts_waitcnt vmcnt(24)" ::: "memory");
#pragma unroll
  for (int c = 0; c < 4; ++c) G2_RW(0, 0, c);
  SBAR();
  int nb = G2_NB, left = 16;
#define G2_KB(sl, WS_, XSC1, BAR)                                                                             \
  do {                                                                                                       \
    constexpr int z = (sl) & 1;                                                                              \
    _Pragma("unroll") for (int m = 0; m < 8; ++m) {                                                          \
      asm volatile("s_waitcnt vmcnt(23)" : "+v"(xf[z][m]));                                                  \
      mfma16a(acc[m][0], cat8(wl[z][0], wh[z][0]), xf[z][m]); SBAR();                                         \
      if (m >= 2 && m < 6) G2_RW(z ^ 1, ((sl) + 1) & 3, m - 2);                                               \
      SBAR();                                                                                                \
      mfma16a(acc[m][1], cat8(wl[z][1], wh[z][1]), xf[z][m]);                                                 \
      mfma16a(acc[m][2], cat8(wl[z][2], wh[z][2]), xf[z][m]);                                                 \
      mfma16a(acc[m][3], cat8(wl[z][3], wh[z][3]), xf[z][m]); SBAR();                                         \
      ldx<XSC1>(xf[z][m], sx2, vx, xo, m * 256);                                                             \
      if (!(m & 1)) G2_WDMA(WS_, sl, wo_, m >> 1);                                                            \
      if (BAR) { if (--nb == 0) { nb = G2_NB; if (left > 0) { --left; __builtin_amdgcn_s_barrier(); } } }     \
      SBAR();                                                                                                \
    }                                                                                                        \
    xo += 8192u; wo_ += 65536u;                                                                              \
  } while (0)
  STAMP(1);
  for (int i = 0; i < 24; i += 4) { G2_KB(0, swd, false, true); G2_KB(1, swd, false, true); G2_KB(2, swd, false, true); G2_KB(3, swd, false, true); }
  wo_ = (unsigned)(8 * kq) * 65536u;
  G2_KB(0, swo, false, true); G2_KB(1, swo, false, true);
  while (left > 0) { --left; __builtin_amdgcn_s_barrier(); }
  STAMP(2);
  { volatile LAS unsigned* rdy = (volatile LAS unsigned*)(lds + P2_CTL) + 4 + kq;
    for (unsigned spins = 0; spins < SPIN_CAP; ++spins) { if (rfl(*rdy) != 0u) break; __builtin_amdgcn_s_sleep(1); } }
  STAMP(3);
  xo = (unsigned)(112 + 8 * kq) * 8192u;
  G2_KB(2, swo, true, false); G2_KB(3, swo, true, false);
  G2_KB(0, swo, true, false); G2_KB(1, swo, true, false); G2_KB(2, swo, true, false); G2_KB(3, swo, true, false);
  wo_ = OOB;
  G2_KB(0, swo, true, false); G2_KB(1, swo, true, false);
  xo = OOB;
  G2_KB(2, swo, true, false); G2_KB(3, swo, true, false);
  STAMP(4);
  asm volatile("s_waitcnt vmcnt(0) lgkmcnt(0)\n\ts_nop 7\n\ts_barrier" ::: "memory");
#pragma unroll
  for (int m = 0; m < 8; ++m) asm volatile("" ::"v"(xf[0][m]), "v"(xf[1][m]));
  { LAS char* dst = lds + kq * 32768 + t16 * 256;
#pragma unroll
    for (int m = 0; m < 8; ++m)
#pragma unroll
      for (int c = 0; c < 4; ++c) *(LAS f32x4*)(dst + m * 4096 + (((4 * c + g) ^ t16) << 4)) = acc[m][c]; }
  STAMP(5); STAMP_OUT(ws, 2);
}

__global__ __launch_bounds__(512, 2) void p2_kernel(const u16* x, const u16* wo, const u16* wd, char* ws, u16* out) {
  extern __shared__ __attribute__((aligned(16))) char smem[];
  LAS char* lds = (LAS char*)smem;
  const int b = blockIdx.x, xcd = b & 7, r = b >> 3, wid = rfl(threadIdx.x >> 6), lane = threadIdx.x & 63;
  const int tg = (xcd >> 1) * 4 + (r & 3), cg = (xcd & 1) * 8 + (r >> 2);
  if (threadIdx.x < 16) ((LAS unsigned*)(lds + P2_CTL))[threadIdx.x] = 0u;
  asm volatile("s_waitcnt lgkmcnt(0)" ::: "memory");
  if (wid < 4) {
    att_role(ws, lds, xcd * 4 + (r >> 3), r & 7, tg, wid, lane);
    asm volatile("s_waitcnt vmcnt(0) lgkmcnt(0)\n\ts_barrier" ::: "memory");
  } else {
    g2_role(wo, wd, ws, lds, tg, cg, wid - 4, lane);
  }
  int tz = threadIdx.x; asm volatile("" : "+v"(tz));
  const int ln = tz & 63;
  u32x4 xr[2];
  size_t off[2];
#pragma unroll
  for (int e = 0; e < 2; ++e) { off[e] = (size_t)(tg * 128 + wid * 16 + e * 8 + (ln >> 3)) * D + cg * 64 + (ln & 7) * 8; xr[e] = *(const u32x4*)(x + off[e]); }
  asm volatile("s_waitcnt lgkmcnt(0)\n\ts_barrier" ::: "memory");
#pragma unroll
  for (int e = 0; e < 2; ++e) {
    const int tk = e * 8 + (ln >> 3);
    LAS const char* src = lds + (wid * 16 + tk) * 256;
    f32x4 lo = f32x4{}, hi = f32x4{};
#pragma unroll
    for (int k = 0; k < 4; ++k) { lo += *(LAS const f32x4*)(src + k * 32768 + (((2 * (ln & 7)) ^ tk) << 4)); hi += *(LAS const f32x4*)(src + k * 32768 + (((2 * (ln & 7) + 1) ^ tk) << 4)); }
    u32x4 w;
    w[0] = cvtpk(lo[0] + __uint_as_float(xr[e][0] << 16), lo[1] + __uint_as_float(xr[e][0] & 0xffff0000u));
    w[1] = cvtpk(lo[2] + __uint_as_float(xr[e][1] << 16), lo[3] + __uint_as_float(xr[e][1] & 0xffff0000u));
    w[2] = cvtpk(hi[0] + __uint_as_float(xr[e][2] << 16), hi[1] + __uint_as_float(xr[e][2] & 0xffff0000u));
    w[3] = cvtpk(hi[2] + __uint_as_float(xr[e][3] << 16), hi[3] + __uint_as_float(xr[e][3] & 0xffff0000u));
    *(u32x4*)(out + off[e]) = w;
  }
}

extern "C" void kernel_launch(void* const* d_in, const int* in_sizes, int n_in, void* d_out, int out_size, void* d_ws, size_t ws_size, hipStream_t stream) {
  const u16* x = (const u16*)d_in[0]; const u16* wqkv = (const u16*)d_in[1]; const u16* wo = (const u16*)d_in[2];
  const u16* wg = (const u16*)d_in[4]; const u16* wu = (const u16*)d_in[5]; const u16* wd = (const u16*)d_in[6];
  char* ws = (char*)d_ws;
  (void)hipFuncSetAttribute((const void*)g1_kernel, hipFuncAttributeMaxDynamicSharedMemorySize, G1_LDS);
  (void)hipFuncSetAttribute((const void*)p2_kernel, hipFuncAttributeMaxDynamicSharedMemorySize, P2_LDS);
  ln_kernel<<<S / 8, 512, 0, stream>>>(x, (u16*)(ws + WS_XN), (unsigned*)(ws + WS_CNT));
  g1_kernel<<<256, 512, G1_LDS, stream>>>((const u16*)(ws + WS_XN), wqkv, wg, wu, ws);
  p2_kernel<<<256, 512, P2_LDS, stream>>>(x, wo, wd, ws, (u16*)d_out);
}
```

```cpp
#include <hip/hip_runtime.h>
#include <hip/hip_bf16.h>
#include <stdint.h>

typedef unsigned short u16;
typedef unsigned long long u64;
typedef short bf16x8 __attribute__((ext_vector_type(8)));
typedef short s16x4 __attribute__((ext_vector_type(4)));
typedef float f32x16 __attribute__((ext_vector_type(16)));
typedef float f32x4 __attribute__((ext_vector_type(4)));
typedef float f32x2 __attribute__((ext_vector_type(2)));
typedef unsigned u32x4 __attribute__((ext_vector_type(4)));
typedef __bf16 bf16x2_t __attribute__((ext_vector_type(2)));
typedef short v4i16_t __attribute__((ext_vector_type(4)));
typedef __amdgpu_buffer_rsrc_t srd_t;
typedef __attribute__((address_space(1))) u64 gu64;
#define LAS __attribute__((address_space(3)))
#define DI __device__ __forceinline__
#define SBAR() __builtin_amdgcn_sched_barrier(0)
#define PIN(x) asm volatile("" : "+v"(x))
#define NOP_ ((void)0)
#define LP(a) ((LAS const char*)(uintptr_t)(a))
#define MFMA32(a, b, c) __builtin_amdgcn_mfma_f32_32x32x16_bf16((a), (b), (c), 0, 0, 0)
#define MFMA16(a, b, c) __builtin_amdgcn_mfma_f32_16x16x32_bf16((a), (b), (c), 0, 0, 0)
#define EX2(v) __builtin_amdgcn_exp2f(v)
#define BF(x) __builtin_bit_cast(bf16x8, x)
#define RLX_AGENT __ATOMIC_RELAXED, __HIP_MEMORY_SCOPE_AGENT

constexpr int S = 2048, D = 1024, F = 3584;
constexpr int NKB = (F + D) / 32;
constexpr float LOG2E = 1.4426950408889634f;
constexpr float QSCALE = 0.17677669529663687f * LOG2E;

constexpr unsigned WS_FLG = 0;
constexpr unsigned FLG_F1 = 256, FLG_F2 = FLG_F1 + 2048, FLG_F3 = FLG_F2 + 2048, FLG_F4 = FLG_F3 + 2048;
constexpr unsigned WS_XN = 131072;
constexpr unsigned WS_Q = WS_XN + S * D * 2;
constexpr unsigned WS_KV = WS_Q + S * D * 2;
constexpr unsigned WS_X2 = WS_KV + S * D * 4;
constexpr unsigned WS_END = WS_X2 + S * (F + D) * 2;
constexpr int G1_XS = 32768, G1_WB = 65536, G1_WS = 40960;
constexpr int P2_G2RING = 65536;
constexpr int LDS_CTL = G1_WB + 2 * G1_WS, LDS_BYTES = LDS_CTL + 64;
constexpr unsigned SPIN_CAP = 1u << 20;

DI unsigned rfl(unsigned x) { return __builtin_amdgcn_readfirstlane(x); }
DI unsigned cvtpk(float lo, float hi) { f32x2 v = {lo, hi}; bf16x2_t b = __builtin_convertvector(v, bf16x2_t); return __builtin_bit_cast(unsigned, b); }
DI float rbf(float f) { return __uint_as_float(cvtpk(f, f) << 16); }
DI float blo(unsigned u) { return __uint_as_float(u << 16); }
DI float bhi(unsigned u) { return __uint_as_float(u & 0xffff0000u); }
DI srd_t mksrd(const void* p, unsigned bytes) { return __builtin_amdgcn_make_buffer_rsrc((void*)p, 0, bytes, 0x00020000); }
template <bool SC1> DI void dma16(srd_t srd, unsigned voff, unsigned soff, unsigned m0v) {
  if (SC1) asm volatile("s_mov_b32 m0, %0\n\ts_nop 0\n\tbuffer_load_dwordx4 %1, %2, %3 offen sc1 lds" ::"s"(m0v), "v"(voff), "s"(srd), "s"(soff) : "memory");
  else asm volatile("s_mov_b32 m0, %0\n\ts_nop 0\n\tbuffer_load_dwordx4 %1, %2, %3 offen lds" ::"s"(m0v), "v"(voff), "s"(srd), "s"(soff) : "memory");
}
template <bool SC1, class T> DI void ld16(T& d, srd_t srd, unsigned voff, unsigned soff, int imm) {
  if (SC1) asm volatile("buffer_load_dwordx4 %0, %1, %2, %3 offen offset:%4 sc1" : "=v"(d) : "v"(voff), "s"(srd), "s"(soff), "i"(imm) : "memory");
  else asm volatile("buffer_load_dwordx4 %0, %1, %2, %3 offen offset:%4" : "=v"(d) : "v"(voff), "s"(srd), "s"(soff), "i"(imm) : "memory");
}
template <class T> DI void ld16_inplace(T& d, srd_t srd, unsigned voff, unsigned soff, int imm) {
  asm volatile("buffer_load_dwordx4 %0, %1, %2, %3 offen offset:%4 sc1" : "+v"(d) : "v"(voff), "s"(srd), "s"(soff), "i"(imm) : "memory");
}
DI void st16_sc1(srd_t srd, unsigned voff, unsigned soff, u32x4 v) {
  asm volatile("buffer_store_dwordx4 %0, %1, %2, %3 offen sc1\n\ts_nop 1" ::"v"(v), "v"(voff), "s"(srd), "s"(soff) : "memory");
}
DI s16x4 ldstr(LAS const char* p) { return __builtin_bit_cast(s16x4, __builtin_amdgcn_ds_read_tr16_b64_v4i16((LAS v4i16_t*)p)); }
DI bf16x8 lds128(LAS const char* p) { return *(LAS const bf16x8*)p; }
DI bf16x8 cat8(s16x4 lo, s16x4 hi) { return __builtin_shufflevector(lo, hi, 0, 1, 2, 3, 4, 5, 6, 7); }
DI u32x4 pack8(const f32x16& v, int s, float sc) {
  u32x4 w;
  w[0] = cvtpk(v[8 * s + 0] * sc, v[8 * s + 1] * sc); w[1] = cvtpk(v[8 * s + 2] * sc, v[8 * s + 3] * sc);
  w[2] = cvtpk(v[8 * s + 4] * sc, v[8 * s + 5] * sc); w[3] = cvtpk(v[8 * s + 6] * sc, v[8 * s + 7] * sc);
  return w;
}
DI u32x4 pack44(const f32x4& a, const f32x4& b, float sc) {
  u32x4 w; w[0] = cvtpk(a[0] * sc, a[1] * sc); w[1] = cvtpk(a[2] * sc, a[3] * sc); w[2] = cvtpk(b[0] * sc, b[1] * sc); w[3] = cvtpk(b[2] * sc, b[3] * sc); return w;
}
DI void mfma16a(f32x4& c, const bf16x8& a, const bf16x8& b) { asm volatile("v_mfma_f32_16x16x32_bf16 %0, %1, %2, %0" : "+v"(c) : "v"(a), "v"(b)); }
template <int CTRL, int RM> DI float dppadd(float v) { return v + __builtin_bit_cast(float, __builtin_amdgcn_update_dpp(0, __builtin_bit_cast(int, v), CTRL, RM, 0xf, true)); }
DI float wave_sum(float v) {
  v = dppadd<0x111, 0xf>(v); v = dppadd<0x112, 0xf>(v); v = dppadd<0x114, 0xf>(v); v = dppadd<0x118, 0xf>(v);
  v = dppadd<0x142, 0xa>(v); v = dppadd<0x143, 0xc>(v);
  return __builtin_bit_cast(float, __builtin_amdgcn_readlane(__builtin_bit_cast(int, v), 63));
}
DI void publish(char* ws, unsigned idx, u64 tag, int lane) { if (lane == 0) __hip_atomic_store((gu64*)(ws + WS_FLG) + idx, tag, RLX_AGENT); }
DI void await(char* ws, unsigned idx, u64 tag) {
  gu64* p = (gu64*)(ws + WS_FLG) + idx;
  for (unsigned spins = 0; spins < SPIN_CAP; ++spins) {
    if (__all(__hip_atomic_load(p, RLX_AGENT) == tag)) break;
    __builtin_amdgcn_s_sleep(1);
  }
  asm volatile("" ::: "memory");
}

#define STAMP(i) do {} while (0)

DI unsigned g2_vw(int lane, int cg) { const unsigned l3 = lane >> 3, l7 = lane & 7; return l3 * 2048u + ((l7 ^ (((l3 >> 1) & 3) << 1)) << 4) + cg * 128u; }

template <bool SWAP0>
DI void g1_body(const u16* x, const u16* wqkv, const u16* wg, const u16* wu, const u16* wd, char* ws, LAS char* lds, int mt, int j, int cg, u64& tag, u32x4 (&hp)[2][4]) {
  const int tid = threadIdx.x, lane = tid & 63, wid = rfl(tid >> 6), wr = wid & 3, wc = wid >> 2;
  const bool isY = !SWAP0 && j >= 24;
  const int a0 = j < 16 ? 32 + j : (j < 24 ? j - 16 : 3 * j - 64);
  const srd_t sx = mksrd(ws + WS_XN, S * D * 2), sq = mksrd(wqkv, D * 3 * D * 2), sg = mksrd(wg, D * F * 2), su = mksrd(wu, D * F * 2);
  const srd_t sa = isY ? sq : sg, sb = isY ? sq : su;
  const unsigned ld1 = isY ? 6144u : 7168u;
  const unsigned c0 = a0 * 128u, c1 = isY ? c0 + 128u : j * 256u, c2 = isY ? c0 + 256u : j * 256u, c3 = isY ? (24 + j) * 128u : j * 256u + 128u;
  const unsigned l3 = lane >> 3, l7 = lane & 7, l4 = lane >> 4;
  const unsigned vx0 = l3 * 2048u + ((l7 ^ l4) << 4), vx1 = vx0 ^ 64u;
  const unsigned wsw = (l7 ^ (((l4 & 1) | ((wid & 1) << 1)) << 1)) << 4;
  const unsigned vw0 = l3 * 6144u + wsw, vw2 = l3 * 7168u + wsw;
  const unsigned vw1 = isY ? vw0 : vw2;
  const unsigned ldsb = (unsigned)(uintptr_t)lds;
  unsigned sox = (unsigned)(mt * 256 + wid * 32) * 2048u;
  unsigned so0 = wid * 8 * 6144u + c0, so1 = wid * 8 * ld1, so2 = wid * 8 * 7168u + c3;
  const unsigned m0x = ldsb + wid * 4096u, m0w = ldsb + G1_WB + wid * 1024u;
#define G1_DX(i, st) dma16<true>(sx, ((i) & 1) ? vx1 : vx0, sox + (i) * 16384u, m0x + (st) * G1_XS + (i) * 1024u)
#define G1_DW(i, st)                                                                             \
  do {                                                                                           \
    if ((i) == 0) dma16<false>(sq, vw0, so0, m0w + (st) * G1_WS);                                 \
    if ((i) == 1) dma16<false>(sa, vw1, so1 + c1, m0w + (st) * G1_WS + 8192u);                    \
    if ((i) == 2) dma16<false>(sb, vw1, so1 + c2, m0w + (st) * G1_WS + 16384u);                   \
    if ((i) == 3) dma16<false>(sg, vw2, so2, m0w + (st) * G1_WS + 24576u);                        \
    if ((i) == 4) dma16<false>(su, vw2, so2, m0w + (st) * G1_WS + 32768u);                        \
  } while (0)
#define G1_ADVX() do { sox += 128u; } while (0)
#define G1_ADVW() do { so0 += 64u * 6144u; so1 += 64u * ld1; so2 += 64u * 7168u; } while (0)
  STAMP(0);
  {
    const int row = mt * 256 + j * 8 + wid;
    const srd_t sxi = mksrd(x, S * D * 2);
    u32x4 a, b;
    ld16<false>(a, sxi, lane * 16u, row * 2048u, 0); ld16<false>(b, sxi, lane * 16u, row * 2048u, 1024);
    asm volatile("s_waitcnt vmcnt(0)" : "+v"(a), "+v"(b));
    float v[16];
#pragma unroll
    for (int i = 0; i < 4; ++i) { v[2 * i] = blo(a[i]); v[2 * i + 1] = bhi(a[i]); v[8 + 2 * i] = blo(b[i]); v[9 + 2 * i] = bhi(b[i]); }
    float s = 0.f;
#pragma unroll
    for (int i = 0; i < 16; ++i) s += v[i];
    const float mean = wave_sum(s) * (1.f / D);
    float q = 0.f;
#pragma unroll
    for (int i = 0; i < 16; ++i) { const float d = v[i] - mean; q += d * d; }
    const float mb = rbf(mean), vb = rbf(wave_sum(q) * (1.f / D));
    const float inv = 1.f / rbf(sqrtf(rbf(vb + 1e-5f)));
    u32x4 oa, ob;
#pragma unroll
    for (int i = 0; i < 4; ++i) { oa[i] = cvtpk(rbf(v[2 * i] - mb) * inv, rbf(v[2 * i + 1] - mb) * inv); ob[i] = cvtpk(rbf(v[8 + 2 * i] - mb) * inv, rbf(v[9 + 2 * i] - mb) * inv); }
    st16_sc1(sx, lane * 16u, row * 2048u, oa); st16_sc1(sx, lane * 16u + 1024u, row * 2048u, ob);
    asm volatile("s_waitcnt vmcnt(0)" : "+s"(tag)::"memory");
    tag += 1;
    publish(ws, FLG_F1 + row, tag, lane);
    STAMP(1);
    G1_DW(0, 0); G1_DW(1, 0); G1_DW(2, 0); G1_DW(3, 0); G1_DW(4, 0); G1_ADVW();
    await(ws, FLG_F1 + mt * 256 + wid * 32 + (lane & 31), tag);
  }
  STAMP(2);
  G1_DX(0, 0); G1_DX(1, 0); G1_DX(2, 0); G1_DX(3, 0); G1_ADVX();
  G1_DX(0, 1); G1_DX(1, 1); G1_DX(2, 1); G1_DX(3, 1); G1_ADVX();
  G1_DW(0, 1); G1_DW(1, 1); G1_DW(2, 1); G1_DW(3, 1); G1_DW(4, 1); G1_ADVW();
  const int t16 = lane & 15, g = lane >> 4, q4 = t16 >> 2, p4 = lane & 3;
  unsigned xa0 = ldsb + (wr * 64 + t16) * 128 + ((g ^ ((t16 >> 1) & 7)) << 4), xa1 = xa0 ^ 64u;
  unsigned wa00 = ldsb + G1_WB + (8 * g + q4) * 128 + (((2 * wc) ^ (((q4 >> 1) & 1) | ((g & 1) << 1))) << 5) + p4 * 8;
  unsigned wa01 = wa00 ^ 32u, wa10 = wa00 + G1_WS, wa11 = wa01 + G1_WS;
  asm volatile("" : "+v"(xa0), "+v"(xa1), "+v"(wa00), "+v"(wa01), "+v"(wa10), "+v"(wa11));
  f32x4 acc[4][10];
#pragma unroll
  for (int m = 0; m < 4; ++m)
#pragma unroll
    for (int n = 0; n < 10; ++n) { acc[m][n] = f32x4{}; PIN(acc[m][n]); }
  bf16x8 xf[2][4];
  s16x4 wl[10], wh[10];
#define G1_RX(st, s, m) xf[s][m] = lds128(LP((s) ? xa1 : xa0) + (st) * G1_XS + (m) * 2048)
#define G1_RW(st, s, n)                                                                                      \
  do {                                                                                                       \
    LAS const char* p_ = LP((st) ? (((n) & 1) ? wa11 : wa10) : (((n) & 1) ? wa01 : wa00)) + ((n) >> 1) * 8192 + (s) * 4096; \
    wl[n] = ldstr(p_); wh[n] = ldstr(p_ + 512);                                                              \
  } while (0)
#define G1_MM(s, n, m)                                                                                       \
  do {                                                                                                       \
    if (SWAP0 && (n) < 2) mfma16a(acc[m][n], xf[s][m], cat8(wl[n], wh[n]));                                   \
    else mfma16a(acc[m][n], cat8(wl[n], wh[n]), xf[s][m]);                                                    \
  } while (0)
#define G1_GRP(s, n, A, B, C) do { G1_MM(s, n, 0); SBAR(); A; SBAR(); G1_MM(s, n, 1); SBAR(); B; SBAR(); G1_MM(s, n, 2); SBAR(); C; SBAR(); G1_MM(s, n, 3); SBAR(); } while (0)
#define G1_WAITBAR() asm volatile("s_waitcnt vmcnt(0) lgkmcnt(0)\n\ts_barrier" ::: "memory")
#define G1_HEAD(st)                                                                                          \
  do {                                                                                                       \
    G1_GRP(0, 0, G1_RW(st, 0, 9), NOP_, NOP_);                                                               \
    G1_GRP(0, 1, G1_RW(st, 1, 0), G1_RX(st, 1, 0), NOP_);                                                    \
    G1_GRP(0, 2, G1_RW(st, 1, 1), G1_RX(st, 1, 1), NOP_);                                                    \
    G1_GRP(0, 3, G1_RW(st, 1, 2), G1_RX(st, 1, 2), NOP_);                                                    \
    G1_GRP(0, 4, G1_RW(st, 1, 3), G1_RX(st, 1, 3), NOP_);                                                    \
    G1_GRP(0, 5, G1_RW(st, 1, 4), NOP_, NOP_);                                                               \
    G1_GRP(0, 6, G1_RW(st, 1, 5), NOP_, NOP_);                                                               \
    G1_GRP(0, 7, G1_RW(st, 1, 6), NOP_, NOP_);                                                               \
    G1_GRP(0, 8, G1_RW(st, 1, 7), NOP_, NOP_);                                                               \
    G1_GRP(0, 9, G1_RW(st, 1, 8), NOP_, NOP_);                                                               \
    G1_GRP(1, 0, G1_RW(st, 1, 9), NOP_, NOP_);                                                               \
    G1_GRP(1, 1, NOP_, NOP_, NOP_);                                                                          \
    G1_GRP(1, 2, NOP_, NOP_, NOP_);                                                                          \
    G1_WAITBAR();                                                                                            \
  } while (0)
#define G1_TILE(st, DM)                                                                                      \
  do {                                                                                                       \
    G1_HEAD(st);                                                                                             \
    G1_GRP(1, 3, G1_RX(st ^ 1, 0, 0), G1_RW(st ^ 1, 0, 0), if (DM) G1_DX(0, st));                             \
    G1_GRP(1, 4, G1_RX(st ^ 1, 0, 1), G1_RW(st ^ 1, 0, 1), if (DM) G1_DW(0, st));                             \
    G1_GRP(1, 5, G1_RX(st ^ 1, 0, 2), G1_RW(st ^ 1, 0, 2), if (DM) G1_DX(1, st));                             \
    G1_GRP(1, 6, G1_RX(st ^ 1, 0, 3), G1_RW(st ^ 1, 0, 3), if (DM) G1_DW(1, st));                             \
    G1_GRP(1, 7, G1_RW(st ^ 1, 0, 4), G1_RW(st ^ 1, 0, 5), if (DM) { G1_DX(2, st); G1_DW(2, st); });          \
    G1_GRP(1, 8, G1_RW(st ^ 1, 0, 6), G1_RW(st ^ 1, 0, 7), if (DM) { G1_DX(3, st); G1_DW(3, st); });          \
    G1_GRP(1, 9, G1_RW(st ^ 1, 0, 8), NOP_, if (DM) { G1_DW(4, st); G1_ADVX(); G1_ADVW(); });                 \
  } while (0)
  asm volatile("s_waitcnt vmcnt(9)\n\ts_barrier" ::: "memory");
  G1_RX(0, 0, 0); G1_RW(0, 0, 0); G1_RX(0, 0, 1); G1_RX(0, 0, 2); G1_RX(0, 0, 3);
  G1_RW(0, 0, 1); G1_RW(0, 0, 2); G1_RW(0, 0, 3); G1_RW(0, 0, 4); G1_RW(0, 0, 5); G1_RW(0, 0, 6); G1_RW(0, 0, 7); G1_RW(0, 0, 8);
  SBAR();
  STAMP(3);
  for (int t = 0; t < 14; t += 2) { G1_TILE(0, true); G1_TILE(1, true); }
  G1_TILE(0, false);
  G1_HEAD(1);
  {
    int tz = threadIdx.x; asm volatile("" : "+v"(tz));
    const int lane = tz & 63, t16 = lane & 15, g = lane >> 4, q4 = t16 >> 2, p4 = lane & 3;
    const srd_t sw = mksrd(ws, WS_END), swd = mksrd(wd, F * D * 2);
    const unsigned T32 = mt * 8 + wr * 2;
    const unsigned vq = g * 512u + t16 * 16u, vk = g * 512u + (16 * (q4 >> 1) + 4 * (q4 & 1) + p4) * 16u;
    auto store_qk = [&](int a, const f32x4& v0, const f32x4& v1, int m) {
      const unsigned head = 2 * (a & 15) + wc; const bool isq = a < 16;
      const unsigned so = isq ? WS_Q + (head * 64 + T32 + (m >> 1)) * 2048u + (m & 1) * 256u : WS_KV + (head * 64 + T32 + (m >> 1)) * 4096u + (m & 1) * 128u;
      st16_sc1(sw, isq ? vq : vk, so, pack44(v0, v1, isq ? QSCALE : 1.f));
    };
    auto store0 = [&](int i) {
      if (SWAP0) st16_sc1(sw, vk, WS_KV + ((2 * (a0 & 15) + wc) * 64 + T32 + (i >> 1)) * 4096u + 2048u + (i & 1) * 128u, pack44(acc[2 * (i >> 1)][i & 1], acc[2 * (i >> 1) + 1][i & 1], 1.f));
      else store_qk(a0, acc[i][0], acc[i][1], i);
    };
    const unsigned vw = g2_vw(lane, cg), so = (unsigned)(28 * wr) * 65536u, m0r = ldsb + P2_G2RING + wr * 16384u;
#define G1_SY(u, m) do { if (isY) { store_qk(a0 + (u), acc[m][2 * (u)], acc[m][2 * (u) + 1], m); store_qk(a0 + (u), acc[(m) + 1][2 * (u)], acc[(m) + 1][2 * (u) + 1], (m) + 1); } } while (0)
    G1_GRP(1, 3, NOP_, NOP_, NOP_); G1_GRP(1, 4, store0(0), NOP_, store0(1)); G1_GRP(1, 5, store0(2), NOP_, store0(3)); G1_GRP(1, 6, G1_SY(1, 0), NOP_, NOP_);
    G1_GRP(1, 7, G1_SY(1, 2), NOP_, NOP_); G1_GRP(1, 8, G1_SY(2, 0), NOP_, NOP_); G1_GRP(1, 9, G1_SY(2, 2), NOP_, NOP_);
    STAMP(4);
    if (wc) {
#pragma unroll
      for (int i = 0; i < 16; ++i) dma16<false>(swd, vw, so + i * 16384u, m0r + i * 1024u);
    }
    asm volatile("s_nop 7" ::: "memory"); SBAR();
#pragma unroll
    for (int pr = 1; pr >= 0; --pr) {
      if (pr == 0 && isY) break;
#pragma unroll
      for (int m = 0; m < 4; ++m) {
        f32x4 hv[2];
#pragma unroll
        for (int c = 0; c < 2; ++c)
#pragma unroll
          for (int r = 0; r < 4; ++r) hv[c][r] = acc[m][4 + 4 * pr + c][r] * __builtin_amdgcn_rcpf(1.f + EX2(acc[m][2 + 4 * pr + c][r] * -LOG2E));
        hp[pr][m] = pack44(hv[0], hv[1], 1.f);
        PIN(hp[pr][m]);
      }
      if (pr == 1) { if (wc) asm volatile("s_waitcnt vmcnt(16)" ::: "memory"); else asm volatile("s_waitcnt vmcnt(0)" ::: "memory"); publish(ws, FLG_F2 + (mt * 32 + j) * 8 + wid, tag, lane); }
    }
  }
  STAMP(5);
}

DI void att_slow(const char* kv, const bf16x8 (&qf)[2][2], f32x16 (&o)[2], float (&linv)[2], int lane) {
  __builtin_amdgcn_fence(__ATOMIC_ACQUIRE, "agent");
#pragma unroll
  for (int m = 0; m < 2; ++m) {
    f32x16 oo = f32x16{}; float mref = -INFINITY, l = 0.f;
    for (int kt = 0; kt < 64; ++kt) {
      const char* p = kv + kt * 4096 + lane * 16;
      const bf16x8 k0 = *(const bf16x8*)p, k1 = *(const bf16x8*)(p + 1024), v0 = *(const bf16x8*)(p + 2048), v1 = *(const bf16x8*)(p + 3072);
      f32x16 s = MFMA32(k0, qf[m][0], f32x16{});
      s = MFMA32(k1, qf[m][1], s);
      float mx = s[0];
#pragma unroll
      for (int r = 1; r < 16; ++r) mx = fmaxf(mx, s[r]);
      mx = fmaxf(fmaxf(mx, __shfl_xor(mx, 32)), mref);
      const float f = EX2(mref - mx);
      mref = mx;
      float sum = 0.f;
#pragma unroll
      for (int r = 0; r < 16; ++r) { s[r] = EX2(s[r] - mref); sum += s[r]; oo[r] *= f; }
      l = l * f + sum;
      oo = MFMA32(v0, BF(pack8(s, 0, 1.f)), oo);
      oo = MFMA32(v1, BF(pack8(s, 1, 1.f)), oo);
    }
    o[m] = oo; linv[m] = 1.f / (l + __shfl_xor(l, 32));
  }
}
DI void lds_meet(unsigned v, unsigned need, unsigned addr) {
  unsigned left = SPIN_CAP;
  asm volatile("1:\n\tv_cmp_le_u32 vcc, %2, %0\n\ts_cmp_eq_u64 vcc, exec\n\ts_cbranch_scc1 2f\n\ts_sleep 1\n\tds_read_b32 %0, %3\n\ts_sub_u32 %1, %1, 1\n\ts_waitcnt lgkmcnt(0)\n\ts_cbranch_scc0 1b\n2:"
               : "+v"(v), "+s"(left) : "s"(need), "v"(addr) : "vcc", "scc", "memory");
}
DI int unit_owner(int a) { return a >= 32 ? a - 32 : (a < 8 ? 16 + a : 24 + (a - 8) / 3); }
DI void store_h(char* ws, const u32x4 (&hp)[2][4], int mt, int j, int wr, int wc, int lane) {
  const srd_t sw = mksrd(ws, WS_END);
  const unsigned vh = (lane >> 4) * 2048u + (lane & 15) * 16u, base = WS_X2 + ((mt * 2 + (wr >> 1)) * NKB + wc) * 8192u + (wr & 1) * 1024u;
#pragma unroll
  for (int m = 0; m < 4; ++m) st16_sc1(sw, vh, base + (j < 24 ? 4 * j + 2 : 2 * j + 48) * 8192u + m * 256u, hp[1][m]);
  if (j < 24) {
#pragma unroll
    for (int m = 0; m < 4; ++m) st16_sc1(sw, vh, base + 4 * j * 8192u + m * 256u, hp[0][m]);
  }
}

DI void att_role(const u16* wo, char* ws, LAS char* lds, int head, int qb, int tg, int cg, int wid, u64 tag, const u32x4 (&hp)[2][4], int mt, int j) {
  int tz = threadIdx.x; asm volatile("" : "+v"(tz));
  const int lane = tz & 63;
  const char* kvg = ws + WS_KV + (size_t)head * 262144;
  const srd_t skv = mksrd(kvg, 262144), sw = mksrd(ws, WS_END);
  const unsigned ldsb = (unsigned)(uintptr_t)lds;
  const unsigned voff = lane * 16u;
  volatile LAS unsigned* ctl = (volatile LAS unsigned*)(lds + LDS_CTL);
  const unsigned ctla = ldsb + LDS_CTL + (lane & 7) * 4u;
#pragma unroll
  for (int i = 0; i < 8; ++i) *(LAS u32x4*)(lds + 32768 + wid * 8192 + i * 1024 + lane * 16) = hp[i >> 2][i & 3];
  asm volatile("s_waitcnt lgkmcnt(0)" ::: "memory");
  ctl[12 + wid] = 1u;
  {
    const int jq = unit_owner(head >> 1), jk = unit_owner(16 + (head >> 1)), jv = unit_owner(32 + (head >> 1));
    const int l = lane & 31, pw = (head & 1) * 4 + (wid >> 1) + 2 * (l & 1);
    const unsigned idx = lane < 32 ? (((l >> 1) & 7) * 32 + (l < 16 ? jk : jv)) * 8 + pw : (qb * 32 + jq) * 8 + (head & 1) * 4 + wid;
    await(ws, FLG_F2 + idx, tag);
  }
  STAMP(6);
#define AT_DMA1(c, i) dma16<true>(skv, voff, (unsigned)(c) * 16384u + wid * 4096u + (i) * 1024u, ldsb + ((c) & 3) * 16384u + wid * 4096u + (i) * 1024u)
#define AT_ARRIVE(k) ctl[wid] = (unsigned)((k) + 2)
#define AT_MEET(v, k) lds_meet(v, (unsigned)((k) + 2), ctla)
  AT_DMA1(0, 0); AT_DMA1(0, 1); AT_DMA1(0, 2); AT_DMA1(0, 3);
  bf16x8 qf[2][2];
  { const unsigned qo = WS_Q + (head * 64 + qb * 8 + wid * 2) * 2048u;
    ld16<true>(qf[0][0], sw, voff, qo, 0); ld16<true>(qf[0][1], sw, voff, qo, 1024); ld16<true>(qf[1][0], sw, voff, qo, 2048); ld16<true>(qf[1][1], sw, voff, qo, 3072); }
  AT_DMA1(1, 0); AT_DMA1(1, 1); AT_DMA1(1, 2); AT_DMA1(1, 3);
  f32x16 o[2] = {f32x16{}, f32x16{}};
  f32x4 ls[2] = {f32x4{}, f32x4{}};
  bf16x8 sel;
  { const short one = ((lane & 1) == ((lane >> 4) & 1) && (lane & 2) == 0) ? (short)0x3f80 : (short)0;
#pragma unroll
    for (int e = 0; e < 8; ++e) sel[e] = one; }
  PIN(sel);
  asm volatile("s_waitcnt vmcnt(0)" : "+v"(qf[0][0]), "+v"(qf[0][1]), "+v"(qf[1][0]), "+v"(qf[1][1])::"memory");
  AT_ARRIVE(-1);
  { unsigned pv = 0; AT_MEET(pv, -1); }
  AT_DMA1(2, 0); AT_DMA1(2, 1); AT_DMA1(2, 2); AT_DMA1(2, 3);
  bf16x8 kf[2][2], vf[2][2];
  f32x16 sa, sb, negm[2];
  u32x4 pa0, pa1, pb0, pb1;
  const unsigned kp = ldsb + lane * 16u;
  kf[0][0] = lds128(LP(kp)); kf[0][1] = lds128(LP(kp) + 1024);
  {
    sa = MFMA32(kf[0][0], qf[0][0], f32x16{}); sa = MFMA32(kf[0][1], qf[0][1], sa);
    sb = MFMA32(kf[0][0], qf[1][0], f32x16{}); sb = MFMA32(kf[0][1], qf[1][1], sb);
    float ma = sa[0], mb = sb[0];
#pragma unroll
    for (int r = 1; r < 16; ++r) { ma = fmaxf(ma, sa[r]); mb = fmaxf(mb, sb[r]); }
    ma = fmaxf(ma, __shfl_xor(ma, 32)); mb = fmaxf(mb, __shfl_xor(mb, 32));
#pragma unroll
    for (int r = 0; r < 16; ++r) { negm[0][r] = -ma; negm[1][r] = -mb; sa[r] -= ma; }
  }
  PIN(negm[0]); PIN(negm[1]);
  pb0 = u32x4{}; pb1 = u32x4{}; vf[1][0] = bf16x8{}; vf[1][1] = bf16x8{};
  PIN(pb0); PIN(pb1); PIN(vf[1][0]); PIN(vf[1][1]);
#define E1(S_, i) S_[i] = EX2(S_[i])
#define CV(P_, i, S_, r) P_[i] = cvtpk(S_[r], S_[(r) + 1])
#define AT_UNIT(SC, PC0, PC1, SN, kz, mn, PP0, PP1, vz, X0, X1, X2, X3)                                       \
  do {                                                                                                       \
    SN = MFMA32(kf[kz][0], qf[mn][0], negm[mn]); SBAR(); X0;                                                  \
    E1(SC, 0); E1(SC, 1); E1(SC, 2); E1(SC, 3); PIN(SC); SBAR();                                               \
    SN = MFMA32(kf[kz][1], qf[mn][1], SN); SBAR(); X1;                                                        \
    CV(PC0, 0, SC, 0); CV(PC0, 1, SC, 2); E1(SC, 4); E1(SC, 5); E1(SC, 6); PIN(SC); PIN(PC0); SBAR();           \
    o[mn] = MFMA32(vf[vz][0], BF(PP0), o[mn]); SBAR(); X2;                                                    \
    E1(SC, 7); CV(PC0, 2, SC, 4); E1(SC, 8); CV(PC0, 3, SC, 6); E1(SC, 9); PIN(SC); PIN(PC0); SBAR();           \
    ls[mn] = MFMA16(sel, BF(PP0), ls[mn]); SBAR();                                                            \
    E1(SC, 10); E1(SC, 11); PIN(SC); SBAR();                                                                  \
    o[mn] = MFMA32(vf[vz][1], BF(PP1), o[mn]); SBAR(); X3;                                                    \
    CV(PC1, 0, SC, 8); E1(SC, 12); CV(PC1, 1, SC, 10); E1(SC, 13); E1(SC, 14); E1(SC, 15); PIN(SC); PIN(PC1); SBAR(); \
    ls[mn] = MFMA16(sel, BF(PP1), ls[mn]); SBAR();                                                            \
    CV(PC1, 2, SC, 12); CV(PC1, 3, SC, 14); PIN(PC1); SBAR();                                                  \
  } while (0)
#define AT_KT(z, KADDR, VADDR, D0, D1)                                                                        \
  do {                                                                                                       \
    AT_UNIT(sa, pa0, pa1, sb, z, 1, pb0, pb1, z ^ 1, kf[z ^ 1][0] = lds128(KADDR), kf[z ^ 1][1] = lds128((KADDR) + 1024), vf[z][0] = lds128(VADDR), vf[z][1] = lds128((VADDR) + 1024)); \
    AT_UNIT(sb, pb0, pb1, sa, z ^ 1, 0, pa0, pa1, z, D0, NOP_, D1, NOP_);                                      \
  } while (0)
  STAMP(7);
  for (int c = 0; c < 16; ++c) {
    const unsigned cp = kp + (c & 3) * 16384u, cn = kp + ((c + 1) & 3) * 16384u;
    const int cd = c + 3 + (c + 3 < 16 ? 0 : 64);
    unsigned pv;
    AT_KT(0, LP(cp) + 4096, LP(cp) + 2048, NOP_, NOP_);
    AT_KT(1, LP(cp) + 8192, LP(cp) + 4096 + 2048, NOP_, pv = ctl[lane & 7]);
    AT_MEET(pv, c - 1);
    SBAR();
    AT_KT(0, LP(cp) + 12288, LP(cp) + 8192 + 2048, AT_DMA1(cd, 0), AT_DMA1(cd, 1));
    AT_KT(1, LP(cn), LP(cp) + 12288 + 2048, AT_DMA1(cd, 2), AT_DMA1(cd, 3));
    asm volatile("s_waitcnt vmcnt(4) lgkmcnt(0)" ::: "memory");
    AT_ARRIVE(c);
  }
  STAMP(8);
  o[1] = MFMA32(vf[1][0], BF(pb0), o[1]); ls[1] = MFMA16(sel, BF(pb0), ls[1]);
  o[1] = MFMA32(vf[1][1], BF(pb1), o[1]); ls[1] = MFMA16(sel, BF(pb1), ls[1]);
  float linv[2];
  bool bad = false;
#pragma unroll
  for (int m = 0; m < 2; ++m) {
    const float lt = (lane & 16) ? ls[m][1] : ls[m][0];
    float chk = lt, nanp = lt;
#pragma unroll
    for (int r = 0; r < 16; ++r) { chk = fmaxf(chk, fabsf(o[m][r])); nanp += o[m][r]; }
    bad = bad || !(chk < 1e30f) || (nanp != nanp);
    linv[m] = 1.f / lt;
  }
  if (__builtin_expect(__any(bad), 0)) att_slow(kvg, qf, o, linv, lane);
#pragma unroll
  for (int m = 0; m < 2; ++m) {
    const unsigned T = qb * 8 + wid * 2 + m;
    const unsigned so = WS_X2 + ((T >> 2) * NKB + 112 + head) * 8192u + (T & 3) * 512u, vo = (lane >> 5) * 2048u + (lane & 31) * 16u;
    st16_sc1(sw, vo, so, pack8(o[m], 0, linv[m]));
    st16_sc1(sw, vo, so + 4096u, pack8(o[m], 1, linv[m]));
  }
  asm volatile("s_waitcnt vmcnt(0)" ::: "memory");
  publish(ws, FLG_F4 + (qb * 32 + head) * 4 + wid, tag, lane);
  STAMP(9);
  AT_ARRIVE(16);
  { unsigned pv = 0; AT_MEET(pv, 16); }
  { const srd_t swo = mksrd(wo, D * D * 2); const unsigned vw = g2_vw(lane, cg), so = (unsigned)(8 * wid + 4) * 65536u;
#pragma unroll
    for (int i = 0; i < 16; ++i) dma16<false>(swo, vw, so + i * 16384u, ldsb + wid * 16384u + i * 1024u); }
  await(ws, FLG_F4 + ((tg >> 1) * 32 + 8 * wid + ((lane >> 1) & 7)) * 4 + 2 * (tg & 1) + (lane & 1), tag);
  asm volatile("s_waitcnt vmcnt(0)" ::: "memory");
  ctl[8 + wid] = 1u;
  STAMP(10);
  if (blockIdx.x == 0 && wid == 0) {
    gu64* p = (gu64*)(ws + WS_FLG) + FLG_F1 + lane;
    for (unsigned spins = 0; spins < SPIN_CAP; ++spins) {
      bool ok = true;
#pragma unroll
      for (int i = 0; i < 32; ++i) ok &= __hip_atomic_load(p + i * 64, RLX_AGENT) == tag;
      if (__all(ok)) break;
    }
    publish(ws, 0, tag, lane);
  }
}

constexpr int g2_cnt(int m, bool x0, bool w0, bool x1, bool w1, bool w2) {
  return (7 - m) + (w2 ? (8 - m) >> 1 : 0) + (x1 ? 8 : 0) + (w1 ? 4 : 0) + (x0 ? m : 0) + (w0 ? (m + 1) >> 1 : 0);
}
DI void g2_role(const u16* wo, const u16* wd, char* ws, LAS char* lds, int tg, int cg, int kq, u64 tag, const u32x4 (&hp)[2][4], int mt, int j) {
  int tz = threadIdx.x; asm volatile("" : "+v"(tz));
  const int lane = tz & 63;
  const srd_t sx2 = mksrd(ws + WS_X2 + (size_t)tg * NKB * 8192, NKB * 8192);
  const srd_t swd = mksrd(wd, F * D * 2), swo = mksrd(wo, D * D * 2);
  const unsigned ldsw = (unsigned)(uintptr_t)lds + P2_G2RING + kq * 16384u;
  const unsigned vw = g2_vw(lane, cg);
  const int t16 = lane & 15, g = lane >> 4, q4 = t16 >> 2, p4 = lane & 3;
  const unsigned vx = g * 2048u + t16 * 16u;
  volatile LAS unsigned* ctl = (volatile LAS unsigned*)(lds + LDS_CTL);
  {
    u32x4 hq[2][4];
    const unsigned cb = (unsigned)(uintptr_t)lds + LDS_CTL;
    lds_meet(0u, 1u, cb + (12 + kq) * 4u);
#pragma unroll
    for (int i = 0; i < 8; ++i) hq[i >> 2][i & 3] = *(LAS const u32x4*)(lds + 32768 + kq * 8192 + i * 1024 + lane * 16);
#pragma unroll
    for (int i = 0; i < 8; ++i) PIN(hq[i >> 2][i & 3]);
    ctl[4 + kq] = ~0u;
    lds_meet(0u, 1u, cb + (lane & 3) * 4u);
    store_h(ws, hp, mt, j, kq, 1, lane);
    store_h(ws, hq, mt, j, kq, 0, lane);
    asm volatile("s_waitcnt vmcnt(0)" ::: "memory");
    publish(ws, FLG_F3 + (mt * 32 + j) * 4 + kq, tag, lane);
  }
  { const int l = lane < (kq == 3 ? 22 : 14) ? lane : 0;
    await(ws, FLG_F3 + ((tg >> 1) * 32 + 7 * kq + (l >> 1)) * 4 + 2 * (tg & 1) + (l & 1), tag); }
  STAMP(6);
  bf16x8 xf[2][8];
  unsigned xo = (unsigned)(28 * kq) * 8192u, wo_ = (unsigned)(28 * kq + 4) * 65536u;
#pragma unroll
  for (int z = 0; z < 2; ++z) {
#pragma unroll
    for (int m = 0; m < 8; ++m) ld16<true>(xf[z][m], sx2, vx, xo, m * 256);
    xo += 8192u; }
  unsigned wa[4];
#pragma unroll
  for (int c = 0; c < 4; ++c) { wa[c] = ldsw + (4 * g + q4) * 128 + ((c ^ ((2 * g + (q4 >> 1)) & 3)) << 5) + p4 * 8; PIN(wa[c]); }
  f32x4 acc[8][4];
#pragma unroll
  for (int m = 0; m < 8; ++m)
#pragma unroll
    for (int c = 0; c < 4; ++c) { acc[m][c] = f32x4{}; PIN(acc[m][c]); }
  s16x4 wl[2][4], wh[2][4];
#define G2_RW(z, off, c) do { LAS const char* p_ = LP(wa[c]) + (off); wl[z][c] = ldstr(p_); wh[z][c] = ldstr(p_ + 2048); } while (0)
#pragma unroll
  for (int c = 0; c < 4; ++c) G2_RW(0, 0, c);
  SBAR();
#define G2_KB(sl, WS_, RD, X0, W0, X1, W1, W2)                                                                \
  do {                                                                                                       \
    constexpr int z = (sl) & 1;                                                                              \
    _Pragma("unroll") for (int m = 0; m < 8; ++m) {                                                          \
      switch (m) {                                                                                           \
        case 0: asm volatile("s_waitcnt vmcnt(%1)" : "+v"(xf[z][m]) : "i"(g2_cnt(0, X0, W0, X1, W1, W2))); break; \
        case 1: asm volatile("s_waitcnt vmcnt(%1)" : "+v"(xf[z][m]) : "i"(g2_cnt(1, X0, W0, X1, W1, W2))); break; \
        case 2: asm volatile("s_waitcnt vmcnt(%1)" : "+v"(xf[z][m]) : "i"(g2_cnt(2, X0, W0, X1, W1, W2))); break; \
        case 3: asm volatile("s_waitcnt vmcnt(%1)" : "+v"(xf[z][m]) : "i"(g2_cnt(3, X0, W0, X1, W1, W2))); break; \
        case 4: asm volatile("s_waitcnt vmcnt(%1)" : "+v"(xf[z][m]) : "i"(g2_cnt(4, X0, W0, X1, W1, W2))); break; \
        case 5: asm volatile("s_waitcnt vmcnt(%1)" : "+v"(xf[z][m]) : "i"(g2_cnt(5, X0, W0, X1, W1, W2))); break; \
        case 6: asm volatile("s_waitcnt vmcnt(%1)" : "+v"(xf[z][m]) : "i"(g2_cnt(6, X0, W0, X1, W1, W2))); break; \
        default: asm volatile("s_waitcnt vmcnt(%1)" : "+v"(xf[z][m]) : "i"(g2_cnt(7, X0, W0, X1, W1, W2))); break; \
      }                                                                                                      \
      mfma16a(acc[m][0], cat8(wl[z][0], wh[z][0]), xf[z][m]); SBAR();                                         \
      if (RD && m >= 2 && m < 6) G2_RW(z ^ 1, (((sl) + 1) & 3) * 4096, m - 2);                                \
      SBAR();                                                                                                \
      mfma16a(acc[m][1], cat8(wl[z][1], wh[z][1]), xf[z][m]);                                                 \
      mfma16a(acc[m][2], cat8(wl[z][2], wh[z][2]), xf[z][m]);                                                 \
      mfma16a(acc[m][3], cat8(wl[z][3], wh[z][3]), xf[z][m]); SBAR();                                         \
      if (X0) ld16_inplace(xf[z][m], sx2, vx, xo, m * 256);                                                  \
      if (W0 && !(m & 1)) dma16<false>(WS_, vw, wo_ + (m >> 1) * 16384u, ldsw + (sl) * 4096u + (m >> 1) * 1024u); \
      SBAR();                                                                                                \
    }                                                                                                        \
    xo += 8192u; wo_ += 65536u;                                                                              \
  } while (0)
#define YES true
#define NO false
  STAMP(7);
  G2_KB(0, swd, YES, YES, YES, YES, NO, NO); G2_KB(1, swd, YES, YES, YES, YES, YES, NO); G2_KB(2, swd, YES, YES, YES, YES, YES, YES); G2_KB(3, swd, YES, YES, YES, YES, YES, YES);
  for (int i = 4; i < 24; i += 4) { G2_KB(0, swd, YES, YES, YES, YES, YES, YES); G2_KB(1, swd, YES, YES, YES, YES, YES, YES); G2_KB(2, swd, YES, YES, YES, YES, YES, YES); G2_KB(3, swd, YES, YES, YES, YES, YES, YES); }
  wo_ = (unsigned)(8 * kq) * 65536u;
  G2_KB(0, swo, YES, YES, YES, YES, YES, YES); G2_KB(1, swo, YES, YES, YES, YES, YES, YES);
  STAMP(8);
  for (unsigned spins = 0; spins < SPIN_CAP; ++spins) { if (rfl(ctl[8 + kq]) != 0u) break; __builtin_amdgcn_s_sleep(1); }
  STAMP(9);
  xo = (unsigned)(112 + 8 * kq) * 8192u;
  G2_KB(2, swo, YES, YES, YES, YES, YES, YES); G2_KB(3, swo, YES, YES, YES, YES, YES, YES);
  G2_KB(0, swo, YES, YES, NO, YES, YES, YES); G2_KB(1, swo, YES, YES, NO, YES, NO, YES); G2_KB(2, swo, YES, YES, NO, YES, NO, NO);
#pragma unroll
  for (int c = 0; c < 4; ++c) wa[c] -= P2_G2RING;
  G2_KB(3, swo, YES, YES, NO, YES, NO, NO);
  G2_KB(0, swo, YES, YES, NO, YES, NO, NO); G2_KB(1, swo, YES, YES, NO, YES, NO, NO);
  G2_KB(2, swo, YES, NO, NO, YES, NO, NO); G2_KB(3, swo, NO, NO, NO, NO, NO, NO);
  STAMP(10);
  asm volatile("s_waitcnt vmcnt(0) lgkmcnt(0)\n\ts_nop 7\n\ts_barrier" ::: "memory");
  { int ty = threadIdx.x; asm volatile("" : "+v"(ty));
    const int t16 = ty & 15, g = (ty >> 4) & 3;
    LAS char* dst = lds + kq * 32768 + t16 * 256;
#pragma unroll
    for (int m = 0; m < 8; ++m)
#pragma unroll
      for (int c = 0; c < 4; ++c) *(LAS f32x4*)(dst + m * 4096 + (((4 * c + g) ^ t16) << 4)) = acc[m][c]; }
}

__global__ __launch_bounds__(512, 2) void fused_kernel(const u16* x, const u16* wqkv, const u16* wo, const u16* wg, const u16* wu, const u16* wd, char* ws, u16* out) {
  extern __shared__ __attribute__((aligned(16))) char smem[];
  LAS char* lds = (LAS char*)smem;
  const int b = blockIdx.x, xcd = b & 7, r = b >> 3, wid = rfl(threadIdx.x >> 6);
  u64 tag = *(const __attribute__((address_space(4))) u64*)(ws + WS_FLG);
  if (threadIdx.x < 16) ((LAS unsigned*)(lds + LDS_CTL))[threadIdx.x] = 0u;
  const int tg = (xcd >> 1) * 4 + (r & 3), cg = (xcd & 1) * 8 + (r >> 2);
  const int mt = r & 7, j = xcd + 8 * (r >> 3);
  u32x4 hp[2][4];
  if (j < 16) g1_body<true>(x, wqkv, wg, wu, wd, ws, lds, mt, j, cg, tag, hp);
  else g1_body<false>(x, wqkv, wg, wu, wd, ws, lds, mt, j, cg, tag, hp);
  if (wid >= 4) {
    g2_role(wo, wd, ws, lds, tg, cg, wid - 4, tag, hp, mt, j);
  } else {
#define ATT_PRIO 1
    __builtin_amdgcn_s_setprio(ATT_PRIO);
    att_role(wo, ws, lds, xcd * 4 + (r >> 3), r & 7, tg, cg, wid, tag, hp, mt, j);
    asm volatile("s_waitcnt vmcnt(0) lgkmcnt(0)\n\ts_barrier" ::: "memory");
  }
  int tz = threadIdx.x; asm volatile("" : "+v"(tz));
  const int ln = tz & 63;
  u32x4 xr[2];
  size_t off[2];
#pragma unroll
  for (int e = 0; e < 2; ++e) { off[e] = (size_t)(tg * 128 + wid * 16 + e * 8 + (ln >> 3)) * D + cg * 64 + (ln & 7) * 8; xr[e] = *(const u32x4*)(x + off[e]); }
  asm volatile("s_waitcnt lgkmcnt(0)\n\ts_barrier" ::: "memory");
  STAMP(11);
#pragma unroll
  for (int e = 0; e < 2; ++e) {
    const int tk = e * 8 + (ln >> 3);
    LAS const char* src = lds + (wid * 16 + tk) * 256;
    f32x4 lo = f32x4{}, hi = f32x4{};
#pragma unroll
    for (int k = 0; k < 4; ++k) { lo += *(LAS const f32x4*)(src + k * 32768 + (((2 * (ln & 7)) ^ tk) << 4)); hi += *(LAS const f32x4*)(src + k * 32768 + (((2 * (ln & 7) + 1) ^ tk) << 4)); }
    u32x4 w;
    w[0] = cvtpk(lo[0] + blo(xr[e][0]), lo[1] + bhi(xr[e][0])); w[1] = cvtpk(lo[2] + blo(xr[e][1]), lo[3] + bhi(xr[e][1]));
    w[2] = cvtpk(hi[0] + blo(xr[e][2]), hi[1] + bhi(xr[e][2])); w[3] = cvtpk(hi[2] + blo(xr[e][3]), hi[3] + bhi(xr[e][3]));
    *(u32x4*)(out + off[e]) = w;
  }
  STAMP(12);
}

extern "C" void kernel_launch(void* const* d_in, const int* in_sizes, int n_in, void* d_out, int out_size, void* d_ws, size_t ws_size, hipStream_t stream) {
  (void)hipFuncSetAttribute((const void*)fused_kernel, hipFuncAttributeMaxDynamicSharedMemorySize, LDS_BYTES);
  fused_kernel<<<256, 512, LDS_BYTES, stream>>>((const u16*)d_in[0], (const u16*)d_in[1], (const u16*)d_in[2], (const u16*)d_in[4], (const u16*)d_in[5], (const u16*)d_in[6], (char*)d_ws, (u16*)d_out);
}
```

```cpp
#include <hip/hip_runtime.h>
#include <hip/hip_bf16.h>
#include <stdint.h>

typedef unsigned short u16;
typedef unsigned long long u64;
typedef short bf16x8 __attribute__((ext_vector_type(8)));
typedef short s16x4 __attribute__((ext_vector_type(4)));
typedef float f32x16 __attribute__((ext_vector_type(16)));
typedef float f32x4 __attribute__((ext_vector_type(4)));
typedef float f32x2 __attribute__((ext_vector_type(2)));
typedef unsigned u32x4 __attribute__((ext_vector_type(4)));
typedef __bf16 bf16x2_t __attribute__((ext_vector_type(2)));
typedef short v4i16_t __attribute__((ext_vector_type(4)));
typedef __amdgpu_buffer_rsrc_t srd_t;
typedef __attribute__((address_space(1))) u64 gu64;
#define LAS __attribute__((address_space(3)))
#define DI __device__ __forceinline__
#define SBAR() __builtin_amdgcn_sched_barrier(0)
#define PIN(x) asm volatile("" : "+v"(x))
#define NOP_ ((void)0)
#define LP(a) ((LAS const char*)(uintptr_t)(a))
#define MFMA32(a, b, c) __builtin_amdgcn_mfma_f32_32x32x16_bf16((a), (b), (c), 0, 0, 0)
#define MFMA16(a, b, c) __builtin_amdgcn_mfma_f32_16x16x32_bf16((a), (b), (c), 0, 0, 0)
#define EX2(v) __builtin_amdgcn_exp2f(v)
#define BF(x) __builtin_bit_cast(bf16x8, x)
#define RLX_AGENT __ATOMIC_RELAXED, __HIP_MEMORY_SCOPE_AGENT

constexpr int S = 2048, D = 1024, F = 3584;
constexpr int NKB = (F + D) / 32;
constexpr float LOG2E = 1.4426950408889634f;
constexpr float QSCALE = 0.17677669529663687f * LOG2E;

constexpr unsigned WS_FLG = 0;
constexpr unsigned FLG_F1 = 256, FLG_F2 = FLG_F1 + 2048, FLG_F3 = FLG_F2 + 2048, FLG_F4 = FLG_F3 + 2048;
#define FST 16
constexpr unsigned WS_XN = 2097152;
constexpr unsigned WS_Q = WS_XN + S * D * 2;
constexpr unsigned WS_KV = WS_Q + S * D * 2;
constexpr unsigned WS_X2 = WS_KV + S * D * 4;
constexpr unsigned WS_END = WS_X2 + S * (F + D) * 2;
constexpr int G1_XS = 32768, G1_WB = 65536, G1_WS = 40960;
constexpr int P2_G2RING = 65536;
constexpr int LDS_CTL = G1_WB + 2 * G1_WS, LDS_DUMP = LDS_CTL + 1024, LDS_BYTES = LDS_DUMP + 8192;
#define CODEPF 3
#define PACEM 1
#define THR 0
#define DLY 0
#define SV 0
#define NAP1 1
#define NAP2 1
#define NAP3 1
#define NAP4 1
constexpr unsigned SPIN_CAP = 1u << 20;

DI unsigned rfl(unsigned x) { return __builtin_amdgcn_readfirstlane(x); }
DI int lane_id() { int l; asm volatile("v_mbcnt_lo_u32_b32 %0, -1, 0\n\tv_mbcnt_hi_u32_b32 %0, -1, %0" : "=v"(l)); return l; }
DI unsigned cvtpk(float lo, float hi) { f32x2 v = {lo, hi}; bf16x2_t b = __builtin_convertvector(v, bf16x2_t); return __builtin_bit_cast(unsigned, b); }
DI float rbf(float f) { return __uint_as_float(cvtpk(f, f) << 16); }
DI float blo(unsigned u) { return __uint_as_float(u << 16); }
DI float bhi(unsigned u) { return __uint_as_float(u & 0xffff0000u); }
DI srd_t mksrd(const void* p, unsigned bytes) { return __builtin_amdgcn_make_buffer_rsrc((void*)p, 0, bytes, 0x00020000); }
template <bool SC1> DI void dma16(srd_t srd, unsigned voff, unsigned soff, unsigned m0v) {
  if (SC1) asm volatile("s_mov_b32 m0, %0\n\ts_nop 0\n\tbuffer_load_dwordx4 %1, %2, %3 offen sc1 lds" ::"s"(m0v), "v"(voff), "s"(srd), "s"(soff) : "memory");
  else asm volatile("s_mov_b32 m0, %0\n\ts_nop 0\n\tbuffer_load_dwordx4 %1, %2, %3 offen lds" ::"s"(m0v), "v"(voff), "s"(srd), "s"(soff) : "memory");
}
template <bool SC1, class T> DI void ld16(T& d, srd_t srd, unsigned voff, unsigned soff, int imm) {
  if (SC1) asm volatile("buffer_load_dwordx4 %0, %1, %2, %3 offen offset:%4 sc1" : "=v"(d) : "v"(voff), "s"(srd), "s"(soff), "i"(imm) : "memory");
  else asm volatile("buffer_load_dwordx4 %0, %1, %2, %3 offen offset:%4" : "=v"(d) : "v"(voff), "s"(srd), "s"(soff), "i"(imm) : "memory");
}
template <class T> DI void ld16_inplace(T& d, srd_t srd, unsigned voff, unsigned soff, int imm) {
  asm volatile("buffer_load_dwordx4 %0, %1, %2, %3 offen offset:%4 sc1" : "+v"(d) : "v"(voff), "s"(srd), "s"(soff), "i"(imm) : "memory");
}
DI void st16_sc1(srd_t srd, unsigned voff, unsigned soff, u32x4 v) {
  asm volatile("buffer_store_dwordx4 %0, %1, %2, %3 offen sc1\n\ts_nop 1" ::"v"(v), "v"(voff), "s"(srd), "s"(soff) : "memory");
}
DI s16x4 ldstr(LAS const char* p) { return __builtin_bit_cast(s16x4, __builtin_amdgcn_ds_read_tr16_b64_v4i16((LAS v4i16_t*)p)); }
DI bf16x8 lds128(LAS const char* p) { return *(LAS const bf16x8*)p; }
DI bf16x8 cat8(s16x4 lo, s16x4 hi) { return __builtin_shufflevector(lo, hi, 0, 1, 2, 3, 4, 5, 6, 7); }
DI u32x4 pack8(const f32x16& v, int s, float sc) {
  u32x4 w;
  w[0] = cvtpk(v[8 * s + 0] * sc, v[8 * s + 1] * sc); w[1] = cvtpk(v[8 * s + 2] * sc, v[8 * s + 3] * sc);
  w[2] = cvtpk(v[8 * s + 4] * sc, v[8 * s + 5] * sc); w[3] = cvtpk(v[8 * s + 6] * sc, v[8 * s + 7] * sc);
  return w;
}
DI u32x4 pack44(const f32x4& a, const f32x4& b, float sc) {
  u32x4 w; w[0] = cvtpk(a[0] * sc, a[1] * sc); w[1] = cvtpk(a[2] * sc, a[3] * sc); w[2] = cvtpk(b[0] * sc, b[1] * sc); w[3] = cvtpk(b[2] * sc, b[3] * sc); return w;
}
DI void mfma16a(f32x4& c, const bf16x8& a, const bf16x8& b) { asm volatile("v_mfma_f32_16x16x32_bf16 %0, %1, %2, %0" : "+v"(c) : "v"(a), "v"(b)); }
template <int CTRL, int RM> DI float dppadd(float v) { return v + __builtin_bit_cast(float, __builtin_amdgcn_update_dpp(0, __builtin_bit_cast(int, v), CTRL, RM, 0xf, true)); }
DI float wave_sum(float v) {
  v = dppadd<0x111, 0xf>(v); v = dppadd<0x112, 0xf>(v); v = dppadd<0x114, 0xf>(v); v = dppadd<0x118, 0xf>(v);
  v = dppadd<0x142, 0xa>(v); v = dppadd<0x143, 0xc>(v);
  return __builtin_bit_cast(float, __builtin_amdgcn_readlane(__builtin_bit_cast(int, v), 63));
}
DI void publish(char* ws, unsigned idx, u64 tag, int lane) { if (lane == 0) __hip_atomic_store((gu64*)(ws + WS_FLG) + idx * FST, tag, RLX_AGENT); }
template <int NAP = 1> DI void await(char* ws, unsigned idx, u64 tag) {
  gu64* p = (gu64*)(ws + WS_FLG) + idx * FST;
  for (unsigned spins = 0; spins < SPIN_CAP; ++spins) {
    if (__all(__hip_atomic_load(p, RLX_AGENT) == tag)) break;
    __builtin_amdgcn_s_sleep(NAP);
  }
  asm volatile("" ::: "memory");
}

#define STW wid
#define STAMP(i) do {} while (0)

DI unsigned g2_vw(int lane, int cg) { const unsigned l3 = lane >> 3, l7 = lane & 7; return l3 * 2048u + ((l7 ^ (((l3 >> 1) & 3) << 1)) << 4) + cg * 128u; }

DI void ln_row(const u16* x, char* ws, int row) {
  const int lane = lane_id();
  const srd_t sxi = mksrd(x, S * D * 2), sx = mksrd(ws + WS_XN, S * D * 2);
  u32x4 a, b;
  ld16<false>(a, sxi, lane * 16u, row * 2048u, 0); ld16<false>(b, sxi, lane * 16u, row * 2048u, 1024);
  asm volatile("s_waitcnt vmcnt(0)" : "+v"(a), "+v"(b));
  float v[16];
#pragma unroll
  for (int i = 0; i < 4; ++i) { v[2 * i] = blo(a[i]); v[2 * i + 1] = bhi(a[i]); v[8 + 2 * i] = blo(b[i]); v[9 + 2 * i] = bhi(b[i]); }
  float s = 0.f;
#pragma unroll
  for (int i = 0; i < 16; ++i) s += v[i];
  const float mean = wave_sum(s) * (1.f / D);
  float q = 0.f;
#pragma unroll
  for (int i = 0; i < 16; ++i) { const float d = v[i] - mean; q += d * d; }
  const float mb = rbf(mean), vb = rbf(wave_sum(q) * (1.f / D));
  const float inv = 1.f / rbf(sqrtf(rbf(vb + 1e-5f)));
  u32x4 oa, ob;
#pragma unroll
  for (int i = 0; i < 4; ++i) { oa[i] = cvtpk(rbf(v[2 * i] - mb) * inv, rbf(v[2 * i + 1] - mb) * inv); ob[i] = cvtpk(rbf(v[8 + 2 * i] - mb) * inv, rbf(v[9 + 2 * i] - mb) * inv); }
  st16_sc1(sx, lane * 16u, row * 2048u, oa); st16_sc1(sx, lane * 16u + 1024u, row * 2048u, ob);
}

DI f32x2 glu2(float g0, float g1, float u0, float u1) { return f32x2{u0 * __builtin_amdgcn_rcpf(1.f + EX2(g0 * -LOG2E)), u1 * __builtin_amdgcn_rcpf(1.f + EX2(g1 * -LOG2E))}; }
DI unsigned h_base(int mt, int pair, int wr, int wc) { return WS_X2 + ((mt * 2 + (wr >> 1)) * NKB + 2 * pair + wc) * 8192u + (wr & 1) * 1024u; }
template <int KIND>
DI void g1_body(const u16* wqkv, const u16* wg, const u16* wu, char* ws, LAS char* lds, int wid, int mt, int j, srd_t scode, u64 tag, f32x4 (&accb)[4][4], u32x4 (&ha)[4]) {
  const int lane = lane_id(), wr = wid & 3, wc = wid >> 2;
  constexpr bool SWAP0 = KIND == 0, isY = KIND == 2;
  const int a0 = KIND == 0 ? 32 + j : (KIND == 1 ? j - 16 : 3 * j - 64);
  const srd_t sx = mksrd(ws + WS_XN, S * D * 2), sq = mksrd(wqkv, D * 3 * D * 2), sg = mksrd(wg, D * F * 2), su = mksrd(wu, D * F * 2);
  const srd_t sa = isY ? sq : sg, sb = isY ? sq : su;
  const unsigned ld1 = isY ? 6144u : 7168u;
  const unsigned c0 = a0 * 128u, c1 = isY ? c0 + 128u : j * 256u, c2 = isY ? c0 + 256u : j * 256u, c3 = isY ? (24 + j) * 128u : j * 256u + 128u;
  const unsigned l3 = lane >> 3, l7 = lane & 7, l4 = lane >> 4;
  const unsigned vx0 = l3 * 2048u + ((l7 ^ l4) << 4), vx1 = vx0 ^ 64u;
  const unsigned wsw = (l7 ^ (((l4 & 1) | ((wid & 1) << 1)) << 1)) << 4;
  const unsigned vw0 = l3 * 6144u + wsw, vw2 = l3 * 7168u + wsw;
  const unsigned vw1 = isY ? vw0 : vw2;
  const unsigned ldsb = (unsigned)(uintptr_t)lds;
  unsigned sox = (unsigned)(mt * 256 + wid * 32) * 2048u;
  unsigned so0 = wid * 8 * 6144u + c0, so1 = wid * 8 * ld1, so2 = wid * 8 * 7168u + c3;
  const unsigned m0x = ldsb + wid * 4096u, m0w = ldsb + G1_WB + wid * 1024u;
#define G1_DX(i, st) dma16<true>(sx, ((i) & 1) ? vx1 : vx0, sox + (i) * 16384u, m0x + (st) * G1_XS + (i) * 1024u)
#define G1_DW(i, st)                                                                             \
  do {                                                                                           \
    if ((i) == 0) dma16<false>(sq, vw0, so0, m0w + (st) * G1_WS);                                 \
    if ((i) == 1) dma16<false>(sa, vw1, so1 + c1, m0w + (st) * G1_WS + 8192u);                    \
    if ((i) == 2) dma16<false>(sb, vw1, so1 + c2, m0w + (st) * G1_WS + 16384u);                   \
    if ((i) == 3) dma16<false>(sg, vw2, so2, m0w + (st) * G1_WS + 24576u);                        \
    if ((i) == 4) dma16<false>(su, vw2, so2, m0w + (st) * G1_WS + 32768u);                        \
  } while (0)
#define G1_ADVX() do { sox += 128u; } while (0)
#define G1_ADVA() do { so0 += 64u * 6144u; so1 += 64u * ld1; } while (0)
#define G1_ADVW() do { G1_ADVA(); so2 += 64u * 7168u; } while (0)
#define G1_DB(st, sl) do { dma16<false>(sg, vw2, so2, m0w + (st) * G1_WS + (sl) * 8192u); dma16<false>(su, vw2, so2, m0w + (st) * G1_WS + ((sl) + 1) * 8192u); so2 += 64u * 7168u; } while (0)
#define G1_CODE() dma16<false>(scode, vx0, (unsigned)(mt * 16384 + ((j >> 3) & 1) * 1024 + wid * 128), ldsb + LDS_DUMP + wid * 1024u)
  f32x4 acc[4][10];
  G1_DW(0, 0); G1_DW(1, 0); G1_DW(2, 0); G1_DW(3, 0); G1_DW(4, 0); G1_ADVW();
#pragma unroll
  for (int m = 0; m < 4; ++m)
#pragma unroll
    for (int n = 0; n < 10; ++n) { acc[m][n] = f32x4{}; PIN(acc[m][n]); }
  await<NAP1>(ws, FLG_F1 + mt * 32 + wid * 4 + (lane & 3), tag);
  STAMP(2);
  G1_DX(0, 0); G1_DX(1, 0); G1_DX(2, 0); G1_DX(3, 0); G1_ADVX();
  G1_DX(0, 1); G1_DX(1, 1); G1_DX(2, 1); G1_DX(3, 1); G1_ADVX();
  G1_DW(0, 1); G1_DW(1, 1); G1_DW(2, 1); G1_DW(3, 1); G1_DW(4, 1); G1_ADVW();
  const int t16 = lane & 15, g = lane >> 4, q4 = t16 >> 2, p4 = lane & 3;
  unsigned xa0 = ldsb + (wr * 64 + t16) * 128 + ((g ^ ((t16 >> 1) & 7)) << 4), xa1 = xa0 ^ 64u;
  unsigned wa00 = ldsb + G1_WB + (8 * g + q4) * 128 + (((2 * wc) ^ (((q4 >> 1) & 1) | ((g & 1) << 1))) << 5) + p4 * 8;
  unsigned wa01 = wa00 ^ 32u, wa10 = wa00 + G1_WS, wa11 = wa01 + G1_WS;
  asm volatile("" : "+v"(xa0), "+v"(xa1), "+v"(wa00), "+v"(wa01), "+v"(wa10), "+v"(wa11));
  bf16x8 xf[2][4];
  s16x4 wl[10], wh[10];
#define G1_RX(st, s, m) xf[s][m] = lds128(LP((s) ? xa1 : xa0) + (st) * G1_XS + (m) * 2048)
#define G1_RW(st, s, n)                                                                                      \
  do {                                                                                                       \
    LAS const char* p_ = LP((st) ? (((n) & 1) ? wa11 : wa10) : (((n) & 1) ? wa01 : wa00)) + ((n) >> 1) * 8192 + (s) * 4096; \
    wl[n] = ldstr(p_); wh[n] = ldstr(p_ + 512);                                                              \
  } while (0)
#define G1_MM(s, n, m)                                                                                       \
  do {                                                                                                       \
    if (SWAP0 && (n) < 2) mfma16a(acc[m][n], xf[s][m], cat8(wl[n], wh[n]));                                   \
    else mfma16a(acc[m][n], cat8(wl[n], wh[n]), xf[s][m]);                                                    \
  } while (0)
#define G1_GRP(s, n, A, B, C) do { G1_MM(s, n, 0); SBAR(); A; SBAR(); G1_MM(s, n, 1); SBAR(); B; SBAR(); G1_MM(s, n, 2); SBAR(); C; SBAR(); G1_MM(s, n, 3); SBAR(); } while (0)
#define G1_WAITBAR() asm volatile("s_waitcnt vmcnt(0) lgkmcnt(0)\n\ts_barrier" ::: "memory")
#define G1_HEAD(st)                                                                                          \
  do {                                                                                                       \
    G1_GRP(0, 0, G1_RW(st, 0, 9), NOP_, NOP_);                                                               \
    G1_GRP(0, 1, G1_RW(st, 1, 0), G1_RX(st, 1, 0), NOP_);                                                    \
    G1_GRP(0, 2, G1_RW(st, 1, 1), G1_RX(st, 1, 1), NOP_);                                                    \
    G1_GRP(0, 3, G1_RW(st, 1, 2), G1_RX(st, 1, 2), NOP_);                                                    \
    G1_GRP(0, 4, G1_RW(st, 1, 3), G1_RX(st, 1, 3), NOP_);                                                    \
    G1_GRP(0, 5, G1_RW(st, 1, 4), NOP_, NOP_);                                                               \
    G1_GRP(0, 6, G1_RW(st, 1, 5), NOP_, NOP_);                                                               \
    G1_GRP(0, 7, G1_RW(st, 1, 6), NOP_, NOP_);                                                               \
    G1_GRP(0, 8, G1_RW(st, 1, 7), NOP_, NOP_);                                                               \
    G1_GRP(0, 9, G1_RW(st, 1, 8), NOP_, NOP_);                                                               \
    G1_GRP(1, 0, G1_RW(st, 1, 9), NOP_, NOP_);                                                               \
    G1_GRP(1, 1, NOP_, NOP_, NOP_);                                                                          \
    G1_GRP(1, 2, NOP_, NOP_, NOP_);                                                                          \
    G1_WAITBAR();                                                                                            \
  } while (0)
#define G1_TILE(st, DM)                                                                                      \
  do {                                                                                                       \
    G1_HEAD(st);                                                                                             \
    G1_GRP(1, 3, G1_RX(st ^ 1, 0, 0), G1_RW(st ^ 1, 0, 0), if (DM) G1_DX(0, st));                             \
    G1_GRP(1, 4, G1_RX(st ^ 1, 0, 1), G1_RW(st ^ 1, 0, 1), if (DM) G1_DW(0, st));                             \
    G1_GRP(1, 5, G1_RX(st ^ 1, 0, 2), G1_RW(st ^ 1, 0, 2), if (DM) G1_DX(1, st));                             \
    G1_GRP(1, 6, G1_RX(st ^ 1, 0, 3), G1_RW(st ^ 1, 0, 3), if (DM) G1_DW(1, st));                             \
    G1_GRP(1, 7, G1_RW(st ^ 1, 0, 4), G1_RW(st ^ 1, 0, 5), if (DM) { G1_DX(2, st); G1_DW(2, st); });          \
    G1_GRP(1, 8, G1_RW(st ^ 1, 0, 6), G1_RW(st ^ 1, 0, 7), if (DM) { G1_DX(3, st); G1_DW(3, st); });          \
    G1_GRP(1, 9, G1_RW(st ^ 1, 0, 8), NOP_, if (DM) { G1_DW(4, st); G1_ADVX(); G1_ADVW(); });                 \
  } while (0)
  asm volatile("s_waitcnt vmcnt(9)\n\ts_barrier" ::: "memory");
  G1_RX(0, 0, 0); G1_RW(0, 0, 0); G1_RX(0, 0, 1); G1_RX(0, 0, 2); G1_RX(0, 0, 3);
  G1_RW(0, 0, 1); G1_RW(0, 0, 2); G1_RW(0, 0, 3); G1_RW(0, 0, 4); G1_RW(0, 0, 5); G1_RW(0, 0, 6); G1_RW(0, 0, 7); G1_RW(0, 0, 8);
  SBAR();
  STAMP(3);
  for (int t = 0; t < 14; t += 2) {
    G1_CODE();
    G1_TILE(0, true); G1_TILE(1, true);
  }
  STAMP(4);
  {
    const srd_t sw = mksrd(ws, WS_END);
    unsigned vq, vk;
    auto store_addr = [&]() { const unsigned l = lane_id(); vq = (l >> 4) * 512u + (l & 15) * 16u; vk = (l >> 4) * 512u + (2 * (l & 8) + (l & 7)) * 16u; };
    const unsigned T32 = mt * 8 + wr * 2;
    auto store_u = [&](int u, int i) {
      const int a = a0 + u; const unsigned head = 2 * (a & 15) + wc; const bool isq = a < 16;
      if (SWAP0) st16_sc1(sw, vk, WS_KV + (head * 64 + T32 + (i >> 1)) * 4096u + 2048u + (i & 1) * 128u, pack44(acc[2 * (i >> 1)][i & 1], acc[2 * (i >> 1) + 1][i & 1], 1.f));
      else st16_sc1(sw, isq ? vq : vk, isq ? WS_Q + (head * 64 + T32 + (i >> 1)) * 2048u + (i & 1) * 256u : WS_KV + (head * 64 + T32 + (i >> 1)) * 4096u + (i & 1) * 128u,
                    pack44(acc[i][2 * u], acc[i][2 * u + 1], isq ? QSCALE : 1.f));
    };
    auto flag2 = [&]() {
      typedef unsigned u32x2 __attribute__((ext_vector_type(2)));
      const u32x2 tv = {(unsigned)tag, (unsigned)(tag >> 32)};
      asm volatile("s_waitcnt vmcnt(0)\n\tbuffer_store_dwordx2 %0, %1, %2, %3 offen sc1" ::"v"(tv), "v"(lane_id() == 0 ? 0u : 0x7ff00000u), "s"(sw), "s"(WS_FLG + (FLG_F2 + (mt * 32 + j) * 8 + wid) * (8u * FST)) : "memory");
    };
#define G1_HA(k) do { if (!isY) { const f32x2 h_ = glu2(acc[(k) >> 2][2 + (((k) >> 1) & 1)][2 * ((k) & 1)], acc[(k) >> 2][2 + (((k) >> 1) & 1)][2 * ((k) & 1) + 1], acc[(k) >> 2][4 + (((k) >> 1) & 1)][2 * ((k) & 1)], acc[(k) >> 2][4 + (((k) >> 1) & 1)][2 * ((k) & 1) + 1]); \
      unsigned w_ = cvtpk(h_[0], h_[1]); PIN(w_); ha[(k) >> 2][(k) & 3] = w_; } } while (0)
#define G1_SY(u, i) do { if (isY) store_u(u, i); } while (0)
#define G1_RD(d, st, s, n)                                                                                   \
  do {                                                                                                       \
    LAS const char* p_ = LP((st) ? (((n) & 1) ? wa11 : wa10) : (((n) & 1) ? wa01 : wa00)) + ((n) >> 1) * 8192 + (s) * 4096; \
    wl[d] = ldstr(p_); wh[d] = ldstr(p_ + 512);                                                              \
  } while (0)
#define G1_MD(d, s, n, m)                                                                                    \
  do {                                                                                                       \
    if (SWAP0 && (n) < 2) mfma16a(acc[m][n], xf[s][m], cat8(wl[d], wh[d]));                                   \
    else mfma16a(acc[m][n], cat8(wl[d], wh[d]), xf[s][m]);                                                    \
  } while (0)
#define G1_GD(d, s, n, A, B, C) do { G1_MD(d, s, n, 0); SBAR(); A; SBAR(); G1_MD(d, s, n, 1); SBAR(); B; SBAR(); G1_MD(d, s, n, 2); SBAR(); C; SBAR(); G1_MD(d, s, n, 3); SBAR(); } while (0)
    G1_GD(0, 0, 0, G1_RD(9, 0, 1, 0), G1_RX(0, 1, 0), NOP_);
    G1_GD(1, 0, 1, G1_RD(0, 0, 1, 1), G1_RX(0, 1, 1), NOP_);
    G1_GD(2, 0, 2, G1_RD(1, 0, 1, 2), G1_RX(0, 1, 2), NOP_);
    G1_GD(3, 0, 3, G1_RD(2, 0, 1, 3), G1_RX(0, 1, 3), NOP_);
    G1_GD(4, 0, 4, G1_RD(3, 0, 1, 4), NOP_, NOP_);
    G1_GD(5, 0, 5, G1_RD(4, 0, 1, 5), NOP_, NOP_);
    G1_GD(9, 1, 0, NOP_, NOP_, NOP_);
    asm volatile("s_waitcnt vmcnt(0)\n\ts_barrier" ::: "memory");
    G1_GD(0, 1, 1, G1_RX(1, 0, 0), G1_RD(5, 1, 0, 0), G1_RX(1, 0, 1));
    G1_GD(1, 1, 2, G1_RX(1, 0, 2), G1_RD(6, 1, 0, 1), G1_RX(1, 0, 3));
    G1_GD(2, 1, 3, G1_RD(7, 1, 1, 0), G1_RD(8, 1, 1, 1), NOP_);
    G1_GD(3, 1, 4, G1_RD(9, 1, 0, 2), G1_RD(0, 1, 0, 3), NOP_);
    G1_GD(4, 1, 5, G1_RD(1, 1, 1, 2), G1_RD(2, 1, 1, 3), NOP_);
    G1_GD(5, 0, 0, { G1_RX(1, 1, 0); G1_RX(1, 1, 1); }, { G1_RX(1, 1, 2); G1_RX(1, 1, 3); }, G1_RD(3, 1, 0, 4));
    G1_GD(6, 0, 1, G1_RD(4, 1, 0, 5), NOP_, NOP_);
    G1_GD(9, 0, 2, G1_RD(5, 1, 1, 4), NOP_, NOP_);
    G1_GD(7, 1, 0, G1_RD(6, 1, 1, 5), NOP_, NOP_);
    G1_GD(8, 1, 1, G1_RD(9, 1, 0, 6), NOP_, NOP_);
    G1_GD(0, 0, 3, G1_RD(7, 1, 0, 7), NOP_, NOP_);
    G1_GD(1, 1, 2, G1_RD(8, 1, 0, 8), NOP_, store_addr());
    G1_GD(2, 1, 3, G1_RD(0, 1, 0, 9), store_u(0, 0), store_u(0, 1));
    G1_GD(3, 0, 4, G1_RD(1, 1, 1, 6), store_u(0, 2), store_u(0, 3));
    G1_GD(4, 0, 5, G1_RD(2, 1, 1, 7), NOP_, NOP_);
    G1_GD(5, 1, 4, G1_RD(3, 1, 1, 8), G1_SY(1, 0), G1_SY(1, 1));
    G1_GD(6, 1, 5, G1_RD(4, 1, 1, 9), G1_SY(1, 2), G1_SY(1, 3));
    G1_GD(9, 0, 6, G1_RD(5, 0, 0, 6), NOP_, NOP_);
    G1_GD(7, 0, 7, G1_RD(6, 0, 0, 7), NOP_, G1_HA(0));
    G1_GD(8, 0, 8, G1_RD(9, 0, 0, 8), { G1_SY(2, 0); G1_HA(1); }, G1_SY(2, 1));
    G1_GD(0, 0, 9, G1_RD(7, 0, 0, 9), { G1_SY(2, 2); G1_HA(2); }, G1_SY(2, 3));
    G1_GD(1, 1, 6, { G1_RX(0, 0, 0); G1_RX(0, 0, 1); }, G1_RD(8, 0, 1, 6), G1_HA(3));
    G1_GD(2, 1, 7, { G1_RX(0, 0, 2); G1_RX(0, 0, 3); }, G1_RD(0, 0, 1, 7), G1_HA(4));
    G1_GD(3, 1, 8, G1_RD(1, 0, 1, 8), G1_HA(5), G1_HA(6));
    G1_GD(4, 1, 9, G1_RD(2, 0, 1, 9), G1_HA(7), NOP_);
    G1_GD(5, 0, 6, { G1_RX(0, 1, 0); G1_RX(0, 1, 1); }, { G1_RX(0, 1, 2); G1_RX(0, 1, 3); }, G1_HA(8));
    G1_GD(6, 0, 7, G1_HA(9), NOP_, NOP_);
    G1_GD(9, 0, 8, G1_HA(10), NOP_, NOP_);
    G1_GD(7, 0, 9, G1_HA(11), NOP_, NOP_);
    asm volatile("s_waitcnt lgkmcnt(0)\n\ts_barrier" ::: "memory");
    G1_GD(8, 1, 6, G1_HA(12), NOP_, NOP_);
    G1_GD(0, 1, 7, G1_HA(13), NOP_, NOP_);
    G1_GD(1, 1, 8, G1_HA(14), NOP_, NOP_);
    G1_GD(2, 1, 9, G1_HA(15), NOP_, NOP_);
    flag2();
  }
  asm volatile("s_nop 7" ::: "memory"); SBAR();
#pragma unroll
  for (int m = 0; m < 4; ++m)
#pragma unroll
    for (int n = 0; n < 4; ++n) accb[m][n] = acc[m][6 + n];
  STAMP(5);
}

DI void att_slow(const char* kv, const bf16x8 (&qf)[2][2], f32x16 (&o)[2], float (&linv)[2], int lane) {
  __builtin_amdgcn_fence(__ATOMIC_ACQUIRE, "agent");
#pragma unroll
  for (int m = 0; m < 2; ++m) {
    f32x16 oo = f32x16{}; float mref = -INFINITY, l = 0.f;
    for (int kt = 0; kt < 64; ++kt) {
      const char* p = kv + kt * 4096 + lane * 16;
      const bf16x8 k0 = *(const bf16x8*)p, k1 = *(const bf16x8*)(p + 1024), v0 = *(const bf16x8*)(p + 2048), v1 = *(const bf16x8*)(p + 3072);
      f32x16 s = MFMA32(k0, qf[m][0], f32x16{});
      s = MFMA32(k1, qf[m][1], s);
      float mx = s[0];
#pragma unroll
      for (int r = 1; r < 16; ++r) mx = fmaxf(mx, s[r]);
      mx = fmaxf(fmaxf(mx, __shfl_xor(mx, 32)), mref);
      const float f = EX2(mref - mx);
      mref = mx;
      float sum = 0.f;
#pragma unroll
      for (int r = 0; r < 16; ++r) { s[r] = EX2(s[r] - mref); sum += s[r]; oo[r] *= f; }
      l = l * f + sum;
      oo = MFMA32(v0, BF(pack8(s, 0, 1.f)), oo);
      oo = MFMA32(v1, BF(pack8(s, 1, 1.f)), oo);
    }
    o[m] = oo; linv[m] = 1.f / (l + __shfl_xor(l, 32));
  }
}
DI void lds_meet(unsigned v, unsigned need, unsigned addr) {
  unsigned left = SPIN_CAP;
  asm volatile("1:\n\tv_cmp_le_u32 vcc, %2, %0\n\ts_cmp_eq_u64 vcc, exec\n\ts_cbranch_scc1 2f\n\ts_sleep 1\n\tds_read_b32 %0, %3\n\ts_sub_u32 %1, %1, 1\n\ts_waitcnt lgkmcnt(0)\n\ts_cbranch_scc0 1b\n2:"
               : "+v"(v), "+s"(left) : "s"(need), "v"(addr) : "vcc", "scc", "memory");
}
DI int unit_owner(int a) { return a >= 32 ? a - 32 : (a < 8 ? 16 + a : 24 + (a - 8) / 3); }
DI void glu_pack(const f32x4 (&a)[4][4], u32x4 (&hp)[4]) {
#pragma unroll
  for (int m = 0; m < 4; ++m) {
#pragma unroll
    for (int k = 0; k < 4; ++k) { const f32x2 h = glu2(a[m][k >> 1][2 * (k & 1)], a[m][k >> 1][2 * (k & 1) + 1], a[m][2 + (k >> 1)][2 * (k & 1)], a[m][2 + (k >> 1)][2 * (k & 1) + 1]); hp[m][k] = cvtpk(h[0], h[1]); }
    PIN(hp[m]);
  }
}

DI void att_role(const u16* wo, char* ws, LAS char* lds, int head, int qb, int tg, int cg, int wid, u64 tag, const f32x4 (&accb)[4][4], const u32x4 (&ha)[4]) {
  const int tz = lane_id();
  const int lane = tz & 63;
  const char* kvg = ws + WS_KV + (size_t)head * 262144;
  const srd_t skv = mksrd(kvg, 262144), sw = mksrd(ws, WS_END);
  const unsigned ldsb = (unsigned)(uintptr_t)lds;
  const unsigned voff = lane * 16u;
  volatile LAS unsigned* ctl = (volatile LAS unsigned*)(lds + LDS_CTL);
  const unsigned ctla = ldsb + LDS_CTL + (lane & 7) * 4u;
  {
    u32x4 hp[4];
    glu_pack(accb, hp);
#pragma unroll
    for (int i = 0; i < 4; ++i) { *(LAS u32x4*)(lds + 131072 + wid * 4096 + i * 1024 + lane * 16) = hp[i]; *(LAS u32x4*)(lds + 49152 + wid * 4096 + i * 1024 + lane * 16) = ha[i]; }
    asm volatile("s_waitcnt lgkmcnt(0)" ::: "memory");
    ctl[12 + wid] = 1u;
  }
  {
    const int jq = unit_owner(head >> 1), jk = unit_owner(16 + (head >> 1)), jv = unit_owner(32 + (head >> 1));
    const int l = lane & 31, pw = (head & 1) * 4 + (wid >> 1) + 2 * (l & 1);
    const unsigned idx = lane < 32 ? (((l >> 1) & 7) * 32 + (l < 16 ? jk : jv)) * 8 + pw : (qb * 32 + jq) * 8 + (head & 1) * 4 + wid;
    await<NAP2>(ws, FLG_F2 + idx, tag);
  }
  STAMP(6);
#define AT_DMA1(c, i) dma16<true>(skv, voff, (unsigned)(c) * 16384u + wid * 4096u + (i) * 1024u, ldsb + ((c) & 3) * 16384u + wid * 4096u + (i) * 1024u)
#define AT_ARRIVE(k) ctl[wid] = (unsigned)((k) + 2)
#define AT_MEET(v, k) lds_meet(v, (unsigned)((k) + 2), ctla)
  AT_DMA1(0, 0); AT_DMA1(0, 1); AT_DMA1(0, 2); AT_DMA1(0, 3);
  bf16x8 qf[2][2];
  { const unsigned qo = WS_Q + (head * 64 + qb * 8 + wid * 2) * 2048u;
    ld16<true>(qf[0][0], sw, voff, qo, 0); ld16<true>(qf[0][1], sw, voff, qo, 1024); ld16<true>(qf[1][0], sw, voff, qo, 2048); ld16<true>(qf[1][1], sw, voff, qo, 3072); }
  AT_DMA1(1, 0); AT_DMA1(1, 1); AT_DMA1(1, 2); AT_DMA1(1, 3);
  AT_DMA1(2, 0); AT_DMA1(2, 1); AT_DMA1(2, 2); AT_DMA1(2, 3);
  f32x16 o[2] = {f32x16{}, f32x16{}};
  f32x4 ls[2] = {f32x4{}, f32x4{}};
  bf16x8 sel;
  { const short one = ((lane & 1) == ((lane >> 4) & 1) && (lane & 2) == 0) ? (short)0x3f80 : (short)0;
#pragma unroll
    for (int e = 0; e < 8; ++e) sel[e] = one; }
  PIN(sel);
  asm volatile("s_waitcnt vmcnt(4)" : "+v"(qf[0][0]), "+v"(qf[0][1]), "+v"(qf[1][0]), "+v"(qf[1][1])::"memory");
  AT_ARRIVE(-1);
  lds_meet(0u, 1u, ldsb + LDS_CTL + (lane & 3) * 4u);
  bf16x8 kf[2][2], vf[2][2];
  f32x16 sa, sb, negm[2];
  u32x4 pa0, pa1, pb0, pb1;
  const unsigned kp = ldsb + lane * 16u;
  kf[0][0] = lds128(LP(kp)); kf[0][1] = lds128(LP(kp) + 1024);
  {
    sa = MFMA32(kf[0][0], qf[0][0], f32x16{}); sa = MFMA32(kf[0][1], qf[0][1], sa);
    sb = MFMA32(kf[0][0], qf[1][0], f32x16{}); sb = MFMA32(kf[0][1], qf[1][1], sb);
    float ma = sa[0], mb = sb[0];
#pragma unroll
    for (int r = 1; r < 16; ++r) { ma = fmaxf(ma, sa[r]); mb = fmaxf(mb, sb[r]); }
    ma = fmaxf(ma, __shfl_xor(ma, 32)); mb = fmaxf(mb, __shfl_xor(mb, 32));
#pragma unroll
    for (int r = 0; r < 16; ++r) { negm[0][r] = -ma; negm[1][r] = -mb; sa[r] -= ma; }
  }
  PIN(negm[0]); PIN(negm[1]);
  pb0 = u32x4{}; pb1 = u32x4{}; vf[1][0] = bf16x8{}; vf[1][1] = bf16x8{};
  PIN(pb0); PIN(pb1); PIN(vf[1][0]); PIN(vf[1][1]);
#define E1(S_, i) S_[i] = EX2(S_[i])
#define CV(P_, i, S_, r) P_[i] = cvtpk(S_[r], S_[(r) + 1])
#define AT_UNIT(SC, PC0, PC1, SN, kz, mn, PP0, PP1, vz, X0, X1, X2, X3)                                       \
  do {                                                                                                       \
    SN = MFMA32(kf[kz][0], qf[mn][0], negm[mn]); SBAR(); X0;                                                  \
    E1(SC, 0); E1(SC, 1); E1(SC, 2); E1(SC, 3); PIN(SC); SBAR();                                               \
    SN = MFMA32(kf[kz][1], qf[mn][1], SN); SBAR(); X1;                                                        \
    CV(PC0, 0, SC, 0); CV(PC0, 1, SC, 2); E1(SC, 4); E1(SC, 5); E1(SC, 6); PIN(SC); PIN(PC0); SBAR();           \
    o[mn] = MFMA32(vf[vz][0], BF(PP0), o[mn]); SBAR(); X2;                                                    \
    E1(SC, 7); CV(PC0, 2, SC, 4); E1(SC, 8); CV(PC0, 3, SC, 6); E1(SC, 9); PIN(SC); PIN(PC0); SBAR();           \
    ls[mn] = MFMA16(sel, BF(PP0), ls[mn]); SBAR();                                                            \
    E1(SC, 10); E1(SC, 11); PIN(SC); SBAR();                                                                  \
    o[mn] = MFMA32(vf[vz][1], BF(PP1), o[mn]); SBAR(); X3;                                                    \
    CV(PC1, 0, SC, 8); E1(SC, 12); CV(PC1, 1, SC, 10); E1(SC, 13); E1(SC, 14); E1(SC, 15); PIN(SC); PIN(PC1); SBAR(); \
    ls[mn] = MFMA16(sel, BF(PP1), ls[mn]); SBAR();                                                            \
    CV(PC1, 2, SC, 12); CV(PC1, 3, SC, 14); PIN(PC1); SBAR();                                                  \
  } while (0)
#define AT_KT(z, KADDR, VADDR, D0, D1)                                                                        \
  do {                                                                                                       \
    AT_UNIT(sa, pa0, pa1, sb, z, 1, pb0, pb1, z ^ 1, kf[z ^ 1][0] = lds128(KADDR), kf[z ^ 1][1] = lds128((KADDR) + 1024), vf[z][0] = lds128(VADDR), vf[z][1] = lds128((VADDR) + 1024)); \
    AT_UNIT(sb, pb0, pb1, sa, z ^ 1, 0, pa0, pa1, z, D0, NOP_, D1, NOP_);                                      \
  } while (0)
  const unsigned vwo = g2_vw(lane, cg);
  STAMP(7);
  for (int c = 0; c < 16; ++c) {
    const unsigned cp = kp + (c & 3) * 16384u, cn = kp + ((c + 1) & 3) * 16384u;
    const bool kvq = c < 13;
    const srd_t sq_ = mksrd(kvq ? (const void*)kvg : (const void*)wo, kvq ? 262144u : D * D * 2u);
    const unsigned vq_ = kvq ? voff : vwo, so_ = kvq ? (c + 3) * 16384u + wid * 4096u : (8 * wid + c - 8) * 65536u, st_ = kvq ? 1024u : 16384u, m0_ = ldsb + ((c + 3) & 3) * 16384u + wid * 4096u;
#define AT_DMA2(i) dma16<true>(sq_, vq_, so_ + (i) * st_, m0_ + (i) * 1024u)
    unsigned pv;
    AT_KT(0, LP(cp) + 4096, LP(cp) + 2048, NOP_, NOP_);
    AT_KT(1, LP(cp) + 8192, LP(cp) + 4096 + 2048, NOP_, pv = ctl[lane & 7]);
    AT_MEET(pv, c - 1);
    SBAR();
    AT_KT(0, LP(cp) + 12288, LP(cp) + 8192 + 2048, AT_DMA2(0), AT_DMA2(1));
    AT_KT(1, LP(cn), LP(cp) + 12288 + 2048, AT_DMA2(2), AT_DMA2(3));
    asm volatile("s_waitcnt vmcnt(4) lgkmcnt(0)" ::: "memory");
    AT_ARRIVE(c);
  }
  STAMP(8);
  o[1] = MFMA32(vf[1][0], BF(pb0), o[1]); ls[1] = MFMA16(sel, BF(pb0), ls[1]);
  o[1] = MFMA32(vf[1][1], BF(pb1), o[1]); ls[1] = MFMA16(sel, BF(pb1), ls[1]);
  float linv[2];
  bool bad = false;
#pragma unroll
  for (int m = 0; m < 2; ++m) {
    const float lt = (lane & 16) ? ls[m][1] : ls[m][0];
    float chk = lt, nanp = lt;
#pragma unroll
    for (int r = 0; r < 16; ++r) { chk = fmaxf(chk, fabsf(o[m][r])); nanp += o[m][r]; }
    bad = bad || !(chk < 1e30f) || (nanp != nanp);
    linv[m] = 1.f / lt;
  }
  if (__builtin_expect(__any(bad), 0)) att_slow(kvg, qf, o, linv, lane);
#pragma unroll
  for (int m = 0; m < 2; ++m) {
    const unsigned T = qb * 8 + wid * 2 + m;
    const unsigned so = WS_X2 + ((T >> 2) * NKB + 112 + head) * 8192u + (T & 3) * 512u, vo = (lane >> 5) * 2048u + (lane & 31) * 16u;
    st16_sc1(sw, vo, so, pack8(o[m], 0, linv[m]));
    st16_sc1(sw, vo, so + 4096u, pack8(o[m], 1, linv[m]));
  }
  asm volatile("s_waitcnt vmcnt(0)" ::: "memory");
  publish(ws, FLG_F4 + (qb * 32 + head) * 4 + wid, tag, lane);
  STAMP(9);
  await<NAP4>(ws, FLG_F4 + ((tg >> 1) * 32 + 8 * wid + ((lane >> 1) & 7)) * 4 + 2 * (tg & 1) + (lane & 1), tag);
  { unsigned pv = 0; AT_MEET(pv, 15); }
  ctl[8 + wid] = 1u;
  STAMP(10);
  if (blockIdx.x == 0 && wid == 0) {
    gu64* p = (gu64*)(ws + WS_FLG) + (FLG_F2 + lane) * FST;
    for (unsigned spins = 0; spins < SPIN_CAP; ++spins) {
      bool ok = true;
#pragma unroll
      for (int i = 0; i < 32; ++i) ok &= __hip_atomic_load(p + i * 64 * FST, RLX_AGENT) == tag;
      if (__all(ok)) break;
    }
    publish(ws, 0, tag, lane);
  }
}

constexpr int g2_cnt(int m, bool x0, bool w0, bool x1, bool w1, bool w2) {
  return (7 - m) + (w2 ? (8 - m) >> 1 : 0) + (x1 ? 8 : 0) + (w1 ? 4 : 0) + (x0 ? m : 0) + (w0 ? (m + 1) >> 1 : 0);
}
#undef STW
#define STW (kq + 4)
DI void g2_role(const u16* x, u32x4 (&xr)[2], const u16* wo, const u16* wd, char* ws, LAS char* lds, int tg, int cg, int kq, u64 tag, const f32x4 (&accb)[4][4], const u32x4 (&ha)[4], int mt, int j) {
  const int tz = lane_id();
  const int lane = tz & 63;
  const srd_t sx2 = mksrd(ws + WS_X2 + (size_t)tg * NKB * 8192, NKB * 8192);
  const srd_t swd = mksrd(wd, F * D * 2), swo = mksrd(wo, D * D * 2);
  const unsigned ldsw = (unsigned)(uintptr_t)lds + P2_G2RING + kq * 16384u;
  const unsigned vw = g2_vw(lane, cg);
  const int t16 = lane & 15, g = lane >> 4, q4 = t16 >> 2, p4 = lane & 3;
  const unsigned vx = g * 2048u + t16 * 16u;
  volatile LAS unsigned* ctl = (volatile LAS unsigned*)(lds + LDS_CTL);
  unsigned wa[4];
  f32x4 acc[8][4];
  {
#pragma unroll
    for (int i = 0; i < 16; ++i) dma16<false>(swd, vw, (unsigned)(28 * kq) * 65536u + i * 16384u, ldsw + i * 1024u);
    u32x4 hp[4], hq[8];
    const srd_t sw = mksrd(ws, WS_END);
    const unsigned cb = (unsigned)(uintptr_t)lds + LDS_CTL;
    glu_pack(accb, hp);
    lds_meet(0u, 1u, cb + (12 + kq) * 4u);
#pragma unroll
    for (int i = 0; i < 8; ++i) hq[i] = *(LAS const u32x4*)(lds + (i < 4 ? 131072 : 49152 - 4096) + kq * 4096 + i * 1024 + lane * 16);
#pragma unroll
    for (int i = 0; i < 8; ++i) PIN(hq[i]);
    ctl[4 + kq] = ~0u;
#pragma unroll
    for (int c = 0; c < 4; ++c) { wa[c] = ldsw + (4 * g + q4) * 128 + ((c ^ ((2 * g + (q4 >> 1)) & 3)) << 5) + p4 * 8; PIN(wa[c]); }
#pragma unroll
    for (int m = 0; m < 8; ++m)
#pragma unroll
      for (int c = 0; c < 4; ++c) { acc[m][c] = f32x4{}; PIN(acc[m][c]); }
    lds_meet(0u, 1u, cb + (lane & 3) * 4u);
    if (DLY) __builtin_amdgcn_s_sleep(DLY);
    const int pair = j < 24 ? 2 * j + 1 : 24 + j;
#pragma unroll
    for (int m = 0; m < 4; ++m) { st16_sc1(sw, vx, h_base(mt, pair, kq, 1) + m * 256u, hp[m]); st16_sc1(sw, vx, h_base(mt, pair, kq, 0) + m * 256u, hq[m]); if (THR) __builtin_amdgcn_s_sleep(THR); }
    if (j < 24) {
#pragma unroll
      for (int m = 0; m < 4; ++m) { st16_sc1(sw, vx, h_base(mt, 2 * j, kq, 1) + m * 256u, ha[m]); st16_sc1(sw, vx, h_base(mt, 2 * j, kq, 0) + m * 256u, hq[4 + m]); if (THR) __builtin_amdgcn_s_sleep(THR); }
    }
    asm volatile("s_waitcnt vmcnt(0)" ::: "memory");
    publish(ws, FLG_F3 + (mt * 32 + j) * 4 + kq, tag, lane);
  }
  { const int l = lane < (kq == 3 ? 22 : 14) ? lane : 0;
    await<NAP3>(ws, FLG_F3 + ((tg >> 1) * 32 + 7 * kq + (l >> 1)) * 4 + 2 * (tg & 1) + (l & 1), tag); }
  STAMP(6);
  bf16x8 xf[2][8];
  unsigned xo = (unsigned)(28 * kq) * 8192u, wo_ = (unsigned)(28 * kq + 4) * 65536u;
#pragma unroll
  for (int z = 0; z < 2; ++z) {
#pragma unroll
    for (int m = 0; m < 8; ++m) ld16<true>(xf[z][m], sx2, vx, xo, m * 256);
    xo += 8192u; }
  s16x4 wl[2][4], wh[2][4];
#define G2_RW(z, off, c) do { LAS const char* p_ = LP(wa[c]) + (off); wl[z][c] = ldstr(p_); wh[z][c] = ldstr(p_ + 2048); } while (0)
#pragma unroll
  for (int c = 0; c < 4; ++c) G2_RW(0, 0, c);
  SBAR();
#define G2_KB(sl, WS_, RD, X0, W0, X1, W1, W2) G2_KBX(sl, WS_, RD, (((sl) + 1) & 3) * 4096, X0, W0, X1, W1, W2, 0, NOP_)
#define G2_KBX(sl, WS_, RD, ROFF, X0, W0, X1, W1, W2, EXTRA, HOOK)                                                                \
  do {                                                                                                       \
    constexpr int z = (sl) & 1;                                                                              \
    _Pragma("unroll") for (int m = 0; m < 8; ++m) {                                                          \
      switch (m) {                                                                                           \
        case 0: asm volatile("s_waitcnt vmcnt(%1)" : "+v"(xf[z][m]) : "i"(g2_cnt(0, X0, W0, X1, W1, W2) + (EXTRA))); break; \
        case 1: asm volatile("s_waitcnt vmcnt(%1)" : "+v"(xf[z][m]) : "i"(g2_cnt(1, X0, W0, X1, W1, W2) + (EXTRA))); break; \
        case 2: asm volatile("s_waitcnt vmcnt(%1)" : "+v"(xf[z][m]) : "i"(g2_cnt(2, X0, W0, X1, W1, W2) + (EXTRA))); break; \
        case 3: asm volatile("s_waitcnt vmcnt(%1)" : "+v"(xf[z][m]) : "i"(g2_cnt(3, X0, W0, X1, W1, W2) + (EXTRA))); break; \
        case 4: asm volatile("s_waitcnt vmcnt(%1)" : "+v"(xf[z][m]) : "i"(g2_cnt(4, X0, W0, X1, W1, W2) + (EXTRA))); break; \
        case 5: asm volatile("s_waitcnt vmcnt(%1)" : "+v"(xf[z][m]) : "i"(g2_cnt(5, X0, W0, X1, W1, W2) + (EXTRA))); break; \
        case 6: asm volatile("s_waitcnt vmcnt(%1)" : "+v"(xf[z][m]) : "i"(g2_cnt(6, X0, W0, X1, W1, W2) + (EXTRA))); break; \
        default: asm volatile("s_waitcnt vmcnt(%1)" : "+v"(xf[z][m]) : "i"(g2_cnt(7, X0, W0, X1, W1, W2) + (EXTRA))); break; \
      }                                                                                                      \
      mfma16a(acc[m][0], cat8(wl[z][0], wh[z][0]), xf[z][m]); SBAR();                                         \
      if (RD && m >= 2 && m < 6) G2_RW(z ^ 1, ROFF, m - 2);                                \
      SBAR();                                                                                                \
      mfma16a(acc[m][1], cat8(wl[z][1], wh[z][1]), xf[z][m]);                                                 \
      mfma16a(acc[m][2], cat8(wl[z][2], wh[z][2]), xf[z][m]);                                                 \
      mfma16a(acc[m][3], cat8(wl[z][3], wh[z][3]), xf[z][m]); SBAR();                                         \
      HOOK;                                                                                                  \
      if (X0) ld16_inplace(xf[z][m], sx2, vx, xo, m * 256);                                                  \
      if (W0 && !(m & 1)) dma16<false>(WS_, vw, wo_ + (m >> 1) * 16384u, ldsw + (sl) * 4096u + (m >> 1) * 1024u); \
      SBAR();                                                                                                \
    }                                                                                                        \
    xo += 8192u; wo_ += 65536u;                                                                              \
  } while (0)
#define YES true
#define NO false
  STAMP(7);
  G2_KB(0, swd, YES, YES, YES, YES, NO, NO); G2_KB(1, swd, YES, YES, YES, YES, YES, NO); G2_KB(2, swd, YES, YES, YES, YES, YES, YES); G2_KB(3, swd, YES, YES, YES, YES, YES, YES);
#define PACE 0
#define G2_KP(sl) G2_KBX(sl, swd, YES, (((sl) + 1) & 3) * 4096, YES, YES, YES, YES, YES, 0, if (PACE && (m % PACEM) == 0) __builtin_amdgcn_s_sleep(PACE))
  for (int i = 4; i < 24; i += 4) { G2_KP(0); G2_KP(1); G2_KP(2); G2_KP(3); }
  wo_ = (unsigned)(8 * kq) * 65536u;
  G2_KB(0, swo, YES, YES, YES, YES, YES, YES); G2_KB(1, swo, YES, YES, YES, YES, YES, YES);
  STAMP(8);
  for (unsigned spins = 0; spins < SPIN_CAP; ++spins) { if (rfl(ctl[8 + kq]) != 0u) break; __builtin_amdgcn_s_sleep(1); }
  STAMP(9);
  xo = (unsigned)(112 + 8 * kq) * 8192u;
  G2_KB(2, swo, YES, YES, YES, YES, YES, YES); G2_KB(3, swo, YES, YES, YES, YES, YES, YES);
  G2_KB(0, swo, YES, YES, YES, YES, YES, YES); G2_KB(1, swo, YES, YES, NO, YES, YES, YES); G2_KB(2, swo, YES, YES, NO, YES, NO, YES); G2_KB(3, swo, YES, YES, NO, YES, NO, NO);
#pragma unroll
  for (int c = 0; c < 4; ++c) wa[c] -= P2_G2RING + kq * 12288u;
  G2_KBX(0, swo, YES, 0, YES, NO, YES, NO, NO, 0, NOP_); G2_KBX(1, swo, YES, 16384, YES, NO, YES, NO, NO, 0, NOP_);
  G2_KBX(2, swo, YES, 32768, NO, NO, YES, NO, NO, 0, NOP_);
  const int ty = lane_id();
  { const srd_t sxi = mksrd(x, S * D * 2);
    const unsigned xv = (unsigned)((ty & 63) >> 3) * 2048u + (ty & 7) * 16u, xs = (unsigned)(tg * 128 + (4 + kq) * 16) * 2048u + cg * 128u;
    ld16<false>(xr[0], sxi, xv, xs, 0); ld16<false>(xr[1], sxi, xv, xs + 16384u, 0); }
  unsigned pa[4];
#pragma unroll
  for (int c = 0; c < 4; ++c) pa[c] = (unsigned)(uintptr_t)lds + kq * 4096u + (ty & 15) * 256u + (((4 * c + ((ty >> 4) & 3)) ^ (ty & 15)) << 4);
  const unsigned pown = P2_G2RING + kq * 12288u;
#define G2_PUT(mm) do { _Pragma("unroll") for (int c = 0; c < 4; ++c) *(LAS f32x4*)((LAS char*)(uintptr_t)((mm) < 4 ? pa[c] : pa[c] + pown) + ((mm) < 4 ? (mm) * 16384 : ((mm) - 4) * 4096)) = acc[mm][c]; } while (0)
  G2_KBX(3, swo, NO, 0, NO, NO, NO, NO, NO, 2, if (m >= 2) G2_PUT(m >= 2 ? m - 2 : 0));
  asm volatile("s_nop 7" ::: "memory");
  G2_PUT(6); G2_PUT(7);
  asm volatile("s_waitcnt vmcnt(0)" : "+v"(xr[0]), "+v"(xr[1]));
  STAMP(10);
}

#undef STW
#define STW wid
__global__ __launch_bounds__(512, 2) void fused_kernel(const u16* x, const u16* wqkv, const u16* wo, const u16* wg, const u16* wu, const u16* wd, char* ws, u16* out) {
  extern __shared__ __attribute__((aligned(16))) char smem[];
  LAS char* lds = (LAS char*)smem;
  const int b = blockIdx.x, xcd = b & 7, r = b >> 3, wid = rfl(threadIdx.x >> 6);
  u64 tag = *(const __attribute__((address_space(4))) u64*)(ws + WS_FLG);
  if (threadIdx.x < 16) ((LAS unsigned*)(lds + LDS_CTL))[threadIdx.x] = 0u;
  const int tg = (xcd >> 1) * 4 + (r & 3), cg = (xcd & 1) * 8 + (r >> 2);
  const int mt = r & 7, j = xcd + 8 * (r >> 3);
  srd_t scode;
  { u64 pc; unsigned len;
    asm volatile("s_getpc_b64 %0\n1:\n\ts_mov_b32 %1, .Lfk_code_end-1b" : "=s"(pc), "=s"(len));
    scode = mksrd((const void*)(pc & ~127ull), len); }
  STAMP(0);
  dma16<false>(scode, (threadIdx.x & 63) * 16u, (unsigned)((r * 8 + wid) & 127) * 1024u, (unsigned)(uintptr_t)lds + LDS_DUMP + wid * 1024u);
  ln_row(x, ws, mt * 256 + j * 8 + wid);
  asm volatile("s_waitcnt vmcnt(0)" : "+s"(tag)::"memory");
  tag += 1;
  STAMP(1);
  asm volatile("s_barrier" ::: "memory");
  if (wid == 0) publish(ws, FLG_F1 + mt * 32 + j, tag, lane_id());
  f32x4 accb[4][4];
  u32x4 ha[4];
  if (j < 16) g1_body<0>(wqkv, wg, wu, ws, lds, wid, mt, j, scode, tag, accb, ha);
  else if (j < 24) g1_body<1>(wqkv, wg, wu, ws, lds, wid, mt, j, scode, tag, accb, ha);
  else g1_body<2>(wqkv, wg, wu, ws, lds, wid, mt, j, scode, tag, accb, ha);
  u32x4 xr[2];
  if (wid >= 4) {
    g2_role(x, xr, wo, wd, ws, lds, tg, cg, wid - 4, tag, accb, ha, mt, j);
  } else {
#define ATT_PRIO 1
    __builtin_amdgcn_s_setprio(ATT_PRIO);
    att_role(wo, ws, lds, xcd * 4 + (r >> 3), r & 7, tg, cg, wid, tag, accb, ha);
    const int ty = lane_id();
#pragma unroll
    for (int e = 0; e < 2; ++e) xr[e] = *(const u32x4*)(x + (size_t)(tg * 128 + wid * 16 + e * 8 + ((ty & 63) >> 3)) * D + cg * 64 + (ty & 7) * 8);
  }
  const int tz = lane_id();
  const int ln = tz & 63;
  size_t off[2];
#pragma unroll
  for (int e = 0; e < 2; ++e) off[e] = (size_t)(tg * 128 + wid * 16 + e * 8 + (ln >> 3)) * D + cg * 64 + (ln & 7) * 8;
  asm volatile("s_waitcnt lgkmcnt(0)\n\ts_barrier" ::: "memory");
  STAMP(11);
#pragma unroll
  for (int e = 0; e < 2; ++e) {
    const int tk = e * 8 + (ln >> 3);
    LAS const char* src = lds + (wid < 4 ? wid * 16384 : P2_G2RING + (wid - 4) * 4096) + tk * 256;
    const int kst = wid < 4 ? 4096 : 16384;
    f32x4 lo = f32x4{}, hi = f32x4{};
#pragma unroll
    for (int k = 0; k < 4; ++k) { lo += *(LAS const f32x4*)(src + k * kst + (((2 * (ln & 7)) ^ tk) << 4)); hi += *(LAS const f32x4*)(src + k * kst + (((2 * (ln & 7) + 1) ^ tk) << 4)); }
    u32x4 w;
    w[0] = cvtpk(lo[0] + blo(xr[e][0]), lo[1] + bhi(xr[e][0])); w[1] = cvtpk(lo[2] + blo(xr[e][1]), lo[3] + bhi(xr[e][1]));
    w[2] = cvtpk(hi[0] + blo(xr[e][2]), hi[1] + bhi(xr[e][2])); w[3] = cvtpk(hi[2] + blo(xr[e][3]), hi[3] + bhi(xr[e][3]));
    *(u32x4*)(out + off[e]) = w;
  }
  STAMP(12);
  asm volatile(".Lfk_code_end:");
}

extern "C" void kernel_launch(void* const* d_in, const int* in_sizes, int n_in, void* d_out, int out_size, void* d_ws, size_t ws_size, hipStream_t stream) {
  (void)hipFuncSetAttribute((const void*)fused_kernel, hipFuncAttributeMaxDynamicSharedMemorySize, LDS_BYTES);
  fused_kernel<<<256, 512, LDS_BYTES, stream>>>((const u16*)d_in[0], (const u16*)d_in[1], (const u16*)d_in[2], (const u16*)d_in[4], (const u16*)d_in[5], (const u16*)d_in[6], (char*)d_ws, (u16*)d_out);
}
```

```cpp
#include <hip/hip_runtime.h>
#include <hip/hip_bf16.h>
#include <stdint.h>

typedef unsigned short u16;
typedef unsigned long long u64;
typedef short bf16x8 __attribute__((ext_vector_type(8)));
typedef short s16x4 __attribute__((ext_vector_type(4)));
typedef float f32x16 __attribute__((ext_vector_type(16)));
typedef float f32x4 __attribute__((ext_vector_type(4)));
typedef float f32x2 __attribute__((ext_vector_type(2)));
typedef unsigned u32x4 __attribute__((ext_vector_type(4)));
typedef unsigned u32x2 __attribute__((ext_vector_type(2)));
typedef __bf16 bf16x2_t __attribute__((ext_vector_type(2)));
typedef short v4i16_t __attribute__((ext_vector_type(4)));
typedef __amdgpu_buffer_rsrc_t srd_t;
typedef __attribute__((address_space(1))) u64 gu64;
#define LAS __attribute__((address_space(3)))
#define DI __device__ __forceinline__
#define SBAR() __builtin_amdgcn_sched_barrier(0)
#define PIN(x) asm volatile("" : "+v"(x))
#define NOP_ ((void)0)
#define LP(a) ((LAS const char*)(uintptr_t)(a))
#define MFMA32(a, b, c) __builtin_amdgcn_mfma_f32_32x32x16_bf16((a), (b), (c), 0, 0, 0)
#define MFMA16(a, b, c) __builtin_amdgcn_mfma_f32_16x16x32_bf16((a), (b), (c), 0, 0, 0)
#define EX2(v) __builtin_amdgcn_exp2f(v)
#define BF(x) __builtin_bit_cast(bf16x8, x)
#define RLX_AGENT __ATOMIC_RELAXED, __HIP_MEMORY_SCOPE_AGENT

constexpr int S = 2048, D = 1024, F = 3584;
constexpr int NKB = (F + D) / 32;
constexpr float LOG2E = 1.4426950408889634f;
constexpr float QSCALE = 0.17677669529663687f * LOG2E;

constexpr unsigned WS_FLG = 0;
constexpr unsigned FLG_F1 = 256, FLG_F2 = FLG_F1 + 2048, FLG_F3 = FLG_F2 + 2048, FLG_F4 = FLG_F3 + 2048;
constexpr unsigned FST = 16;
constexpr unsigned WS_XN = 2097152;
constexpr unsigned WS_Q = WS_XN + S * D * 2;
constexpr unsigned WS_KV = WS_Q + S * D * 2;
constexpr unsigned WS_X2 = WS_KV + S * D * 4;
constexpr unsigned WS_END = WS_X2 + S * (F + D) * 2;
constexpr int G1_XS = 32768, G1_WB = 65536, G1_WS = 40960;
constexpr int P2_G2RING = 65536;
constexpr int LDS_CTL = G1_WB + 2 * G1_WS, LDS_DUMP = LDS_CTL + 1024, LDS_BYTES = LDS_DUMP + 8192;
constexpr unsigned SPIN_CAP = 1u << 20;

DI unsigned rfl(unsigned x) { return __builtin_amdgcn_readfirstlane(x); }
DI int lane_id() { int l; asm volatile("v_mbcnt_lo_u32_b32 %0, -1, 0\n\tv_mbcnt_hi_u32_b32 %0, -1, %0" : "=v"(l)); return l; }
DI unsigned cvtpk(float lo, float hi) { f32x2 v = {lo, hi}; bf16x2_t b = __builtin_convertvector(v, bf16x2_t); return __builtin_bit_cast(unsigned, b); }
DI float rbf(float f) { return __uint_as_float(cvtpk(f, f) << 16); }
DI float blo(unsigned u) { return __uint_as_float(u << 16); }
DI float bhi(unsigned u) { return __uint_as_float(u & 0xffff0000u); }
DI srd_t mksrd(const void* p, unsigned bytes) { return __builtin_amdgcn_make_buffer_rsrc((void*)p, 0, bytes, 0x00020000); }
template <bool SC1> DI void dma16(srd_t srd, unsigned voff, unsigned soff, unsigned m0v) {
  if (SC1) asm volatile("s_mov_b32 m0, %0\n\ts_nop 0\n\tbuffer_load_dwordx4 %1, %2, %3 offen sc1 lds" ::"s"(m0v), "v"(voff), "s"(srd), "s"(soff) : "memory");
  else asm volatile("s_mov_b32 m0, %0\n\ts_nop 0\n\tbuffer_load_dwordx4 %1, %2, %3 offen lds" ::"s"(m0v), "v"(voff), "s"(srd), "s"(soff) : "memory");
}
template <bool SC1, class T> DI void ld16(T& d, srd_t srd, unsigned voff, unsigned soff, int imm) {
  if (SC1) asm volatile("buffer_load_dwordx4 %0, %1, %2, %3 offen offset:%4 sc1" : "=v"(d) : "v"(voff), "s"(srd), "s"(soff), "i"(imm) : "memory");
  else asm volatile("buffer_load_dwordx4 %0, %1, %2, %3 offen offset:%4" : "=v"(d) : "v"(voff), "s"(srd), "s"(soff), "i"(imm) : "memory");
}
template <class T> DI void ld16_inplace(T& d, srd_t srd, unsigned voff, unsigned soff, int imm) {
  asm volatile("buffer_load_dwordx4 %0, %1, %2, %3 offen offset:%4 sc1" : "+v"(d) : "v"(voff), "s"(srd), "s"(soff), "i"(imm) : "memory");
}
DI void st16_sc1(srd_t srd, unsigned voff, unsigned soff, u32x4 v) {
  asm volatile("buffer_store_dwordx4 %0, %1, %2, %3 offen sc1\n\ts_nop 1" ::"v"(v), "v"(voff), "s"(srd), "s"(soff) : "memory");
}
DI s16x4 ldstr(LAS const char* p) { return __builtin_bit_cast(s16x4, __builtin_amdgcn_ds_read_tr16_b64_v4i16((LAS v4i16_t*)p)); }
DI bf16x8 lds128(LAS const char* p) { return *(LAS const bf16x8*)p; }
DI bf16x8 cat8(s16x4 lo, s16x4 hi) { return __builtin_shufflevector(lo, hi, 0, 1, 2, 3, 4, 5, 6, 7); }
DI u32x4 pack8(const f32x16& v, int s, float sc) {
  u32x4 w;
  w[0] = cvtpk(v[8 * s + 0] * sc, v[8 * s + 1] * sc); w[1] = cvtpk(v[8 * s + 2] * sc, v[8 * s + 3] * sc);
  w[2] = cvtpk(v[8 * s + 4] * sc, v[8 * s + 5] * sc); w[3] = cvtpk(v[8 * s + 6] * sc, v[8 * s + 7] * sc);
  return w;
}
DI u32x4 pack44(const f32x4& a, const f32x4& b, float sc) {
  u32x4 w; w[0] = cvtpk(a[0] * sc, a[1] * sc); w[1] = cvtpk(a[2] * sc, a[3] * sc); w[2] = cvtpk(b[0] * sc, b[1] * sc); w[3] = cvtpk(b[2] * sc, b[3] * sc); return w;
}
DI void mfma16a(f32x4& c, const bf16x8& a, const bf16x8& b) { asm volatile("v_mfma_f32_16x16x32_bf16 %0, %1, %2, %0" : "+v"(c) : "v"(a), "v"(b)); }
template <int N> DI void mfma16w(f32x4& c, const bf16x8& a, const bf16x8& b) { asm volatile("s_waitcnt vmcnt(%3)\n\tv_mfma_f32_16x16x32_bf16 %0, %1, %2, %0" : "+v"(c) : "v"(a), "v"(b), "i"(N)); }
template <int CTRL, int RM> DI float dppadd(float v) { return v + __builtin_bit_cast(float, __builtin_amdgcn_update_dpp(0, __builtin_bit_cast(int, v), CTRL, RM, 0xf, true)); }
DI float wave_sum(float v) {
  v = dppadd<0x111, 0xf>(v); v = dppadd<0x112, 0xf>(v); v = dppadd<0x114, 0xf>(v); v = dppadd<0x118, 0xf>(v);
  v = dppadd<0x142, 0xa>(v); v = dppadd<0x143, 0xc>(v);
  return __builtin_bit_cast(float, __builtin_amdgcn_readlane(__builtin_bit_cast(int, v), 63));
}
DI void publish(char* ws, unsigned idx, u64 tag, int lane) {
  const u32x2 tv = {(unsigned)tag, (unsigned)(tag >> 32)};
  asm volatile("buffer_store_dwordx2 %0, %1, %2, %3 offen sc1" ::"v"(tv), "v"(lane == 0 ? 0u : 0x7ff00000u), "s"(mksrd(ws + WS_FLG, WS_XN)), "s"(idx * (8u * FST)) : "memory");
}
DI void publish_last(char* ws, unsigned cnt_addr, unsigned n, unsigned idx, u64 tag, int lane) {
  unsigned old;
  asm volatile("ds_add_rtn_u32 %0, %1, %2\n\ts_waitcnt lgkmcnt(0)" : "=v"(old) : "v"(cnt_addr), "v"(lane == 0 ? 1u : 0u) : "memory");
  if (rfl(old) == n - 1) publish(ws, idx, tag, lane);
}
DI void await(char* ws, unsigned idx, u64 tag) {
  gu64* p = (gu64*)(ws + WS_FLG) + idx * FST;
  for (unsigned spins = 0; spins < SPIN_CAP; ++spins) {
    if (__all(__hip_atomic_load(p, RLX_AGENT) == tag)) break;
    __builtin_amdgcn_s_sleep(1);
  }
  asm volatile("" ::: "memory");
}

#define STW wid
#define STAMP(i) do {} while (0)

DI unsigned g2_vw(int lane, int cg) { const unsigned l3 = lane >> 3, l7 = lane & 7; return l3 * 2048u + ((l7 ^ (((l3 >> 1) & 3) << 1)) << 4) + cg * 128u; }

DI void ln_row(const u16* x, char* ws, int row) {
  const int lane = lane_id();
  const srd_t sxi = mksrd(x, S * D * 2), sx = mksrd(ws + WS_XN, S * D * 2);
  u32x4 a, b;
  ld16<false>(a, sxi, lane * 16u, row * 2048u, 0); ld16<false>(b, sxi, lane * 16u, row * 2048u, 1024);
  asm volatile("s_waitcnt vmcnt(0)" : "+v"(a), "+v"(b));
  float v[16];
#pragma unroll
  for (int i = 0; i < 4; ++i) { v[2 * i] = blo(a[i]); v[2 * i + 1] = bhi(a[i]); v[8 + 2 * i] = blo(b[i]); v[9 + 2 * i] = bhi(b[i]); }
  float s = 0.f;
#pragma unroll
  for (int i = 0; i < 16; ++i) s += v[i];
  const float mean = wave_sum(s) * (1.f / D);
  float q = 0.f;
#pragma unroll
  for (int i = 0; i < 16; ++i) { const float d = v[i] - mean; q += d * d; }
  const float mb = rbf(mean), vb = rbf(wave_sum(q) * (1.f / D));
  const float inv = __builtin_amdgcn_rcpf(rbf(__builtin_amdgcn_sqrtf(rbf(vb + 1e-5f))));
  u32x4 oa, ob;
#pragma unroll
  for (int i = 0; i < 4; ++i) { oa[i] = cvtpk(rbf(v[2 * i] - mb) * inv, rbf(v[2 * i + 1] - mb) * inv); ob[i] = cvtpk(rbf(v[8 + 2 * i] - mb) * inv, rbf(v[9 + 2 * i] - mb) * inv); }
  st16_sc1(sx, lane * 16u, row * 2048u, oa); st16_sc1(sx, lane * 16u + 1024u, row * 2048u, ob);
}

DI f32x2 glu2(float g0, float g1, float u0, float u1) { return f32x2{u0 * __builtin_amdgcn_rcpf(1.f + EX2(g0 * -LOG2E)), u1 * __builtin_amdgcn_rcpf(1.f + EX2(g1 * -LOG2E))}; }
DI unsigned h_base(int mt, int pair, int wr, int wc) { return WS_X2 + ((mt * 2 + (wr >> 1)) * NKB + 2 * pair + wc) * 8192u + (wr & 1) * 1024u; }
template <int KIND>
DI void g1_body(const u16* wd, int cg, const u16* wqkv, const u16* wg, const u16* wu, char* ws, LAS char* lds, int wid, int mt, int j, srd_t scode, u64 tag, f32x4 (&accb)[4][4], u32x4 (&ha)[4]) {
  const int lane = lane_id(), wr = wid & 3, wc = wid >> 2;
  constexpr bool SWAP0 = KIND == 0, isY = KIND == 2;
  const int a0 = KIND == 0 ? 32 + j : (KIND == 1 ? j - 16 : 3 * j - 64);
  const srd_t sx = mksrd(ws + WS_XN, S * D * 2), sq = mksrd(wqkv, D * 3 * D * 2), sg = mksrd(wg, D * F * 2), su = mksrd(wu, D * F * 2);
  const srd_t sa = isY ? sq : sg, sb = isY ? sq : su;
  const unsigned ld1 = isY ? 6144u : 7168u;
  const unsigned c0 = a0 * 128u, c1 = isY ? c0 + 128u : j * 256u, c2 = isY ? c0 + 256u : j * 256u, c3 = isY ? (24 + j) * 128u : j * 256u + 128u;
  const unsigned l3 = lane >> 3, l7 = lane & 7, l4 = lane >> 4;
  const unsigned vx0 = l3 * 2048u + ((l7 ^ l4) << 4), vx1 = vx0 ^ 64u;
  const unsigned wsw = (l7 ^ (((l4 & 1) | ((wid & 1) << 1)) << 1)) << 4;
  const unsigned vw0 = l3 * 6144u + wsw, vw2 = l3 * 7168u + wsw;
  const unsigned vw1 = isY ? vw0 : vw2;
  const unsigned ldsb = (unsigned)(uintptr_t)lds;
  unsigned sox = (unsigned)(mt * 256 + wid * 32) * 2048u;
  unsigned so0 = wid * 8 * 6144u + c0, so1 = wid * 8 * ld1, so2 = wid * 8 * 7168u + c3;
  const unsigned m0x = ldsb + wid * 4096u, m0w = ldsb + G1_WB + wid * 1024u;
#define G1_DX(i, st) dma16<true>(sx, ((i) & 1) ? vx1 : vx0, sox + (i) * 16384u, m0x + (st) * G1_XS + (i) * 1024u)
#define G1_DW(i, st)                                                                             \
  do {                                                                                           \
    if ((i) == 0) dma16<false>(sq, vw0, so0, m0w + (st) * G1_WS);                                 \
    if ((i) == 1) dma16<false>(sa, vw1, so1 + c1, m0w + (st) * G1_WS + 8192u);                    \
    if ((i) == 2) dma16<false>(sb, vw1, so1 + c2, m0w + (st) * G1_WS + 16384u);                   \
    if ((i) == 3) dma16<false>(sg, vw2, so2, m0w + (st) * G1_WS + 24576u);                        \
    if ((i) == 4) dma16<false>(su, vw2, so2, m0w + (st) * G1_WS + 32768u);                        \
  } while (0)
#define G1_ADVX() do { sox += 128u; } while (0)
#define G1_ADVW() do { so0 += 64u * 6144u; so1 += 64u * ld1; so2 += 64u * 7168u; } while (0)
#define G1_CODE() dma16<false>(scode, vx0, (unsigned)(mt * 16384 + ((j >> 3) & 1) * 1024 + wid * 128), ldsb + LDS_DUMP + wid * 1024u)
  f32x4 acc[4][10];
  G1_DW(0, 0); G1_DW(1, 0); G1_DW(2, 0); G1_DW(3, 0); G1_DW(4, 0); G1_ADVW();
#pragma unroll
  for (int m = 0; m < 4; ++m)
#pragma unroll
    for (int n = 0; n < 10; ++n) { acc[m][n] = f32x4{}; PIN(acc[m][n]); }
  await(ws, FLG_F1 + mt * 32 + wid * 4 + (lane & 3), tag);
  STAMP(2);
  G1_DX(0, 0); G1_DX(1, 0); G1_DX(2, 0); G1_DX(3, 0); G1_ADVX();
  G1_DX(0, 1); G1_DX(1, 1); G1_DX(2, 1); G1_DX(3, 1); G1_ADVX();
  G1_DW(0, 1); G1_DW(1, 1); G1_DW(2, 1); G1_DW(3, 1); G1_DW(4, 1); G1_ADVW();
  const int t16 = lane & 15, g = lane >> 4, q4 = t16 >> 2, p4 = lane & 3;
  unsigned xa0 = ldsb + (wr * 64 + t16) * 128 + ((g ^ ((t16 >> 1) & 7)) << 4), xa1 = xa0 ^ 64u;
  unsigned wa00 = ldsb + G1_WB + (8 * g + q4) * 128 + (((2 * wc) ^ (((q4 >> 1) & 1) | ((g & 1) << 1))) << 5) + p4 * 8;
  unsigned wa01 = wa00 ^ 32u, wa10 = wa00 + G1_WS, wa11 = wa01 + G1_WS;
  asm volatile("" : "+v"(xa0), "+v"(xa1), "+v"(wa00), "+v"(wa01), "+v"(wa10), "+v"(wa11));
  bf16x8 xf[2][4];
  s16x4 wl[10], wh[10];
#define G1_RX(st, s, m) xf[s][m] = lds128(LP((s) ? xa1 : xa0) + (st) * G1_XS + (m) * 2048)
#define G1_RW(st, s, n)                                                                                      \
  do {                                                                                                       \
    LAS const char* p_ = LP((st) ? (((n) & 1) ? wa11 : wa10) : (((n) & 1) ? wa01 : wa00)) + ((n) >> 1) * 8192 + (s) * 4096; \
    wl[n] = ldstr(p_); wh[n] = ldstr(p_ + 512);                                                              \
  } while (0)
#define G1_MM(s, n, m)                                                                                       \
  do {                                                                                                       \
    if (SWAP0 && (n) < 2) mfma16a(acc[m][n], xf[s][m], cat8(wl[n], wh[n]));                                   \
    else mfma16a(acc[m][n], cat8(wl[n], wh[n]), xf[s][m]);                                                    \
  } while (0)
#define G1_GRP(s, n, A, B, C) do { G1_MM(s, n, 0); SBAR(); A; SBAR(); G1_MM(s, n, 1); SBAR(); B; SBAR(); G1_MM(s, n, 2); SBAR(); C; SBAR(); G1_MM(s, n, 3); SBAR(); } while (0)
#define G1_WAITBAR() asm volatile("s_waitcnt vmcnt(0) lgkmcnt(0)\n\ts_barrier" ::: "memory")
#define G1_HEAD(st)                                                                                          \
  do {                                                                                                       \
    G1_GRP(0, 0, G1_RW(st, 0, 9), NOP_, NOP_);                                                               \
    G1_GRP(0, 1, G1_RW(st, 1, 0), G1_RX(st, 1, 0), NOP_);                                                    \
    G1_GRP(0, 2, G1_RW(st, 1, 1), G1_RX(st, 1, 1), NOP_);                                                    \
    G1_GRP(0, 3, G1_RW(st, 1, 2), G1_RX(st, 1, 2), NOP_);                                                    \
    G1_GRP(0, 4, G1_RW(st, 1, 3), G1_RX(st, 1, 3), NOP_);                                                    \
    G1_GRP(0, 5, G1_RW(st, 1, 4), NOP_, NOP_);                                                               \
    G1_GRP(0, 6, G1_RW(st, 1, 5), NOP_, NOP_);                                                               \
    G1_GRP(0, 7, G1_RW(st, 1, 6), NOP_, NOP_);                                                               \
    G1_GRP(0, 8, G1_RW(st, 1, 7), NOP_, NOP_);                                                               \
    G1_GRP(0, 9, G1_RW(st, 1, 8), NOP_, NOP_);                                                               \
    G1_GRP(1, 0, G1_RW(st, 1, 9), NOP_, NOP_);                                                               \
    G1_GRP(1, 1, NOP_, NOP_, NOP_);                                                                          \
    G1_GRP(1, 2, NOP_, NOP_, NOP_);                                                                          \
    G1_WAITBAR();                                                                                            \
  } while (0)
#define G1_TILE(st, DM)                                                                                      \
  do {                                                                                                       \
    G1_HEAD(st);                                                                                             \
    G1_GRP(1, 3, G1_RX(st ^ 1, 0, 0), G1_RW(st ^ 1, 0, 0), if (DM) G1_DX(0, st));                             \
    G1_GRP(1, 4, G1_RX(st ^ 1, 0, 1), G1_RW(st ^ 1, 0, 1), if (DM) G1_DW(0, st));                             \
    G1_GRP(1, 5, G1_RX(st ^ 1, 0, 2), G1_RW(st ^ 1, 0, 2), if (DM) G1_DX(1, st));                             \
    G1_GRP(1, 6, G1_RX(st ^ 1, 0, 3), G1_RW(st ^ 1, 0, 3), if (DM) G1_DW(1, st));                             \
    G1_GRP(1, 7, G1_RW(st ^ 1, 0, 4), G1_RW(st ^ 1, 0, 5), if (DM) { G1_DX(2, st); G1_DW(2, st); });          \
    G1_GRP(1, 8, G1_RW(st ^ 1, 0, 6), G1_RW(st ^ 1, 0, 7), if (DM) { G1_DX(3, st); G1_DW(3, st); });          \
    G1_GRP(1, 9, G1_RW(st ^ 1, 0, 8), NOP_, if (DM) { G1_DW(4, st); G1_ADVX(); G1_ADVW(); });                 \
  } while (0)
  asm volatile("s_waitcnt vmcnt(9)\n\ts_barrier" ::: "memory");
  G1_RX(0, 0, 0); G1_RW(0, 0, 0); G1_RX(0, 0, 1); G1_RX(0, 0, 2); G1_RX(0, 0, 3);
  G1_RW(0, 0, 1); G1_RW(0, 0, 2); G1_RW(0, 0, 3); G1_RW(0, 0, 4); G1_RW(0, 0, 5); G1_RW(0, 0, 6); G1_RW(0, 0, 7); G1_RW(0, 0, 8);
  SBAR();
  STAMP(3);
  for (int t = 0; t < 14; t += 2) {
    G1_CODE();
    G1_TILE(0, true); G1_TILE(1, true);
  }
  STAMP(4);
  {
    const srd_t sw = mksrd(ws, WS_END);
    unsigned vq, vk;
    auto store_addr = [&]() { const unsigned l = lane_id(); vq = (l >> 4) * 512u + (l & 15) * 16u; vk = (l >> 4) * 512u + (2 * (l & 8) + (l & 7)) * 16u; };
    const unsigned T32 = mt * 8 + wr * 2;
    auto store_u = [&](int u, int i) {
      const int a = a0 + u; const unsigned head = 2 * (a & 15) + wc; const bool isq = a < 16;
      if (SWAP0) st16_sc1(sw, vk, WS_KV + (head * 64 + T32 + (i >> 1)) * 4096u + 2048u + (i & 1) * 128u, pack44(acc[2 * (i >> 1)][i & 1], acc[2 * (i >> 1) + 1][i & 1], 1.f));
      else st16_sc1(sw, isq ? vq : vk, isq ? WS_Q + (head * 64 + T32 + (i >> 1)) * 2048u + (i & 1) * 256u : WS_KV + (head * 64 + T32 + (i >> 1)) * 4096u + (i & 1) * 128u,
                    pack44(acc[i][2 * u], acc[i][2 * u + 1], isq ? QSCALE : 1.f));
    };
#define G1_HA(k) do { if (!isY) { const f32x2 h_ = glu2(acc[(k) >> 2][2 + (((k) >> 1) & 1)][2 * ((k) & 1)], acc[(k) >> 2][2 + (((k) >> 1) & 1)][2 * ((k) & 1) + 1], acc[(k) >> 2][4 + (((k) >> 1) & 1)][2 * ((k) & 1)], acc[(k) >> 2][4 + (((k) >> 1) & 1)][2 * ((k) & 1) + 1]); \
      unsigned w_ = cvtpk(h_[0], h_[1]); PIN(w_); ha[(k) >> 2][(k) & 3] = w_; } } while (0)
#define G1_SY(u, i) do { if (isY) store_u(u, i); } while (0)
#define G1_RD(d, st, s, n)                                                                                   \
  do {                                                                                                       \
    LAS const char* p_ = LP((st) ? (((n) & 1) ? wa11 : wa10) : (((n) & 1) ? wa01 : wa00)) + ((n) >> 1) * 8192 + (s) * 4096; \
    wl[d] = ldstr(p_); wh[d] = ldstr(p_ + 512);                                                              \
  } while (0)
#define G1_MD(d, s, n, m)                                                                                    \
  do {                                                                                                       \
    if (SWAP0 && (n) < 2) mfma16a(acc[m][n], xf[s][m], cat8(wl[d], wh[d]));                                   \
    else mfma16a(acc[m][n], cat8(wl[d], wh[d]), xf[s][m]);                                                    \
  } while (0)
#define G1_GD(d, s, n, A, B, C) do { G1_MD(d, s, n, 0); SBAR(); A; SBAR(); G1_MD(d, s, n, 1); SBAR(); B; SBAR(); G1_MD(d, s, n, 2); SBAR(); C; SBAR(); G1_MD(d, s, n, 3); SBAR(); } while (0)
    G1_GD(0, 0, 0, G1_RD(9, 0, 1, 0), G1_RX(0, 1, 0), NOP_);
    G1_GD(1, 0, 1, G1_RD(0, 0, 1, 1), G1_RX(0, 1, 1), NOP_);
    G1_GD(2, 0, 2, G1_RD(1, 0, 1, 2), G1_RX(0, 1, 2), NOP_);
    G1_GD(3, 0, 3, G1_RD(2, 0, 1, 3), G1_RX(0, 1, 3), NOP_);
    G1_GD(4, 0, 4, G1_RD(3, 0, 1, 4), NOP_, NOP_);
    G1_GD(5, 0, 5, G1_RD(4, 0, 1, 5), NOP_, NOP_);
    G1_GD(9, 1, 0, NOP_, NOP_, NOP_);
    asm volatile("s_waitcnt vmcnt(0)\n\ts_barrier" ::: "memory");
    G1_GD(0, 1, 1, G1_RX(1, 0, 0), G1_RD(5, 1, 0, 0), G1_RX(1, 0, 1));
    G1_GD(1, 1, 2, G1_RX(1, 0, 2), G1_RD(6, 1, 0, 1), G1_RX(1, 0, 3));
    G1_GD(2, 1, 3, G1_RD(7, 1, 1, 0), G1_RD(8, 1, 1, 1), NOP_);
    G1_GD(3, 1, 4, G1_RD(9, 1, 0, 2), G1_RD(0, 1, 0, 3), NOP_);
    G1_GD(4, 1, 5, G1_RD(1, 1, 1, 2), G1_RD(2, 1, 1, 3), NOP_);
    G1_GD(5, 0, 0, { G1_RX(1, 1, 0); G1_RX(1, 1, 1); }, { G1_RX(1, 1, 2); G1_RX(1, 1, 3); }, G1_RD(3, 1, 0, 4));
    G1_GD(6, 0, 1, G1_RD(4, 1, 0, 5), NOP_, NOP_);
    G1_GD(9, 0, 2, G1_RD(5, 1, 1, 4), NOP_, NOP_);
    G1_GD(7, 1, 0, G1_RD(6, 1, 1, 5), NOP_, NOP_);
    G1_GD(8, 1, 1, G1_RD(9, 1, 0, 6), NOP_, NOP_);
    G1_GD(0, 0, 3, G1_RD(7, 1, 0, 7), NOP_, NOP_);
    G1_GD(1, 1, 2, G1_RD(8, 1, 0, 8), NOP_, store_addr());
    G1_GD(2, 1, 3, G1_RD(0, 1, 0, 9), store_u(0, 0), store_u(0, 1));
    G1_GD(3, 0, 4, G1_RD(1, 1, 1, 6), store_u(0, 2), store_u(0, 3));
    G1_GD(4, 0, 5, G1_RD(2, 1, 1, 7), NOP_, NOP_);
    G1_GD(5, 1, 4, G1_RD(3, 1, 1, 8), G1_SY(1, 0), G1_SY(1, 1));
    G1_GD(6, 1, 5, G1_RD(4, 1, 1, 9), G1_SY(1, 2), G1_SY(1, 3));
    G1_GD(9, 0, 6, G1_RD(5, 0, 0, 6), NOP_, NOP_);
    G1_GD(7, 0, 7, G1_RD(6, 0, 0, 7), NOP_, G1_HA(0));
    G1_GD(8, 0, 8, G1_RD(9, 0, 0, 8), { G1_SY(2, 0); G1_HA(1); }, G1_SY(2, 1));
    G1_GD(0, 0, 9, G1_RD(7, 0, 0, 9), { G1_SY(2, 2); G1_HA(2); }, G1_SY(2, 3));
    G1_GD(1, 1, 6, { G1_RX(0, 0, 0); G1_RX(0, 0, 1); }, G1_RD(8, 0, 1, 6), G1_HA(3));
    G1_GD(2, 1, 7, { G1_RX(0, 0, 2); G1_RX(0, 0, 3); }, G1_RD(0, 0, 1, 7), G1_HA(4));
    G1_GD(3, 1, 8, G1_RD(1, 0, 1, 8), G1_HA(5), G1_HA(6));
    G1_GD(4, 1, 9, G1_RD(2, 0, 1, 9), G1_HA(7), NOP_);
    G1_GD(5, 0, 6, { G1_RX(0, 1, 0); G1_RX(0, 1, 1); }, { G1_RX(0, 1, 2); G1_RX(0, 1, 3); }, G1_HA(8));
    G1_GD(6, 0, 7, G1_HA(9), NOP_, NOP_);
    G1_GD(9, 0, 8, G1_HA(10), NOP_, NOP_);
    G1_GD(7, 0, 9, G1_HA(11), NOP_, NOP_);
    G1_WAITBAR();
    G1_GD(8, 1, 6, if (wid == 7) publish(ws, FLG_F2 + mt * 32 + j, tag, lane_id()), G1_HA(12), NOP_);
#define G1_PF(k) do { if (wc) { const unsigned vw_ = g2_vw(lane_id(), cg); _Pragma("unroll") for (int i = 4 * (k); i < 4 * (k) + 8; ++i) dma16<false>(mksrd(wd, F * D * 2), vw_, (unsigned)(28 * wr) * 65536u + i * 16384u, ldsb + P2_G2RING + wr * 16384u + i * 1024u); } } while (0)
    G1_GD(0, 1, 7, G1_HA(13), G1_PF(0), NOP_);
    G1_GD(1, 1, 8, G1_HA(14), G1_PF(2), NOP_);
    G1_GD(2, 1, 9, G1_HA(15), NOP_, NOP_);
  }
  asm volatile("s_nop 7" ::: "memory"); SBAR();
#pragma unroll
  for (int m = 0; m < 4; ++m)
#pragma unroll
    for (int n = 0; n < 4; ++n) accb[m][n] = acc[m][6 + n];
  STAMP(5);
}

DI void att_slow(const char* kv, const bf16x8 (&qf)[2][2], f32x16 (&o)[2], float (&linv)[2], int lane) {
  __builtin_amdgcn_fence(__ATOMIC_ACQUIRE, "agent");
#pragma unroll
  for (int m = 0; m < 2; ++m) {
    f32x16 oo = f32x16{}; float mref = -INFINITY, l = 0.f;
    for (int kt = 0; kt < 64; ++kt) {
      const char* p = kv + kt * 4096 + lane * 16;
      const bf16x8 k0 = *(const bf16x8*)p, k1 = *(const bf16x8*)(p + 1024), v0 = *(const bf16x8*)(p + 2048), v1 = *(const bf16x8*)(p + 3072);
      f32x16 s = MFMA32(k0, qf[m][0], f32x16{});
      s = MFMA32(k1, qf[m][1], s);
      float mx = s[0];
#pragma unroll
      for (int r = 1; r < 16; ++r) mx = fmaxf(mx, s[r]);
      mx = fmaxf(fmaxf(mx, __shfl_xor(mx, 32)), mref);
      const float f = EX2(mref - mx);
      mref = mx;
      float sum = 0.f;
#pragma unroll
      for (int r = 0; r < 16; ++r) { s[r] = EX2(s[r] - mref); sum += s[r]; oo[r] *= f; }
      l = l * f + sum;
      oo = MFMA32(v0, BF(pack8(s, 0, 1.f)), oo);
      oo = MFMA32(v1, BF(pack8(s, 1, 1.f)), oo);
    }
    o[m] = oo; linv[m] = 1.f / (l + __shfl_xor(l, 32));
  }
}
DI void lds_meet(unsigned v, unsigned need, unsigned addr) {
  unsigned left = SPIN_CAP;
  asm volatile("1:\n\tv_cmp_le_u32 vcc, %2, %0\n\ts_cmp_eq_u64 vcc, exec\n\ts_cbranch_scc1 2f\n\ts_sleep 1\n\tds_read_b32 %0, %3\n\ts_sub_u32 %1, %1, 1\n\ts_waitcnt lgkmcnt(0)\n\ts_cbranch_scc0 1b\n2:"
               : "+v"(v), "+s"(left) : "s"(need), "v"(addr) : "vcc", "scc", "memory");
}
DI void lds_meet_fast(unsigned v, unsigned need, unsigned addr) {
  unsigned left;
  asm volatile("v_cmp_le_u32 vcc, %2, %0\n\ts_cmp_eq_u64 vcc, exec\n\ts_cbranch_scc0 3f\n2:\n\t.subsection 1\n3:\n\ts_mov_b32 %1, 0x100000\n4:\n\ts_sleep 1\n\tds_read_b32 %0, %3\n\ts_waitcnt lgkmcnt(0)\n\t"
               "v_cmp_le_u32 vcc, %2, %0\n\ts_cmp_eq_u64 vcc, exec\n\ts_cbranch_scc1 2b\n\ts_sub_u32 %1, %1, 1\n\ts_cbranch_scc0 4b\n\ts_branch 2b\n\t.subsection 0"
               : "+v"(v), "=&s"(left) : "s"(need), "v"(addr) : "vcc", "scc", "memory");
}
DI int unit_owner(int a) { return a >= 32 ? a - 32 : (a < 8 ? 16 + a : 24 + (a - 8) / 3); }
DI void glu_pack(const f32x4 (&a)[4][4], u32x4 (&hp)[4]) {
#pragma unroll
  for (int m = 0; m < 4; ++m) {
#pragma unroll
    for (int k = 0; k < 4; ++k) { const f32x2 h = glu2(a[m][k >> 1][2 * (k & 1)], a[m][k >> 1][2 * (k & 1) + 1], a[m][2 + (k >> 1)][2 * (k & 1)], a[m][2 + (k >> 1)][2 * (k & 1) + 1]); hp[m][k] = cvtpk(h[0], h[1]); }
    PIN(hp[m]);
  }
}

DI unsigned att_dep(int head, int qb, int lane) {
  const int jq = unit_owner(head >> 1), jk = unit_owner(16 + (head >> 1)), jv = unit_owner(32 + (head >> 1));
  unsigned ikv = (lane & 7) * 32 + ((lane & 8) ? jk : jv); PIN(ikv);
  return FLG_F2 + (lane < 16 ? ikv : qb * 32 + jq);
}
DI void dma16x4_sc1(srd_t srd, unsigned voff, unsigned soff, unsigned m0v) {
  asm volatile("s_mov_b32 m0, %0\n\ts_nop 0\n\tbuffer_load_dwordx4 %1, %2, %3 offen sc1 lds\n\tbuffer_load_dwordx4 %1, %2, %3 offen offset:1024 sc1 lds\n\t"
               "buffer_load_dwordx4 %1, %2, %3 offen offset:2048 sc1 lds\n\tbuffer_load_dwordx4 %1, %2, %3 offen offset:3072 sc1 lds" ::"s"(m0v), "v"(voff), "s"(srd), "s"(soff) : "memory");
}
DI void att_role(const u16* wo, char* ws, LAS char* lds, int head, int qb, int tg, int cg, int wid, u64 tag, const u32x4 (&hp)[4], const u32x4 (&ha)[4]) {
  const int tz = lane_id();
  const int lane = tz & 63;
  const char* kvg = ws + WS_KV + (size_t)head * 262144;
  const srd_t skv = mksrd(kvg, 262144), sw = mksrd(ws, WS_END);
  const unsigned ldsb = (unsigned)(uintptr_t)lds;
  const unsigned voff = lane * 16u;
  volatile LAS unsigned* ctl = (volatile LAS unsigned*)(lds + LDS_CTL);
  const unsigned ctla = ldsb + LDS_CTL + (lane & 7) * 4u;
  {
#pragma unroll
    for (int i = 0; i < 4; ++i) { *(LAS u32x4*)(lds + 131072 + wid * 4096 + i * 1024 + lane * 16) = hp[i]; *(LAS u32x4*)(lds + 49152 + wid * 4096 + i * 1024 + lane * 16) = ha[i]; }
    asm volatile("s_waitcnt lgkmcnt(0)" ::: "memory");
    ctl[12 + wid] = 1u;
  }
#define AT_ARRIVE(k) ctl[wid] = (unsigned)((k) + 2)
#define AT_MEET(v, k) lds_meet(v, (unsigned)((k) + 2), ctla)
  f32x16 o[2] = {f32x16{}, f32x16{}};
  f32x4 ls[2] = {f32x4{}, f32x4{}};
  bf16x8 sel;
  { const short one = ((lane & 1) == ((lane >> 4) & 1) && (lane & 2) == 0) ? (short)0x3f80 : (short)0;
#pragma unroll
    for (int e = 0; e < 8; ++e) sel[e] = one; }
  bf16x8 kf[2][2], vf[2][2], qf[2][2];
  f32x16 sa, sb, negm[2];
  u32x4 pa0, pa1, pb0 = u32x4{}, pb1 = u32x4{};
  vf[1][0] = bf16x8{}; vf[1][1] = bf16x8{};
  PIN(sel); PIN(o[0]); PIN(o[1]); PIN(ls[0]); PIN(ls[1]); PIN(pb0); PIN(pb1); PIN(vf[1][0]); PIN(vf[1][1]);
  const unsigned kp = ldsb + lane * 16u;
  {
    asm volatile("s_waitcnt vmcnt(0)" ::: "memory");
    const u32x2 f = *(volatile LAS const u32x2*)(lds + LDS_DUMP + wid * 1024 + lane * 16);
    if (!__all(f[0] == (unsigned)tag && f[1] == (unsigned)(tag >> 32))) await(ws, att_dep(head, qb, lane), tag);
  }
  STAMP(6);
  dma16x4_sc1(skv, voff, wid * 4096u, ldsb + wid * 4096u);
  dma16x4_sc1(sw, voff, WS_Q + (head * 64 + qb * 8 + wid * 2) * 2048u, ldsb + 32768u + wid * 4096u);
  dma16x4_sc1(skv, voff, 16384u + wid * 4096u, ldsb + 16384u + wid * 4096u);
  asm volatile("s_waitcnt vmcnt(0)" ::: "memory");
  AT_ARRIVE(-1);
#pragma unroll
  for (int i = 0; i < 4; ++i) qf[i >> 1][i & 1] = lds128(LP(kp) + 32768 + wid * 4096 + i * 1024);
  asm volatile("" : "+v"(qf[0][0]), "+v"(qf[0][1]), "+v"(qf[1][0]), "+v"(qf[1][1]));
  lds_meet(ctl[lane & 3], 1u, ldsb + LDS_CTL + (lane & 3) * 4u);
  kf[0][0] = lds128(LP(kp)); kf[0][1] = lds128(LP(kp) + 1024);
  {
    sa = MFMA32(kf[0][0], qf[0][0], f32x16{}); sa = MFMA32(kf[0][1], qf[0][1], sa);
    sb = MFMA32(kf[0][0], qf[1][0], f32x16{}); sb = MFMA32(kf[0][1], qf[1][1], sb);
    dma16x4_sc1(skv, voff, 32768u + wid * 4096u, ldsb + 32768u + wid * 4096u);
    float ma = sa[0], mb = sb[0];
#pragma unroll
    for (int r = 1; r < 16; ++r) { ma = fmaxf(ma, sa[r]); mb = fmaxf(mb, sb[r]); }
    { const auto ra = __builtin_amdgcn_permlane32_swap(__float_as_uint(ma), __float_as_uint(ma), false, false), rb = __builtin_amdgcn_permlane32_swap(__float_as_uint(mb), __float_as_uint(mb), false, false);
      ma = fmaxf(__uint_as_float(ra[0]), __uint_as_float(ra[1])); mb = fmaxf(__uint_as_float(rb[0]), __uint_as_float(rb[1])); }
#pragma unroll
    for (int r = 0; r < 16; ++r) { negm[0][r] = -ma; negm[1][r] = -mb; sa[r] -= ma; }
  }
  PIN(negm[0]); PIN(negm[1]);
#define E1(S_, i) S_[i] = EX2(S_[i])
#define CV(P_, i, S_, r) P_[i] = cvtpk(S_[r], S_[(r) + 1])
#define AT_UNIT(SC, PC0, PC1, SN, kz, mn, PP0, PP1, vz, X0, X1, X2, X3)                                       \
  do {                                                                                                       \
    SN = MFMA32(kf[kz][0], qf[mn][0], negm[mn]); SBAR(); X0;                                                  \
    E1(SC, 0); E1(SC, 1); E1(SC, 2); E1(SC, 3); PIN(SC); SBAR();                                               \
    SN = MFMA32(kf[kz][1], qf[mn][1], SN); SBAR(); X1;                                                        \
    CV(PC0, 0, SC, 0); CV(PC0, 1, SC, 2); E1(SC, 4); E1(SC, 5); E1(SC, 6); PIN(SC); PIN(PC0); SBAR();           \
    o[mn] = MFMA32(vf[vz][0], BF(PP0), o[mn]); SBAR(); X2;                                                    \
    E1(SC, 7); CV(PC0, 2, SC, 4); E1(SC, 8); CV(PC0, 3, SC, 6); E1(SC, 9); PIN(SC); PIN(PC0); SBAR();           \
    ls[mn] = MFMA16(sel, BF(PP0), ls[mn]); SBAR();                                                            \
    E1(SC, 10); E1(SC, 11); PIN(SC); SBAR();                                                                  \
    o[mn] = MFMA32(vf[vz][1], BF(PP1), o[mn]); SBAR(); X3;                                                    \
    CV(PC1, 0, SC, 8); E1(SC, 12); CV(PC1, 1, SC, 10); E1(SC, 13); E1(SC, 14); E1(SC, 15); PIN(SC); PIN(PC1); SBAR(); \
    ls[mn] = MFMA16(sel, BF(PP1), ls[mn]); SBAR();                                                            \
    CV(PC1, 2, SC, 12); CV(PC1, 3, SC, 14); PIN(PC1); SBAR();                                                  \
  } while (0)
#define AT_KT(z, KADDR, VADDR, D0, D1)                                                                        \
  do {                                                                                                       \
    AT_UNIT(sa, pa0, pa1, sb, z, 1, pb0, pb1, z ^ 1, kf[z ^ 1][0] = lds128(KADDR), kf[z ^ 1][1] = lds128((KADDR) + 1024), vf[z][0] = lds128(VADDR), vf[z][1] = lds128((VADDR) + 1024)); \
    asm volatile("" : "+v"(kf[z ^ 1][0]), "+v"(kf[z ^ 1][1]), "+v"(vf[z][0]), "+v"(vf[z][1]));   \
    AT_UNIT(sb, pb0, pb1, sa, z ^ 1, 0, pa0, pa1, z, D0, NOP_, D1, NOP_);                                      \
  } while (0)
  const unsigned vwo = g2_vw(lane, cg), m0b = ldsb + wid * 4096u;
  unsigned arr = 2u, ctlw = ldsb + LDS_CTL + wid * 4u;
  PIN(arr); PIN(ctlw);
#define AT_INT(p, SRD, VO, SO, ST)                                                                            \
  do {                                                                                                       \
    unsigned pv;                                                                                             \
    AT_KT(0, LP(kp) + (p) * 16384 + 4096, LP(kp) + (p) * 16384 + 2048, NOP_, NOP_);                           \
    AT_KT(1, LP(kp) + (p) * 16384 + 8192, LP(kp) + (p) * 16384 + 6144, pv = ctl[lane & 7], NOP_);             \
    lds_meet_fast(pv, 4 * it + (p) + 1, ctla);     \
    SBAR();                                                                                                  \
    AT_KT(0, LP(kp) + (p) * 16384 + 12288, LP(kp) + (p) * 16384 + 10240, dma16<true>(SRD, VO, (SO), m0b + (((p) + 3) & 3) * 16384u), dma16<true>(SRD, VO, (SO) + (ST), m0b + (((p) + 3) & 3) * 16384u + 1024u)); \
    AT_KT(1, LP(kp) + (((p) + 1) & 3) * 16384, LP(kp) + (p) * 16384 + 14336, dma16<true>(SRD, VO, (SO) + 2 * (ST), m0b + (((p) + 3) & 3) * 16384u + 2048u), dma16<true>(SRD, VO, (SO) + 3 * (ST), m0b + (((p) + 3) & 3) * 16384u + 3072u)); \
    asm volatile("s_waitcnt vmcnt(4)\n\tds_write_b32 %1, %0\n\tv_add_u32 %0, 1, %0" : "+v"(arr) : "v"(ctlw) : "memory");     \
  } while (0)
  STAMP(7);
  for (int it = 0; it < 4; ++it) {
    const bool kvq = it < 3;
    const srd_t sq_ = mksrd(kvq ? (const void*)kvg : (const void*)wo, kvq ? 262144u : D * D * 2u);
    const unsigned sk_ = (4 * it + 3) * 16384u + wid * 4096u;
    const unsigned vq_ = kvq ? voff : vwo, so_ = kvq ? sk_ + 16384u : (8 * wid + 5) * 65536u, st_ = kvq ? 1024u : 16384u, sn_ = kvq ? 16384u : 65536u;
    AT_INT(0, skv, voff, sk_, 1024u);
    AT_INT(1, sq_, vq_, so_, st_);
    AT_INT(2, sq_, vq_, so_ + sn_, st_);
    AT_INT(3, sq_, vq_, so_ + 2 * sn_, st_);
  }
  STAMP(8);
  o[1] = MFMA32(vf[1][0], BF(pb0), o[1]); ls[1] = MFMA16(sel, BF(pb0), ls[1]);
  o[1] = MFMA32(vf[1][1], BF(pb1), o[1]); ls[1] = MFMA16(sel, BF(pb1), ls[1]);
  auto store_o = [&](int m, float inv) {
    const unsigned T = qb * 8 + wid * 2 + m;
    const unsigned so = WS_X2 + ((T >> 2) * NKB + 112 + head) * 8192u + (T & 3) * 512u, vo = (lane >> 5) * 2048u + (lane & 31) * 16u;
    st16_sc1(sw, vo, so, pack8(o[m], 0, inv));
    st16_sc1(sw, vo, so + 4096u, pack8(o[m], 1, inv));
  };
  float lt[2];
#pragma unroll
  for (int m = 0; m < 2; ++m) { lt[m] = (lane & 16) ? ls[m][1] : ls[m][0]; store_o(m, __builtin_amdgcn_rcpf(lt[m])); }
  bool bad = false;
#pragma unroll
  for (int m = 0; m < 2; ++m) {
    float chk = lt[m], nanp = lt[m];
#pragma unroll
    for (int r = 0; r < 16; ++r) { chk = fmaxf(chk, fabsf(o[m][r])); nanp += o[m][r]; }
    bad = bad || !(chk < 1e30f) || (nanp != nanp);
  }
  if (__builtin_expect(__any(bad), 0)) {
    float linv[2];
    asm volatile("s_waitcnt vmcnt(0)" ::: "memory");
    att_slow(kvg, qf, o, linv, lane);
    store_o(0, linv[0]); store_o(1, linv[1]);
  }
  asm volatile("s_waitcnt vmcnt(0)" ::: "memory");
  publish_last(ws, ldsb + LDS_CTL + 17 * 4u, 4u, FLG_F4 + qb * 32 + head, tag, lane);
  STAMP(9);
  await(ws, FLG_F4 + (tg >> 1) * 32 + 8 * wid + (lane & 7), tag);
  { unsigned pv = 0; AT_MEET(pv, 15); }
  ctl[8 + wid] = 1u;
  STAMP(10);
}

constexpr int g2_cnt(int m, bool x0, bool w0, bool x1, bool w1, bool w2) {
  return (7 - m) + (w2 ? (8 - m) >> 1 : 0) + (x1 ? 8 : 0) + (w1 ? 4 : 0) + (x0 ? m : 0) + (w0 ? (m + 1) >> 1 : 0);
}
#undef STW
#define STW (kq + 4)
DI void g2_role(const u16* x, u32x4 (&xr)[2], const u16* wo, const u16* wd, char* ws, LAS char* lds, int tg, int cg, int kq, u64 tag, const u32x4 (&hp)[4], const u32x4 (&ha)[4], int mt, int j, srd_t scode) {
  const int tz = lane_id();
  const int lane = tz & 63;
  const srd_t sx2 = mksrd(ws + WS_X2 + (size_t)tg * NKB * 8192, NKB * 8192);
  const srd_t swd = mksrd(wd, F * D * 2), swo = mksrd(wo, D * D * 2);
  const unsigned ldsw = (unsigned)(uintptr_t)lds + P2_G2RING + kq * 16384u;
  const unsigned vw = g2_vw(lane, cg);
  const int t16 = lane & 15, g = lane >> 4, q4 = t16 >> 2, p4 = lane & 3;
  const unsigned vx = g * 2048u + t16 * 16u;
  volatile LAS unsigned* ctl = (volatile LAS unsigned*)(lds + LDS_CTL);
  unsigned wa[4];
  f32x4 acc[8][4];
  {
    u32x4 hq[8];
    const srd_t sw = mksrd(ws, WS_END);
    const unsigned cb = (unsigned)(uintptr_t)lds + LDS_CTL;
    lds_meet(0u, 1u, cb + (12 + kq) * 4u);
#pragma unroll
    for (int i = 0; i < 8; ++i) hq[i] = *(LAS const u32x4*)(lds + (i < 4 ? 131072 : 49152 - 4096) + kq * 4096 + i * 1024 + lane * 16);
#pragma unroll
    for (int i = 0; i < 8; ++i) PIN(hq[i]);
    ctl[4 + kq] = ~0u;
#pragma unroll
    for (int c = 0; c < 4; ++c) { wa[c] = ldsw + (4 * g + q4) * 128 + ((c ^ ((2 * g + (q4 >> 1)) & 3)) << 5) + p4 * 8; PIN(wa[c]); }
#pragma unroll
    for (int m = 0; m < 8; ++m)
#pragma unroll
      for (int c = 0; c < 4; ++c) { acc[m][c] = f32x4{}; PIN(acc[m][c]); }
    lds_meet(0u, 1u, cb + (lane & 3) * 4u);
    const int pair = j < 24 ? 2 * j + 1 : 24 + j;
#pragma unroll
    for (int m = 0; m < 4; ++m) { st16_sc1(sw, vx, h_base(mt, pair, kq, 1) + m * 256u, hp[m]); st16_sc1(sw, vx, h_base(mt, pair, kq, 0) + m * 256u, hq[m]); }
    if (j < 24) {
#pragma unroll
      for (int m = 0; m < 4; ++m) { st16_sc1(sw, vx, h_base(mt, 2 * j, kq, 1) + m * 256u, ha[m]); st16_sc1(sw, vx, h_base(mt, 2 * j, kq, 0) + m * 256u, hq[4 + m]); }
    }
    asm volatile("s_waitcnt vmcnt(0)" ::: "memory");
    publish_last(ws, cb + 16 * 4u, 4u, FLG_F3 + mt * 32 + j, tag, lane);
  }
  if (blockIdx.x == 0 && kq == 0) {
    for (int i = 0; i < 4; ++i) await(ws, FLG_F1 + i * 64 + lane, tag);
    publish(ws, 0, tag, lane);
  }
  await(ws, FLG_F3 + (tg >> 1) * 32 + 7 * kq + (lane < (kq == 3 ? 11 : 7) ? lane : 0), tag);
  STAMP(6);
  bf16x8 xf[2][8];
  unsigned xo = (unsigned)(28 * kq) * 8192u, wo_ = (unsigned)(28 * kq + 4) * 65536u;
#pragma unroll
  for (int z = 0; z < 2; ++z) {
#pragma unroll
    for (int m = 0; m < 8; ++m) ld16<true>(xf[z][m], sx2, vx, xo, m * 256);
    xo += 8192u; }
  s16x4 wl[2][4], wh[2][4];
#define G2_RW(z, off, c) do { LAS const char* p_ = LP(wa[c]) + (off); wl[z][c] = ldstr(p_); wh[z][c] = ldstr(p_ + 2048); } while (0)
#pragma unroll
  for (int c = 0; c < 4; ++c) G2_RW(0, 0, c);
  SBAR();
#define G2_KB(sl, WS_, RD, X0, W0, X1, W1, W2) G2_KBX(sl, WS_, RD, (((sl) + 1) & 3) * 4096, X0, W0, X1, W1, W2, 0, NOP_)
#define G2_KBX(sl, WS_, RD, ROFF, X0, W0, X1, W1, W2, EXTRA, HOOK)                                                                \
  do {                                                                                                       \
    constexpr int z = (sl) & 1;                                                                              \
    _Pragma("unroll") for (int m = 0; m < 8; ++m) {                                                          \
      switch (m) {                                                                                           \
        case 0: mfma16w<g2_cnt(0, X0, W0, X1, W1, W2) + (EXTRA)>(acc[m][0], cat8(wl[z][0], wh[z][0]), xf[z][m]); break; \
        case 1: mfma16w<g2_cnt(1, X0, W0, X1, W1, W2) + (EXTRA)>(acc[m][0], cat8(wl[z][0], wh[z][0]), xf[z][m]); break; \
        case 2: mfma16w<g2_cnt(2, X0, W0, X1, W1, W2) + (EXTRA)>(acc[m][0], cat8(wl[z][0], wh[z][0]), xf[z][m]); break; \
        case 3: mfma16w<g2_cnt(3, X0, W0, X1, W1, W2) + (EXTRA)>(acc[m][0], cat8(wl[z][0], wh[z][0]), xf[z][m]); break; \
        case 4: mfma16w<g2_cnt(4, X0, W0, X1, W1, W2) + (EXTRA)>(acc[m][0], cat8(wl[z][0], wh[z][0]), xf[z][m]); break; \
        case 5: mfma16w<g2_cnt(5, X0, W0, X1, W1, W2) + (EXTRA)>(acc[m][0], cat8(wl[z][0], wh[z][0]), xf[z][m]); break; \
        case 6: mfma16w<g2_cnt(6, X0, W0, X1, W1, W2) + (EXTRA)>(acc[m][0], cat8(wl[z][0], wh[z][0]), xf[z][m]); break; \
        default: mfma16w<g2_cnt(7, X0, W0, X1, W1, W2) + (EXTRA)>(acc[m][0], cat8(wl[z][0], wh[z][0]), xf[z][m]); break; \
      }                                                                                                      \
      SBAR();                                                                                                \
      if (RD && m >= 2 && m < 6) G2_RW(z ^ 1, ROFF, m - 2);                                \
      SBAR();                                                                                                \
      mfma16a(acc[m][1], cat8(wl[z][1], wh[z][1]), xf[z][m]);                                                 \
      mfma16a(acc[m][2], cat8(wl[z][2], wh[z][2]), xf[z][m]);                                                 \
      mfma16a(acc[m][3], cat8(wl[z][3], wh[z][3]), xf[z][m]); SBAR();                                         \
      HOOK;                                                                                                  \
      if (X0) ld16_inplace(xf[z][m], sx2, vx, xo, m * 256);                                                  \
      if (W0 && !(m & 1)) dma16<false>(WS_, vw, wo_ + (m >> 1) * 16384u, ldsw + (sl) * 4096u + (m >> 1) * 1024u); \
      SBAR();                                                                                                \
    }                                                                                                        \
    xo += 8192u; wo_ += 65536u;                                                                              \
  } while (0)
#define YES true
#define NO false
  STAMP(7);
  G2_KB(0, swd, YES, YES, YES, YES, NO, NO); G2_KB(1, swd, YES, YES, YES, YES, YES, NO); G2_KB(2, swd, YES, YES, YES, YES, YES, YES); G2_KB(3, swd, YES, YES, YES, YES, YES, YES);
  for (int i = 4; i < 24; i += 4) {
    dma16<false>(scode, vx, (unsigned)((mt * 8192) + ((j >> 3) & 1) * 1024 + kq * 256), (unsigned)(uintptr_t)lds + LDS_DUMP + (4 + kq) * 1024u);
    G2_KB(0, swd, YES, YES, YES, YES, YES, YES); G2_KB(1, swd, YES, YES, YES, YES, YES, YES); G2_KB(2, swd, YES, YES, YES, YES, YES, YES); G2_KB(3, swd, YES, YES, YES, YES, YES, YES);
  }
  wo_ = (unsigned)(8 * kq) * 65536u;
  G2_KB(0, swo, YES, YES, YES, YES, YES, YES); G2_KB(1, swo, YES, YES, YES, YES, YES, YES);
  G2_KB(2, swo, YES, NO, YES, YES, YES, YES); G2_KB(3, swo, YES, NO, YES, NO, YES, YES);
  asm volatile("" : "+v"(wl[0][0]), "+v"(wh[0][0]), "+v"(wl[0][1]), "+v"(wh[0][1]), "+v"(wl[0][2]), "+v"(wh[0][2]), "+v"(wl[0][3]), "+v"(wh[0][3]));
#pragma unroll
  for (int p = 0; p < 4; ++p) dma16<false>(swo, vw, wo_ + p * 16384u, ldsw + p * 1024u);
  STAMP(8);
  lds_meet(0u, 1u, (unsigned)(uintptr_t)lds + LDS_CTL + (8 + kq) * 4u);
  STAMP(9);
  xo = (unsigned)(112 + 8 * kq) * 8192u;
#pragma unroll
  for (int z = 0; z < 2; ++z) {
#pragma unroll
    for (int m = 0; m < 8; ++m) ld16_inplace(xf[z][m], sx2, vx, xo, m * 256);
    xo += 8192u; }
  G2_KB(0, swo, YES, YES, NO, YES, NO, NO); G2_KB(1, swo, YES, YES, NO, YES, NO, NO); G2_KB(2, swo, YES, YES, NO, YES, NO, NO); G2_KB(3, swo, YES, YES, NO, YES, NO, NO);
#pragma unroll
  for (int c = 0; c < 4; ++c) wa[c] -= P2_G2RING + kq * 12288u;
  G2_KBX(0, swo, YES, 0, YES, NO, YES, NO, NO, 0, NOP_); G2_KBX(1, swo, YES, 16384, YES, NO, YES, NO, NO, 0, NOP_);
  G2_KBX(2, swo, YES, 32768, NO, NO, YES, NO, NO, 0, NOP_);
  const int ty = lane_id();
  { const srd_t sxi = mksrd(x, S * D * 2);
    const unsigned xv = (unsigned)((ty & 63) >> 3) * 2048u + (ty & 7) * 16u, xs = (unsigned)(tg * 128 + (4 + kq) * 16) * 2048u + cg * 128u;
    ld16<false>(xr[0], sxi, xv, xs, 0); ld16<false>(xr[1], sxi, xv, xs + 16384u, 0); }
  unsigned pa[4];
#pragma unroll
  for (int c = 0; c < 4; ++c) pa[c] = (unsigned)(uintptr_t)lds + kq * 4096u + (ty & 15) * 256u + (((4 * c + ((ty >> 4) & 3)) ^ (ty & 15)) << 4);
  const unsigned pown = P2_G2RING + kq * 12288u;
#define G2_PUT(mm) do { _Pragma("unroll") for (int c = 0; c < 4; ++c) *(LAS f32x4*)((LAS char*)(uintptr_t)((mm) < 4 ? pa[c] : pa[c] + pown) + ((mm) < 4 ? (mm) * 16384 : ((mm) - 4) * 4096)) = acc[mm][c]; } while (0)
  G2_KBX(3, swo, NO, 0, NO, NO, NO, NO, NO, 2, if (m >= 2) G2_PUT(m >= 2 ? m - 2 : 0));
  asm volatile("s_nop 7" ::: "memory");
  G2_PUT(6); G2_PUT(7);
  asm volatile("s_waitcnt vmcnt(0)" : "+v"(xr[0]), "+v"(xr[1]));
  STAMP(10);
}

#undef STW
#define STW wid
__global__ __launch_bounds__(512, 2) void fused_kernel(const u16* x, const u16* wqkv, const u16* wo, const u16* wg, const u16* wu, const u16* wd, char* ws, u16* out) {
  extern __shared__ __attribute__((aligned(16))) char smem[];
  LAS char* lds = (LAS char*)smem;
  const int b = blockIdx.x, xcd = b & 7, r = b >> 3, wid = rfl(threadIdx.x >> 6);
  u64 tag = *(const __attribute__((address_space(4))) u64*)(ws + WS_FLG);
  ((LAS unsigned*)(lds + LDS_CTL))[threadIdx.x & 31] = 0u;
  const int tg = (xcd >> 1) * 4 + (r & 3), cg = (xcd & 1) * 8 + (r >> 2);
  const int mt = r & 7, j = xcd + 8 * (r >> 3);
  srd_t scode;
  { u64 pc; unsigned len;
    asm volatile("s_getpc_b64 %0\n1:\n\ts_mov_b32 %1, .Lfk_code_end-1b" : "=s"(pc), "=s"(len));
    scode = mksrd((const void*)(pc & ~127ull), len); }
  STAMP(0);
  dma16<false>(scode, (threadIdx.x & 63) * 16u, (unsigned)((r * 8 + wid) & 127) * 1024u, (unsigned)(uintptr_t)lds + LDS_DUMP + wid * 1024u);
  ln_row(x, ws, mt * 256 + j * 8 + wid);
  asm volatile("s_waitcnt vmcnt(0)" : "+s"(tag)::"memory");
  tag = tag * 6364136223846793005ull + 1442695040888963407ull;
  STAMP(1);
  asm volatile("s_barrier" ::: "memory");
  if (wid == 0) publish(ws, FLG_F1 + mt * 32 + j, tag, lane_id());
  f32x4 accb[4][4];
  u32x4 ha[4];
  if (j < 16) g1_body<0>(wd, cg, wqkv, wg, wu, ws, lds, wid, mt, j, scode, tag, accb, ha);
  else if (j < 24) g1_body<1>(wd, cg, wqkv, wg, wu, ws, lds, wid, mt, j, scode, tag, accb, ha);
  else g1_body<2>(wd, cg, wqkv, wg, wu, ws, lds, wid, mt, j, scode, tag, accb, ha);
  const int head = xcd * 4 + (r >> 3);
  if (wid < 4) { __builtin_amdgcn_s_setprio(1); dma16<true>(mksrd(ws, WS_XN), att_dep(head, mt, lane_id()) * (8u * FST), 0u, (unsigned)(uintptr_t)lds + LDS_DUMP + wid * 1024u); }
  u32x4 hp[4];
  glu_pack(accb, hp);
  u32x4 xr[2];
  if (wid >= 4) {
    g2_role(x, xr, wo, wd, ws, lds, tg, cg, wid - 4, tag, hp, ha, mt, j, scode);
  } else {
    att_role(wo, ws, lds, head, mt, tg, cg, wid, tag, hp, ha);
    const int ty = lane_id();
#pragma unroll
    for (int e = 0; e < 2; ++e) xr[e] = *(const u32x4*)(x + (size_t)(tg * 128 + wid * 16 + e * 8 + ((ty & 63) >> 3)) * D + cg * 64 + (ty & 7) * 8);
  }
  const int ln = lane_id();
  unsigned ra[4][4], vo = (unsigned)((tg * 128 + wid * 16 + (ln >> 3)) * D + cg * 64 + (ln & 7) * 8) * 2u;
  f32x4 sum[4];
#pragma unroll
  for (int i = 0; i < 4; ++i) {
    const unsigned tk = (i >> 1) * 8 + (ln >> 3);
#pragma unroll
    for (int k = 0; k < 4; ++k) { ra[i][k] = (unsigned)(uintptr_t)lds + (wid < 4 ? wid * 16384 + k * 4096 : P2_G2RING + (wid - 4) * 4096 + k * 16384) + tk * 256 + (((2 * (ln & 7) + (i & 1)) ^ tk) << 4); PIN(ra[i][k]); }
    sum[i] = f32x4{blo(xr[i >> 1][2 * (i & 1)]), bhi(xr[i >> 1][2 * (i & 1)]), blo(xr[i >> 1][2 * (i & 1) + 1]), bhi(xr[i >> 1][2 * (i & 1) + 1])};
    PIN(sum[i]);
  }
  PIN(vo);
  asm volatile("s_waitcnt lgkmcnt(0)\n\ts_barrier" ::: "memory");
  STAMP(11);
  f32x4 part[4][4];
#pragma unroll
  for (int i = 0; i < 4; ++i)
#pragma unroll
    for (int k = 0; k < 4; ++k) part[i][k] = *(LAS const f32x4*)LP(ra[i][k]);
  SBAR();
#pragma unroll
  for (int i = 0; i < 4; ++i) sum[i] += (part[i][0] + part[i][1]) + (part[i][2] + part[i][3]);
  st16_sc1(mksrd(out, S * D * 2), vo, 0u, pack44(sum[0], sum[1], 1.f));
  st16_sc1(mksrd(out, S * D * 2), vo, 8u * D * 2u, pack44(sum[2], sum[3], 1.f));
  STAMP(12);
  asm volatile(".Lfk_code_end:");
}

extern "C" void kernel_launch(void* const* d_in, const int* in_sizes, int n_in, void* d_out, int out_size, void* d_ws, size_t ws_size, hipStream_t stream) {
  (void)hipFuncSetAttribute((const void*)fused_kernel, hipFuncAttributeMaxDynamicSharedMemorySize, LDS_BYTES);
  fused_kernel<<<256, 512, LDS_BYTES, stream>>>((const u16*)d_in[0], (const u16*)d_in[1], (const u16*)d_in[2], (const u16*)d_in[4], (const u16*)d_in[5], (const u16*)d_in[6], (char*)d_ws, (u16*)d_out);
}
```

```cpp
#include <hip/hip_runtime.h>
#include <hip/hip_bf16.h>
#include <stdint.h>

typedef unsigned short u16;
typedef unsigned long long u64;
typedef short bf16x8 __attribute__((ext_vector_type(8)));
typedef short s16x4 __attribute__((ext_vector_type(4)));
typedef float f32x16 __attribute__((ext_vector_type(16)));
typedef float f32x4 __attribute__((ext_vector_type(4)));
typedef float f32x2 __attribute__((ext_vector_type(2)));
typedef unsigned u32x4 __attribute__((ext_vector_type(4)));
typedef unsigned u32x2 __attribute__((ext_vector_type(2)));
typedef __bf16 bf16x2_t __attribute__((ext_vector_type(2)));
typedef short v4i16_t __attribute__((ext_vector_type(4)));
typedef __amdgpu_buffer_rsrc_t srd_t;
typedef __attribute__((address_space(1))) u64 gu64;
#define LAS __attribute__((address_space(3)))
#define DI __device__ __forceinline__
#define SBAR() __builtin_amdgcn_sched_barrier(0)
#define PIN(x) asm volatile("" : "+v"(x))
#define NOP_ ((void)0)
#define LP(a) ((LAS const char*)(uintptr_t)(a))
#define MFMA32(a, b, c) __builtin_amdgcn_mfma_f32_32x32x16_bf16((a), (b), (c), 0, 0, 0)
#define MFMA16(a, b, c) __builtin_amdgcn_mfma_f32_16x16x32_bf16((a), (b), (c), 0, 0, 0)
#define EX2(v) __builtin_amdgcn_exp2f(v)
#define BF(x) __builtin_bit_cast(bf16x8, x)
#define RLX_AGENT __ATOMIC_RELAXED, __HIP_MEMORY_SCOPE_AGENT

constexpr int S = 2048, D = 1024, F = 3584;
constexpr int NKB = (F + D) / 32;
constexpr float LOG2E = 1.4426950408889634f;
constexpr float QSCALE = 0.17677669529663687f * LOG2E;

constexpr unsigned WS_FLG = 0;
constexpr unsigned FLG_F1 = 256, FLG_F2 = FLG_F1 + 2048, FLG_F3 = FLG_F2 + 2048, FLG_F4 = FLG_F3 + 2048;
constexpr unsigned FST = 16;
constexpr unsigned WS_XN = 2097152;
constexpr unsigned WS_Q = WS_XN + S * D * 2;
constexpr unsigned WS_KV = WS_Q + S * D * 2;
constexpr unsigned WS_X2 = WS_KV + S * D * 4;
constexpr unsigned WS_END = WS_X2 + S * (F + D) * 2;
constexpr int G1_XS = 32768, G1_WB = 65536, G1_WS = 40960;
constexpr int P2_G2RING = 65536;
constexpr int LDS_CTL = G1_WB + 2 * G1_WS, LDS_DUMP = LDS_CTL + 1024, LDS_BYTES = LDS_DUMP + 8192;
constexpr unsigned SPIN_CAP = 1u << 20;

DI unsigned rfl(unsigned x) { return __builtin_amdgcn_readfirstlane(x); }
DI int lane_id() { int l; asm volatile("v_mbcnt_lo_u32_b32 %0, -1, 0\n\tv_mbcnt_hi_u32_b32 %0, -1, %0" : "=v"(l)); return l; }
DI unsigned cvtpk(float lo, float hi) { f32x2 v = {lo, hi}; bf16x2_t b = __builtin_convertvector(v, bf16x2_t); return __builtin_bit_cast(unsigned, b); }
DI float rbf(float f) { return __uint_as_float(cvtpk(f, f) << 16); }
DI float blo(unsigned u) { return __uint_as_float(u << 16); }
DI float bhi(unsigned u) { return __uint_as_float(u & 0xffff0000u); }
DI srd_t mksrd(const void* p, unsigned bytes) { return __builtin_amdgcn_make_buffer_rsrc((void*)p, 0, bytes, 0x00020000); }
template <bool SC1> DI void dma16(srd_t srd, unsigned voff, unsigned soff, unsigned m0v) {
  if (SC1) asm volatile("s_mov_b32 m0, %0\n\ts_nop 0\n\tbuffer_load_dwordx4 %1, %2, %3 offen sc1 lds" ::"s"(m0v), "v"(voff), "s"(srd), "s"(soff) : "memory");
  else asm volatile("s_mov_b32 m0, %0\n\ts_nop 0\n\tbuffer_load_dwordx4 %1, %2, %3 offen lds" ::"s"(m0v), "v"(voff), "s"(srd), "s"(soff) : "memory");
}
template <bool SC1, class T> DI void ld16(T& d, srd_t srd, unsigned voff, unsigned soff, int imm) {
  if (SC1) asm volatile("buffer_load_dwordx4 %0, %1, %2, %3 offen offset:%4 sc1" : "=v"(d) : "v"(voff), "s"(srd), "s"(soff), "i"(imm) : "memory");
  else asm volatile("buffer_load_dwordx4 %0, %1, %2, %3 offen offset:%4" : "=v"(d) : "v"(voff), "s"(srd), "s"(soff), "i"(imm) : "memory");
}
template <class T> DI void ld16_inplace(T& d, srd_t srd, unsigned voff, unsigned soff, int imm) {
  asm volatile("buffer_load_dwordx4 %0, %1, %2, %3 offen offset:%4 sc1" : "+v"(d) : "v"(voff), "s"(srd), "s"(soff), "i"(imm) : "memory");
}
DI void st16_sc1(srd_t srd, unsigned voff, unsigned soff, u32x4 v) {
  asm volatile("buffer_store_dwordx4 %0, %1, %2, %3 offen sc1\n\ts_nop 1" ::"v"(v), "v"(voff), "s"(srd), "s"(soff) : "memory");
}
DI s16x4 ldstr(LAS const char* p) { return __builtin_bit_cast(s16x4, __builtin_amdgcn_ds_read_tr16_b64_v4i16((LAS v4i16_t*)p)); }
DI bf16x8 lds128(LAS const char* p) { return *(LAS const bf16x8*)p; }
DI bf16x8 cat8(s16x4 lo, s16x4 hi) { return __builtin_shufflevector(lo, hi, 0, 1, 2, 3, 4, 5, 6, 7); }
DI u32x4 pack8(const f32x16& v, int s, float sc) {
  u32x4 w;
  w[0] = cvtpk(v[8 * s + 0] * sc, v[8 * s + 1] * sc); w[1] = cvtpk(v[8 * s + 2] * sc, v[8 * s + 3] * sc);
  w[2] = cvtpk(v[8 * s + 4] * sc, v[8 * s + 5] * sc); w[3] = cvtpk(v[8 * s + 6] * sc, v[8 * s + 7] * sc);
  return w;
}
DI u32x4 pack44(const f32x4& a, const f32x4& b, float sc) {
  u32x4 w; w[0] = cvtpk(a[0] * sc, a[1] * sc); w[1] = cvtpk(a[2] * sc, a[3] * sc); w[2] = cvtpk(b[0] * sc, b[1] * sc); w[3] = cvtpk(b[2] * sc, b[3] * sc); return w;
}
DI void mfma16a(f32x4& c, const bf16x8& a, const bf16x8& b) { asm volatile("v_mfma_f32_16x16x32_bf16 %0, %1, %2, %0" : "+v"(c) : "v"(a), "v"(b)); }
template <int N> DI void mfma16w(f32x4& c, const bf16x8& a, const bf16x8& b) { asm volatile("s_waitcnt vmcnt(%3)\n\tv_mfma_f32_16x16x32_bf16 %0, %1, %2, %0" : "+v"(c) : "v"(a), "v"(b), "i"(N)); }
template <int CTRL, int RM> DI float dppadd(float v) { return v + __builtin_bit_cast(float, __builtin_amdgcn_update_dpp(0, __builtin_bit_cast(int, v), CTRL, RM, 0xf, true)); }
DI float wave_sum(float v) {
  v = dppadd<0x111, 0xf>(v); v = dppadd<0x112, 0xf>(v); v = dppadd<0x114, 0xf>(v); v = dppadd<0x118, 0xf>(v);
  v = dppadd<0x142, 0xa>(v); v = dppadd<0x143, 0xc>(v);
  return __builtin_bit_cast(float, __builtin_amdgcn_readlane(__builtin_bit_cast(int, v), 63));
}
DI void publish(char* ws, unsigned idx, u64 tag, int lane) {
  const u32x2 tv = {(unsigned)tag, (unsigned)(tag >> 32)};
  asm volatile("buffer_store_dwordx2 %0, %1, %2, %3 offen sc1" ::"v"(tv), "v"(lane == 0 ? 0u : 0x7ff00000u), "s"(mksrd(ws + WS_FLG, WS_XN)), "s"(idx * (8u * FST)) : "memory");
}
DI void publish_last(char* ws, unsigned cnt_addr, unsigned n, unsigned idx, u64 tag, int lane) {
  unsigned old;
  asm volatile("ds_add_rtn_u32 %0, %1, %2\n\ts_waitcnt lgkmcnt(0)" : "=v"(old) : "v"(cnt_addr), "v"(lane == 0 ? 1u : 0u) : "memory");
  if (rfl(old) == n - 1) publish(ws, idx, tag, lane);
}
DI void await(char* ws, unsigned idx, u64 tag) {
  gu64* p = (gu64*)(ws + WS_FLG) + idx * FST;
  for (unsigned spins = 0; spins < SPIN_CAP; ++spins) {
    if (__all(__hip_atomic_load(p, RLX_AGENT) == tag)) break;
    __builtin_amdgcn_s_sleep(1);
  }
  asm volatile("" ::: "memory");
}

DI unsigned g2_vw(int lane, int cg) { const unsigned l3 = lane >> 3, l7 = lane & 7; return l3 * 2048u + ((l7 ^ (((l3 >> 1) & 3) << 1)) << 4) + cg * 128u; }

DI void ln_row(const u16* x, char* ws, int row, srd_t scode, unsigned code_off, unsigned dump) {
  const int lane = lane_id();
  const srd_t sxi = mksrd(x, S * D * 2), sx = mksrd(ws + WS_XN, S * D * 2);
  u32x4 a, b;
  ld16<false>(a, sxi, lane * 16u, row * 2048u, 0); ld16<false>(b, sxi, lane * 16u, row * 2048u, 1024);
  dma16<false>(scode, lane * 16u, code_off, dump);
  asm volatile("s_waitcnt vmcnt(1)" : "+v"(a), "+v"(b));
  f32x2 v[8];
#pragma unroll
  for (int i = 0; i < 4; ++i) { v[i] = f32x2{blo(a[i]), bhi(a[i])}; v[4 + i] = f32x2{blo(b[i]), bhi(b[i])}; }
  const f32x2 s2 = ((v[0] + v[1]) + (v[2] + v[3])) + ((v[4] + v[5]) + (v[6] + v[7]));
  const float mean = wave_sum(s2[0] + s2[1]) * (1.f / D);
  f32x2 q[4];
#pragma unroll
  for (int i = 0; i < 4; ++i) { const f32x2 d0 = v[i] - mean, d1 = v[4 + i] - mean; q[i] = __builtin_elementwise_fma(d1, d1, d0 * d0); }
  const f32x2 q2 = (q[0] + q[1]) + (q[2] + q[3]);
  const float mb = rbf(mean);
  unsigned dn[8];
#pragma unroll
  for (int i = 0; i < 8; ++i) { const f32x2 d = v[i] - mb; dn[i] = cvtpk(d[0], d[1]); }
  const float vb = rbf(wave_sum(q2[0] + q2[1]) * (1.f / D));
  const float inv = __builtin_amdgcn_rcpf(rbf(__builtin_amdgcn_sqrtf(rbf(vb + 1e-5f))));
  u32x4 oa, ob;
#pragma unroll
  for (int i = 0; i < 4; ++i) { oa[i] = cvtpk(blo(dn[i]) * inv, bhi(dn[i]) * inv); ob[i] = cvtpk(blo(dn[4 + i]) * inv, bhi(dn[4 + i]) * inv); }
  st16_sc1(sx, lane * 16u, row * 2048u, oa); st16_sc1(sx, lane * 16u + 1024u, row * 2048u, ob);
}

DI f32x2 glu2(float g0, float g1, float u0, float u1) { return f32x2{u0 * __builtin_amdgcn_rcpf(1.f + EX2(g0 * -LOG2E)), u1 * __builtin_amdgcn_rcpf(1.f + EX2(g1 * -LOG2E))}; }
DI unsigned h_base(int mt, int pair, int wr, int wc) { return WS_X2 + ((mt * 2 + (wr >> 1)) * NKB + 2 * pair + wc) * 8192u + (wr & 1) * 1024u; }
DI int unit_owner(int a) { return a >= 32 ? a - 32 : (a < 8 ? 16 + a : 24 + (a - 8) / 3); }
DI unsigned att_dep(int head, int qb, int lane) {
  const int jq = unit_owner(head >> 1), jk = unit_owner(16 + (head >> 1)), jv = unit_owner(32 + (head >> 1));
  unsigned ikv = (lane & 7) * 32 + ((lane & 8) ? jk : jv); PIN(ikv);
  return FLG_F2 + (lane < 16 ? ikv : qb * 32 + jq);
}
template <int KIND>
DI void g1_body(int head, const u16* wd, int cg, const u16* wqkv, const u16* wg, const u16* wu, char* ws, LAS char* lds, int wid, int mt, int j, srd_t scode, u64 tag, f32x4 (&accb)[4][4], u32x4 (&ha)[4]) {
  const int lane = lane_id(), wr = wid & 3, wc = wid >> 2;
  constexpr bool SWAP0 = KIND == 0, isY = KIND == 2;
  const int a0 = KIND == 0 ? 32 + j : (KIND == 1 ? j - 16 : 3 * j - 64);
  const srd_t sx = mksrd(ws + WS_XN, S * D * 2), sq = mksrd(wqkv, D * 3 * D * 2), sg = mksrd(wg, D * F * 2), su = mksrd(wu, D * F * 2);
  const srd_t sa = isY ? sq : sg, sb = isY ? sq : su;
  const unsigned ld1 = isY ? 6144u : 7168u;
  const unsigned c0 = a0 * 128u, c1 = isY ? c0 + 128u : j * 256u, c2 = isY ? c0 + 256u : j * 256u, c3 = isY ? (24 + j) * 128u : j * 256u + 128u;
  const unsigned l3 = lane >> 3, l7 = lane & 7, l4 = lane >> 4;
  const unsigned vx0 = l3 * 2048u + ((l7 ^ l4) << 4), vx1 = vx0 ^ 64u;
  const unsigned wsw = (l7 ^ (((l4 & 1) | ((wid & 1) << 1)) << 1)) << 4;
  const unsigned vw0 = l3 * 6144u + wsw, vw2 = l3 * 7168u + wsw;
  const unsigned vw1 = isY ? vw0 : vw2;
  const unsigned ldsb = (unsigned)(uintptr_t)lds;
  unsigned sox = (unsigned)(mt * 256 + wid * 32) * 2048u;
  unsigned so0 = wid * 8 * 6144u + c0, so1 = wid * 8 * ld1, so2 = wid * 8 * 7168u + c3;
  const unsigned m0x = ldsb + wid * 4096u, m0w = ldsb + G1_WB + wid * 1024u;
#define G1_DX(i, st) dma16<true>(sx, ((i) & 1) ? vx1 : vx0, sox + (i) * 16384u, m0x + (st) * G1_XS + (i) * 1024u)
#define G1_DW(i, st)                                                                             \
  do {                                                                                           \
    if ((i) == 0) dma16<false>(sq, vw0, so0, m0w + (st) * G1_WS);                                 \
    if ((i) == 1) dma16<false>(sa, vw1, so1 + c1, m0w + (st) * G1_WS + 8192u);                    \
    if ((i) == 2) dma16<false>(sb, vw1, so1 + c2, m0w + (st) * G1_WS + 16384u);                   \
    if ((i) == 3) dma16<false>(sg, vw2, so2, m0w + (st) * G1_WS + 24576u);                        \
    if ((i) == 4) dma16<false>(su, vw2, so2, m0w + (st) * G1_WS + 32768u);                        \
  } while (0)
#define G1_ADVX() do { sox += 128u; } while (0)
#define G1_ADVW() do { so0 += 64u * 6144u; so1 += 64u * ld1; so2 += 64u * 7168u; } while (0)
#define G1_CODE() dma16<false>(scode, vx0, (unsigned)(mt * 16384 + ((j >> 3) & 1) * 1024 + wid * 128), ldsb + LDS_DUMP + wid * 1024u)
  f32x4 acc[4][10];
  G1_DW(0, 0); G1_DW(1, 0); G1_DW(2, 0); G1_DW(3, 0); G1_DW(4, 0); G1_ADVW();
#pragma unroll
  for (int m = 0; m < 4; ++m)
#pragma unroll
    for (int n = 0; n < 10; ++n) { acc[m][n] = f32x4{}; PIN(acc[m][n]); }
  await(ws, FLG_F1 + mt * 32 + wid * 4 + (lane & 3), tag);
  G1_DX(0, 0); G1_DX(1, 0); G1_DX(2, 0); G1_DX(3, 0); G1_ADVX();
  G1_DX(0, 1); G1_DX(1, 1); G1_DX(2, 1); G1_DX(3, 1); G1_ADVX();
  G1_DW(0, 1); G1_DW(1, 1); G1_DW(2, 1); G1_DW(3, 1); G1_DW(4, 1); G1_ADVW();
  const int t16 = lane & 15, g = lane >> 4, q4 = t16 >> 2, p4 = lane & 3;
  unsigned xa0 = ldsb + (wr * 64 + t16) * 128 + ((g ^ ((t16 >> 1) & 7)) << 4), xa1 = xa0 ^ 64u;
  unsigned wa00 = ldsb + G1_WB + (8 * g + q4) * 128 + (((2 * wc) ^ (((q4 >> 1) & 1) | ((g & 1) << 1))) << 5) + p4 * 8;
  unsigned wa01 = wa00 ^ 32u, wa10 = wa00 + G1_WS, wa11 = wa01 + G1_WS;
  asm volatile("" : "+v"(xa0), "+v"(xa1), "+v"(wa00), "+v"(wa01), "+v"(wa10), "+v"(wa11));
  bf16x8 xf[2][4];
  s16x4 wl[10], wh[10];
#define G1_RX(st, s, m) xf[s][m] = lds128(LP((s) ? xa1 : xa0) + (st) * G1_XS + (m) * 2048)
#define G1_RW(st, s, n)                                                                                      \
  do {                                                                                                       \
    LAS const char* p_ = LP((st) ? (((n) & 1) ? wa11 : wa10) : (((n) & 1) ? wa01 : wa00)) + ((n) >> 1) * 8192 + (s) * 4096; \
    wl[n] = ldstr(p_); wh[n] = ldstr(p_ + 512);                                                              \
  } while (0)
#define G1_MM(s, n, m)                                                                                       \
  do {                                                                                                       \
    if (SWAP0 && (n) < 2) mfma16a(acc[m][n], xf[s][m], cat8(wl[n], wh[n]));                                   \
    else mfma16a(acc[m][n], cat8(wl[n], wh[n]), xf[s][m]);                                                    \
  } while (0)
#define G1_GRP(s, n, A, B, C) do { G1_MM(s, n, 0); SBAR(); A; SBAR(); G1_MM(s, n, 1); SBAR(); B; SBAR(); G1_MM(s, n, 2); SBAR(); C; SBAR(); G1_MM(s, n, 3); SBAR(); } while (0)
#define G1_WAITBAR() asm volatile("s_waitcnt vmcnt(0) lgkmcnt(0)\n\ts_barrier" ::: "memory")
#define G1_HEAD(st)                                                                                          \
  do {                                                                                                       \
    G1_GRP(0, 0, G1_RW(st, 0, 9), NOP_, NOP_);                                                               \
    G1_GRP(0, 1, G1_RW(st, 1, 0), G1_RX(st, 1, 0), NOP_);                                                    \
    G1_GRP(0, 2, G1_RW(st, 1, 1), G1_RX(st, 1, 1), NOP_);                                                    \
    G1_GRP(0, 3, G1_RW(st, 1, 2), G1_RX(st, 1, 2), NOP_);                                                    \
    G1_GRP(0, 4, G1_RW(st, 1, 3), G1_RX(st, 1, 3), NOP_);                                                    \
    G1_GRP(0, 5, G1_RW(st, 1, 4), NOP_, NOP_);                                                               \
    G1_GRP(0, 6, G1_RW(st, 1, 5), NOP_, NOP_);                                                               \
    G1_GRP(0, 7, G1_RW(st, 1, 6), NOP_, NOP_);                                                               \
    G1_GRP(0, 8, G1_RW(st, 1, 7), NOP_, NOP_);                                                               \
    G1_GRP(0, 9, G1_RW(st, 1, 8), NOP_, NOP_);                                                               \
    G1_GRP(1, 0, G1_RW(st, 1, 9), NOP_, NOP_);                                                               \
    G1_GRP(1, 1, NOP_, NOP_, NOP_);                                                                          \
    G1_GRP(1, 2, NOP_, NOP_, NOP_);                                                                          \
    G1_WAITBAR();                                                                                            \
  } while (0)
#define G1_TILE(st, DM)                                                                                      \
  do {                                                                                                       \
    G1_HEAD(st);                                                                                             \
    G1_GRP(1, 3, G1_RX(st ^ 1, 0, 0), G1_RW(st ^ 1, 0, 0), if (DM) G1_DX(0, st));                             \
    G1_GRP(1, 4, G1_RX(st ^ 1, 0, 1), G1_RW(st ^ 1, 0, 1), if (DM) G1_DW(0, st));                             \
    G1_GRP(1, 5, G1_RX(st ^ 1, 0, 2), G1_RW(st ^ 1, 0, 2), if (DM) G1_DX(1, st));                             \
    G1_GRP(1, 6, G1_RX(st ^ 1, 0, 3), G1_RW(st ^ 1, 0, 3), if (DM) G1_DW(1, st));                             \
    G1_GRP(1, 7, G1_RW(st ^ 1, 0, 4), G1_RW(st ^ 1, 0, 5), if (DM) { G1_DX(2, st); G1_DW(2, st); });          \
    G1_GRP(1, 8, G1_RW(st ^ 1, 0, 6), G1_RW(st ^ 1, 0, 7), if (DM) { G1_DX(3, st); G1_DW(3, st); });          \
    G1_GRP(1, 9, G1_RW(st ^ 1, 0, 8), NOP_, if (DM) { G1_DW(4, st); G1_ADVX(); G1_ADVW(); });                 \
  } while (0)
  asm volatile("s_waitcnt vmcnt(9)\n\ts_barrier" ::: "memory");
  G1_RX(0, 0, 0); G1_RW(0, 0, 0); G1_RX(0, 0, 1); G1_RX(0, 0, 2); G1_RX(0, 0, 3);
  G1_RW(0, 0, 1); G1_RW(0, 0, 2); G1_RW(0, 0, 3); G1_RW(0, 0, 4); G1_RW(0, 0, 5); G1_RW(0, 0, 6); G1_RW(0, 0, 7); G1_RW(0, 0, 8);
  SBAR();
  for (int t = 0; t < 14; t += 2) {
    G1_CODE();
    G1_TILE(0, true); G1_TILE(1, true);
  }
  {
    const srd_t sw = mksrd(ws, WS_END);
    unsigned vq, vk;
    auto store_addr = [&]() { const unsigned l = lane_id(); vq = (l >> 4) * 512u + (l & 15) * 16u; vk = (l >> 4) * 512u + (2 * (l & 8) + (l & 7)) * 16u; };
    const unsigned T32 = mt * 8 + wr * 2;
    auto store_u = [&](int u, int i) {
      const int a = a0 + u; const unsigned head = 2 * (a & 15) + wc; const bool isq = a < 16;
      if (SWAP0) st16_sc1(sw, vk, WS_KV + (head * 64 + T32 + (i >> 1)) * 4096u + 2048u + (i & 1) * 128u, pack44(acc[2 * (i >> 1)][i & 1], acc[2 * (i >> 1) + 1][i & 1], 1.f));
      else st16_sc1(sw, isq ? vq : vk, isq ? WS_Q + (head * 64 + T32 + (i >> 1)) * 2048u + (i & 1) * 256u : WS_KV + (head * 64 + T32 + (i >> 1)) * 4096u + (i & 1) * 128u,
                    pack44(acc[i][2 * u], acc[i][2 * u + 1], isq ? QSCALE : 1.f));
    };
#define G1_HA(k) do { if (!isY) { const f32x2 h_ = glu2(acc[(k) >> 2][2 + (((k) >> 1) & 1)][2 * ((k) & 1)], acc[(k) >> 2][2 + (((k) >> 1) & 1)][2 * ((k) & 1) + 1], acc[(k) >> 2][4 + (((k) >> 1) & 1)][2 * ((k) & 1)], acc[(k) >> 2][4 + (((k) >> 1) & 1)][2 * ((k) & 1) + 1]); \
      unsigned w_ = cvtpk(h_[0], h_[1]); PIN(w_); ha[(k) >> 2][(k) & 3] = w_; } } while (0)
#define G1_SY(u, i) do { if (isY) store_u(u, i); } while (0)
#define G1_RD(d, st, s, n)                                                                                   \
  do {                                                                                                       \
    LAS const char* p_ = LP((st) ? (((n) & 1) ? wa11 : wa10) : (((n) & 1) ? wa01 : wa00)) + ((n) >> 1) * 8192 + (s) * 4096; \
    wl[d] = ldstr(p_); wh[d] = ldstr(p_ + 512);                                                              \
  } while (0)
#define G1_MD(d, s, n, m)                                                                                    \
  do {                                                                                                       \
    if (SWAP0 && (n) < 2) mfma16a(acc[m][n], xf[s][m], cat8(wl[d], wh[d]));                                   \
    else mfma16a(acc[m][n], cat8(wl[d], wh[d]), xf[s][m]);                                                    \
  } while (0)
#define G1_GD(d, s, n, A, B, C) do { G1_MD(d, s, n, 0); SBAR(); A; SBAR(); G1_MD(d, s, n, 1); SBAR(); B; SBAR(); G1_MD(d, s, n, 2); SBAR(); C; SBAR(); G1_MD(d, s, n, 3); SBAR(); } while (0)
    G1_GD(0, 0, 0, G1_RD(9, 0, 1, 0), G1_RX(0, 1, 0), NOP_);
    G1_GD(1, 0, 1, G1_RD(0, 0, 1, 1), G1_RX(0, 1, 1), NOP_);
    G1_GD(2, 0, 2, G1_RD(1, 0, 1, 2), G1_RX(0, 1, 2), NOP_);
    G1_GD(3, 0, 3, G1_RD(2, 0, 1, 3), G1_RX(0, 1, 3), NOP_);
    G1_GD(4, 0, 4, G1_RD(3, 0, 1, 4), NOP_, NOP_);
    G1_GD(5, 0, 5, G1_RD(4, 0, 1, 5), NOP_, NOP_);
    G1_GD(9, 1, 0, NOP_, NOP_, NOP_);
    asm volatile("s_waitcnt vmcnt(0)\n\ts_barrier" ::: "memory");
    G1_GD(0, 1, 1, G1_RX(1, 0, 0), G1_RD(5, 1, 0, 0), G1_RX(1, 0, 1));
    G1_GD(1, 1, 2, G1_RX(1, 0, 2), G1_RD(6, 1, 0, 1), G1_RX(1, 0, 3));
    G1_GD(2, 1, 3, G1_RD(7, 1, 1, 0), G1_RD(8, 1, 1, 1), NOP_);
    G1_GD(3, 1, 4, G1_RD(9, 1, 0, 2), G1_RD(0, 1, 0, 3), NOP_);
    G1_GD(4, 1, 5, G1_RD(1, 1, 1, 2), G1_RD(2, 1, 1, 3), NOP_);
    G1_GD(5, 0, 0, { G1_RX(1, 1, 0); G1_RX(1, 1, 1); }, { G1_RX(1, 1, 2); G1_RX(1, 1, 3); }, G1_RD(3, 1, 0, 4));
    G1_GD(6, 0, 1, G1_RD(4, 1, 0, 5), NOP_, NOP_);
    G1_GD(9, 0, 2, G1_RD(5, 1, 1, 4), NOP_, NOP_);
    G1_GD(7, 1, 0, G1_RD(6, 1, 1, 5), NOP_, NOP_);
    G1_GD(8, 1, 1, G1_RD(9, 1, 0, 6), NOP_, NOP_);
    G1_GD(0, 0, 3, G1_RD(7, 1, 0, 7), NOP_, NOP_);
    G1_GD(1, 1, 2, G1_RD(8, 1, 0, 8), NOP_, store_addr());
    G1_GD(2, 1, 3, G1_RD(0, 1, 0, 9), store_u(0, 0), store_u(0, 1));
    G1_GD(3, 0, 4, G1_RD(1, 1, 1, 6), store_u(0, 2), store_u(0, 3));
    G1_GD(4, 0, 5, G1_RD(2, 1, 1, 7), NOP_, NOP_);
    G1_GD(5, 1, 4, G1_RD(3, 1, 1, 8), G1_SY(1, 0), G1_SY(1, 1));
    G1_GD(6, 1, 5, G1_RD(4, 1, 1, 9), G1_SY(1, 2), G1_SY(1, 3));
    G1_GD(9, 0, 6, G1_RD(5, 0, 0, 6), NOP_, NOP_);
    G1_GD(7, 0, 7, G1_RD(6, 0, 0, 7), NOP_, G1_HA(0));
    G1_GD(8, 0, 8, G1_RD(9, 0, 0, 8), { G1_SY(2, 0); G1_HA(1); }, G1_SY(2, 1));
    G1_GD(0, 0, 9, G1_RD(7, 0, 0, 9), { G1_SY(2, 2); G1_HA(2); }, G1_SY(2, 3));
    G1_GD(1, 1, 6, { G1_RX(0, 0, 0); G1_RX(0, 0, 1); }, G1_RD(8, 0, 1, 6), G1_HA(3));
    G1_GD(2, 1, 7, { G1_RX(0, 0, 2); G1_RX(0, 0, 3); }, G1_RD(0, 0, 1, 7), G1_HA(4));
    G1_GD(3, 1, 8, G1_RD(1, 0, 1, 8), G1_HA(5), G1_HA(6));
    G1_GD(4, 1, 9, G1_RD(2, 0, 1, 9), G1_HA(7), NOP_);
    G1_GD(5, 0, 6, { G1_RX(0, 1, 0); G1_RX(0, 1, 1); }, { G1_RX(0, 1, 2); G1_RX(0, 1, 3); }, G1_HA(8));
    G1_GD(6, 0, 7, G1_HA(9), NOP_, NOP_);
    G1_GD(9, 0, 8, G1_HA(10), NOP_, NOP_);
    G1_GD(7, 0, 9, G1_HA(11), NOP_, NOP_);
    G1_WAITBAR();
    G1_GD(8, 1, 6, if (wid == 7) publish(ws, FLG_F2 + mt * 32 + j, tag, lane_id()), G1_HA(12), NOP_);
#define G1_PF(k) do { if (wc) { const unsigned vw_ = g2_vw(lane_id(), cg); _Pragma("unroll") for (int i = 4 * (k); i < 4 * (k) + 8; ++i) dma16<false>(mksrd(wd, F * D * 2), vw_, (unsigned)(28 * wr) * 65536u + i * 16384u, ldsb + P2_G2RING + wr * 16384u + i * 1024u); } } while (0)
    G1_GD(0, 1, 7, G1_HA(13), G1_PF(0), NOP_);
    G1_GD(1, 1, 8, G1_HA(14), G1_PF(2), NOP_);
    G1_GD(2, 1, 9, G1_HA(15), NOP_, NOP_);
    if (wc == 0) { __builtin_amdgcn_s_setprio(1); dma16<true>(mksrd(ws, WS_XN), att_dep(head, mt, lane_id()) * (8u * FST), 0u, ldsb + LDS_DUMP + wid * 1024u); }
  }
  asm volatile("s_nop 7" ::: "memory"); SBAR();
#pragma unroll
  for (int m = 0; m < 4; ++m)
#pragma unroll
    for (int n = 0; n < 4; ++n) accb[m][n] = acc[m][6 + n];
}

DI void att_slow(const char* kv, const bf16x8 (&qf)[2][2], f32x16 (&o)[2], float (&linv)[2], int lane) {
  __builtin_amdgcn_fence(__ATOMIC_ACQUIRE, "agent");
#pragma unroll
  for (int m = 0; m < 2; ++m) {
    f32x16 oo = f32x16{}; float mref = -INFINITY, l = 0.f;
    for (int kt = 0; kt < 64; ++kt) {
      const char* p = kv + kt * 4096 + lane * 16;
      const bf16x8 k0 = *(const bf16x8*)p, k1 = *(const bf16x8*)(p + 1024), v0 = *(const bf16x8*)(p + 2048), v1 = *(const bf16x8*)(p + 3072);
      f32x16 s = MFMA32(k0, qf[m][0], f32x16{});
      s = MFMA32(k1, qf[m][1], s);
      float mx = s[0];
#pragma unroll
      for (int r = 1; r < 16; ++r) mx = fmaxf(mx, s[r]);
      mx = fmaxf(fmaxf(mx, __shfl_xor(mx, 32)), mref);
      const float f = EX2(mref - mx);
      mref = mx;
      float sum = 0.f;
#pragma unroll
      for (int r = 0; r < 16; ++r) { s[r] = EX2(s[r] - mref); sum += s[r]; oo[r] *= f; }
      l = l * f + sum;
      oo = MFMA32(v0, BF(pack8(s, 0, 1.f)), oo);
      oo = MFMA32(v1, BF(pack8(s, 1, 1.f)), oo);
    }
    o[m] = oo; linv[m] = 1.f / (l + __shfl_xor(l, 32));
  }
}
DI void lds_meet(unsigned v, unsigned need, unsigned addr) {
  unsigned left = SPIN_CAP;
  asm volatile("1:\n\tv_cmp_le_u32 vcc, %2, %0\n\ts_cmp_eq_u64 vcc, exec\n\ts_cbranch_scc1 2f\n\ts_sleep 1\n\tds_read_b32 %0, %3\n\ts_sub_u32 %1, %1, 1\n\ts_waitcnt lgkmcnt(0)\n\ts_cbranch_scc0 1b\n2:"
               : "+v"(v), "+s"(left) : "s"(need), "v"(addr) : "vcc", "scc", "memory");
}
DI void lds_meet_fast(unsigned v, unsigned need, unsigned addr) {
  unsigned left;
  asm volatile("v_cmp_le_u32 vcc, %2, %0\n\ts_cmp_eq_u64 vcc, exec\n\ts_cbranch_scc0 3f\n2:\n\t.subsection 1\n3:\n\ts_mov_b32 %1, 0x100000\n4:\n\ts_sleep 1\n\tds_read_b32 %0, %3\n\ts_waitcnt lgkmcnt(0)\n\t"
               "v_cmp_le_u32 vcc, %2, %0\n\ts_cmp_eq_u64 vcc, exec\n\ts_cbranch_scc1 2b\n\ts_sub_u32 %1, %1, 1\n\ts_cbranch_scc0 4b\n\ts_branch 2b\n\t.subsection 0"
               : "+v"(v), "=&s"(left) : "s"(need), "v"(addr) : "vcc", "scc", "memory");
}
DI void glu_pack(const f32x4 (&a)[4][4], u32x4 (&hp)[4]) {
#pragma unroll
  for (int m = 0; m < 4; ++m) {
#pragma unroll
    for (int k = 0; k < 4; ++k) { const f32x2 h = glu2(a[m][k >> 1][2 * (k & 1)], a[m][k >> 1][2 * (k & 1) + 1], a[m][2 + (k >> 1)][2 * (k & 1)], a[m][2 + (k >> 1)][2 * (k & 1) + 1]); hp[m][k] = cvtpk(h[0], h[1]); }
    PIN(hp[m]);
  }
}

DI void dma16x4_sc1(srd_t srd, unsigned voff, unsigned soff, unsigned m0v) {
  asm volatile("s_mov_b32 m0, %0\n\ts_nop 0\n\tbuffer_load_dwordx4 %1, %2, %3 offen sc1 lds\n\tbuffer_load_dwordx4 %1, %2, %3 offen offset:1024 sc1 lds\n\t"
               "buffer_load_dwordx4 %1, %2, %3 offen offset:2048 sc1 lds\n\tbuffer_load_dwordx4 %1, %2, %3 offen offset:3072 sc1 lds" ::"s"(m0v), "v"(voff), "s"(srd), "s"(soff) : "memory");
}
DI void att_role(const u16* wo, char* ws, LAS char* lds, int head, int qb, int tg, int cg, int wid, u64 tag, const u32x4 (&hp)[4], const u32x4 (&ha)[4]) {
  const int tz = lane_id();
  const int lane = tz & 63;
  const char* kvg = ws + WS_KV + (size_t)head * 262144;
  const srd_t skv = mksrd(kvg, 262144), sw = mksrd(ws, WS_END);
  const unsigned ldsb = (unsigned)(uintptr_t)lds;
  const unsigned voff = lane * 16u;
  volatile LAS unsigned* ctl = (volatile LAS unsigned*)(lds + LDS_CTL);
  const unsigned ctla = ldsb + LDS_CTL + (lane & 7) * 4u;
  {
#pragma unroll
    for (int i = 0; i < 4; ++i) { *(LAS u32x4*)(lds + 131072 + wid * 4096 + i * 1024 + lane * 16) = hp[i]; *(LAS u32x4*)(lds + 49152 + wid * 4096 + i * 1024 + lane * 16) = ha[i]; }
    asm volatile("s_waitcnt lgkmcnt(0)" ::: "memory");
    ctl[12 + wid] = 1u;
  }
#define AT_ARRIVE(k) ctl[wid] = (unsigned)((k) + 2)
#define AT_MEET(v, k) lds_meet(v, (unsigned)((k) + 2), ctla)
  f32x16 o[2] = {f32x16{}, f32x16{}};
  f32x4 ls[2] = {f32x4{}, f32x4{}};
  bf16x8 sel;
  { const short one = ((lane & 1) == ((lane >> 4) & 1) && (lane & 2) == 0) ? (short)0x3f80 : (short)0;
#pragma unroll
    for (int e = 0; e < 8; ++e) sel[e] = one; }
  bf16x8 kf[2][2], vf[2][2], qf[2][2];
  f32x16 sa, sb, negm[2];
  u32x4 pa0, pa1, pb0 = u32x4{}, pb1 = u32x4{};
  vf[1][0] = bf16x8{}; vf[1][1] = bf16x8{};
  PIN(sel); PIN(o[0]); PIN(o[1]); PIN(ls[0]); PIN(ls[1]); PIN(pb0); PIN(pb1); PIN(vf[1][0]); PIN(vf[1][1]);
  const unsigned kp = ldsb + lane * 16u;
  {
    asm volatile("s_waitcnt vmcnt(0)" ::: "memory");
    const u32x2 f = *(volatile LAS const u32x2*)(lds + LDS_DUMP + wid * 1024 + lane * 16);
    if (!__all(f[0] == (unsigned)tag && f[1] == (unsigned)(tag >> 32))) await(ws, att_dep(head, qb, lane), tag);
  }
  dma16x4_sc1(skv, voff, wid * 4096u, ldsb + wid * 4096u);
  dma16x4_sc1(sw, voff, WS_Q + (head * 64 + qb * 8 + wid * 2) * 2048u, ldsb + 32768u + wid * 4096u);
  dma16x4_sc1(skv, voff, 16384u + wid * 4096u, ldsb + 16384u + wid * 4096u);
  asm volatile("s_waitcnt vmcnt(0)" ::: "memory");
  AT_ARRIVE(-1);
#pragma unroll
  for (int i = 0; i < 4; ++i) qf[i >> 1][i & 1] = lds128(LP(kp) + 32768 + wid * 4096 + i * 1024);
  asm volatile("" : "+v"(qf[0][0]), "+v"(qf[0][1]), "+v"(qf[1][0]), "+v"(qf[1][1]));
  lds_meet(ctl[lane & 3], 1u, ldsb + LDS_CTL + (lane & 3) * 4u);
  kf[0][0] = lds128(LP(kp)); kf[0][1] = lds128(LP(kp) + 1024);
  {
    sa = MFMA32(kf[0][0], qf[0][0], f32x16{}); sa = MFMA32(kf[0][1], qf[0][1], sa);
    sb = MFMA32(kf[0][0], qf[1][0], f32x16{}); sb = MFMA32(kf[0][1], qf[1][1], sb);
    dma16x4_sc1(skv, voff, 32768u + wid * 4096u, ldsb + 32768u + wid * 4096u);
    float ma = sa[0], mb = sb[0];
#pragma unroll
    for (int r = 1; r < 16; ++r) { ma = fmaxf(ma, sa[r]); mb = fmaxf(mb, sb[r]); }
    { const auto ra = __builtin_amdgcn_permlane32_swap(__float_as_uint(ma), __float_as_uint(ma), false, false), rb = __builtin_amdgcn_permlane32_swap(__float_as_uint(mb), __float_as_uint(mb), false, false);
      ma = fmaxf(__uint_as_float(ra[0]), __uint_as_float(ra[1])); mb = fmaxf(__uint_as_float(rb[0]), __uint_as_float(rb[1])); }
#pragma unroll
    for (int r = 0; r < 16; ++r) { negm[0][r] = -ma; negm[1][r] = -mb; sa[r] -= ma; }
  }
  PIN(negm[0]); PIN(negm[1]);
#define E1(S_, i) S_[i] = EX2(S_[i])
#define CV(P_, i, S_, r) P_[i] = cvtpk(S_[r], S_[(r) + 1])
#define AT_UNIT(SC, PC0, PC1, SN, kz, mn, PP0, PP1, vz, X0, X1, X2, X3)                                       \
  do {                                                                                                       \
    SN = MFMA32(kf[kz][0], qf[mn][0], negm[mn]); SBAR(); X0;                                                  \
    E1(SC, 0); E1(SC, 1); E1(SC, 2); E1(SC, 3); PIN(SC); SBAR();                                               \
    SN = MFMA32(kf[kz][1], qf[mn][1], SN); SBAR(); X1;                                                        \
    CV(PC0, 0, SC, 0); CV(PC0, 1, SC, 2); E1(SC, 4); E1(SC, 5); E1(SC, 6); PIN(SC); PIN(PC0); SBAR();           \
    o[mn] = MFMA32(vf[vz][0], BF(PP0), o[mn]); SBAR(); X2;                                                    \
    E1(SC, 7); CV(PC0, 2, SC, 4); E1(SC, 8); CV(PC0, 3, SC, 6); E1(SC, 9); PIN(SC); PIN(PC0); SBAR();           \
    ls[mn] = MFMA16(sel, BF(PP0), ls[mn]); SBAR();                                                            \
    E1(SC, 10); E1(SC, 11); PIN(SC); SBAR();                                                                  \
    o[mn] = MFMA32(vf[vz][1], BF(PP1), o[mn]); SBAR(); X3;                                                    \
    CV(PC1, 0, SC, 8); E1(SC, 12); CV(PC1, 1, SC, 10); E1(SC, 13); E1(SC, 14); E1(SC, 15); PIN(SC); PIN(PC1); SBAR(); \
    ls[mn] = MFMA16(sel, BF(PP1), ls[mn]); SBAR();                                                            \
    CV(PC1, 2, SC, 12); CV(PC1, 3, SC, 14); PIN(PC1); SBAR();                                                  \
  } while (0)
#define AT_KT(z, KADDR, VADDR, D0, D1)                                                                        \
  do {                                                                                                       \
    AT_UNIT(sa, pa0, pa1, sb, z, 1, pb0, pb1, z ^ 1, kf[z ^ 1][0] = lds128(KADDR), kf[z ^ 1][1] = lds128((KADDR) + 1024), vf[z][0] = lds128(VADDR), vf[z][1] = lds128((VADDR) + 1024)); \
    asm volatile("" : "+v"(kf[z ^ 1][0]), "+v"(kf[z ^ 1][1]), "+v"(vf[z][0]), "+v"(vf[z][1]));   \
    AT_UNIT(sb, pb0, pb1, sa, z ^ 1, 0, pa0, pa1, z, D0, NOP_, D1, NOP_);                                      \
  } while (0)
  const unsigned vwo = g2_vw(lane, cg), m0b = ldsb + wid * 4096u;
  unsigned arr = 2u, ctlw = ldsb + LDS_CTL + wid * 4u;
  PIN(arr); PIN(ctlw);
#define AT_INT(p, SRD, VO, SO, ST)                                                                            \
  do {                                                                                                       \
    unsigned pv;                                                                                             \
    AT_KT(0, LP(kp) + (p) * 16384 + 4096, LP(kp) + (p) * 16384 + 2048, NOP_, NOP_);                           \
    AT_KT(1, LP(kp) + (p) * 16384 + 8192, LP(kp) + (p) * 16384 + 6144, pv = ctl[lane & 7], NOP_);             \
    lds_meet_fast(pv, 4 * it + (p) + 1, ctla);     \
    SBAR();                                                                                                  \
    AT_KT(0, LP(kp) + (p) * 16384 + 12288, LP(kp) + (p) * 16384 + 10240, dma16<true>(SRD, VO, (SO), m0b + (((p) + 3) & 3) * 16384u), dma16<true>(SRD, VO, (SO) + (ST), m0b + (((p) + 3) & 3) * 16384u + 1024u)); \
    AT_KT(1, LP(kp) + (((p) + 1) & 3) * 16384, LP(kp) + (p) * 16384 + 14336, dma16<true>(SRD, VO, (SO) + 2 * (ST), m0b + (((p) + 3) & 3) * 16384u + 2048u), dma16<true>(SRD, VO, (SO) + 3 * (ST), m0b + (((p) + 3) & 3) * 16384u + 3072u)); \
    asm volatile("s_waitcnt vmcnt(4)\n\tds_write_b32 %1, %0\n\tv_add_u32 %0, 1, %0" : "+v"(arr) : "v"(ctlw) : "memory");     \
  } while (0)
  for (int it = 0; it < 4; ++it) {
    const bool kvq = it < 3;
    const srd_t sq_ = mksrd(kvq ? (const void*)kvg : (const void*)wo, kvq ? 262144u : D * D * 2u);
    const unsigned sk_ = (4 * it + 3) * 16384u + wid * 4096u;
    const unsigned vq_ = kvq ? voff : vwo, so_ = kvq ? sk_ + 16384u : (8 * wid + 5) * 65536u, st_ = kvq ? 1024u : 16384u, sn_ = kvq ? 16384u : 65536u;
    AT_INT(0, skv, voff, sk_, 1024u);
    AT_INT(1, sq_, vq_, so_, st_);
    AT_INT(2, sq_, vq_, so_ + sn_, st_);
    AT_INT(3, sq_, vq_, so_ + 2 * sn_, st_);
  }
  o[1] = MFMA32(vf[1][0], BF(pb0), o[1]); ls[1] = MFMA16(sel, BF(pb0), ls[1]);
  o[1] = MFMA32(vf[1][1], BF(pb1), o[1]); ls[1] = MFMA16(sel, BF(pb1), ls[1]);
  auto store_o = [&](int m, float inv) {
    const unsigned T = qb * 8 + wid * 2 + m;
    const unsigned so = WS_X2 + ((T >> 2) * NKB + 112 + head) * 8192u + (T & 3) * 512u, vo = (lane >> 5) * 2048u + (lane & 31) * 16u;
    st16_sc1(sw, vo, so, pack8(o[m], 0, inv));
    st16_sc1(sw, vo, so + 4096u, pack8(o[m], 1, inv));
  };
  float lt[2];
#pragma unroll
  for (int m = 0; m < 2; ++m) { lt[m] = (lane & 16) ? ls[m][1] : ls[m][0]; store_o(m, __builtin_amdgcn_rcpf(lt[m])); }
  bool bad = false;
#pragma unroll
  for (int m = 0; m < 2; ++m) {
    float chk = lt[m], nanp = lt[m];
#pragma unroll
    for (int r = 0; r < 16; ++r) { chk = fmaxf(chk, fabsf(o[m][r])); nanp += o[m][r]; }
    bad = bad || !(chk < 1e30f) || (nanp != nanp);
  }
  if (__builtin_expect(__any(bad), 0)) {
    float linv[2];
    asm volatile("s_waitcnt vmcnt(0)" ::: "memory");
    att_slow(kvg, qf, o, linv, lane);
    store_o(0, linv[0]); store_o(1, linv[1]);
  }
  asm volatile("s_waitcnt vmcnt(0)" ::: "memory");
  publish_last(ws, ldsb + LDS_CTL + 17 * 4u, 4u, FLG_F4 + qb * 32 + head, tag, lane);
  await(ws, FLG_F4 + (tg >> 1) * 32 + 8 * wid + (lane & 7), tag);
  { unsigned pv = 0; AT_MEET(pv, 15); }
  ctl[8 + wid] = 1u;
}

constexpr int g2_cnt(int m, bool x0, bool w0, bool x1, bool w1, bool w2) {
  return (7 - m) + (w2 ? (8 - m) >> 1 : 0) + (x1 ? 8 : 0) + (w1 ? 4 : 0) + (x0 ? m : 0) + (w0 ? (m + 1) >> 1 : 0);
}
DI void g2_role(const u16* x, u32x4 (&xr)[2], const u16* wo, const u16* wd, char* ws, LAS char* lds, int tg, int cg, int kq, u64 tag, const u32x4 (&hp)[4], const u32x4 (&ha)[4], int mt, int j, srd_t scode) {
  const int tz = lane_id();
  const int lane = tz & 63;
  const srd_t sx2 = mksrd(ws + WS_X2 + (size_t)tg * NKB * 8192, NKB * 8192);
  const srd_t swd = mksrd(wd, F * D * 2), swo = mksrd(wo, D * D * 2);
  const unsigned ldsw = (unsigned)(uintptr_t)lds + P2_G2RING + kq * 16384u;
  const unsigned vw = g2_vw(lane, cg);
  const int t16 = lane & 15, g = lane >> 4, q4 = t16 >> 2, p4 = lane & 3;
  const unsigned vx = g * 2048u + t16 * 16u;
  volatile LAS unsigned* ctl = (volatile LAS unsigned*)(lds + LDS_CTL);
  unsigned wa[4];
  f32x4 acc[8][4];
  {
    u32x4 hq[8];
    const srd_t sw = mksrd(ws, WS_END);
    const unsigned cb = (unsigned)(uintptr_t)lds + LDS_CTL;
    lds_meet(0u, 1u, cb + (12 + kq) * 4u);
#pragma unroll
    for (int i = 0; i < 8; ++i) hq[i] = *(LAS const u32x4*)(lds + (i < 4 ? 131072 : 49152 - 4096) + kq * 4096 + i * 1024 + lane * 16);
#pragma unroll
    for (int i = 0; i < 8; ++i) PIN(hq[i]);
    ctl[4 + kq] = ~0u;
#pragma unroll
    for (int c = 0; c < 4; ++c) { wa[c] = ldsw + (4 * g + q4) * 128 + ((c ^ ((2 * g + (q4 >> 1)) & 3)) << 5) + p4 * 8; PIN(wa[c]); }
#pragma unroll
    for (int m = 0; m < 8; ++m)
#pragma unroll
      for (int c = 0; c < 4; ++c) { acc[m][c] = f32x4{}; PIN(acc[m][c]); }
    lds_meet(0u, 1u, cb + (lane & 3) * 4u);
    const int pair = j < 24 ? 2 * j + 1 : 24 + j;
#pragma unroll
    for (int m = 0; m < 4; ++m) { st16_sc1(sw, vx, h_base(mt, pair, kq, 1) + m * 256u, hp[m]); st16_sc1(sw, vx, h_base(mt, pair, kq, 0) + m * 256u, hq[m]); }
    if (j < 24) {
#pragma unroll
      for (int m = 0; m < 4; ++m) { st16_sc1(sw, vx, h_base(mt, 2 * j, kq, 1) + m * 256u, ha[m]); st16_sc1(sw, vx, h_base(mt, 2 * j, kq, 0) + m * 256u, hq[4 + m]); }
    }
    asm volatile("s_waitcnt vmcnt(0)" ::: "memory");
    publish_last(ws, cb + 16 * 4u, 4u, FLG_F3 + mt * 32 + j, tag, lane);
  }
  if (blockIdx.x == 0 && kq == 0) {
    for (int i = 0; i < 4; ++i) await(ws, FLG_F1 + i * 64 + lane, tag);
    publish(ws, 0, tag, lane);
  }
  await(ws, FLG_F3 + (tg >> 1) * 32 + 7 * kq + (lane < (kq == 3 ? 11 : 7) ? lane : 0), tag);
  bf16x8 xf[2][8];
  unsigned xo = (unsigned)(28 * kq) * 8192u, wo_ = (unsigned)(28 * kq + 4) * 65536u;
#pragma unroll
  for (int z = 0; z < 2; ++z) {
#pragma unroll
    for (int m = 0; m < 8; ++m) ld16<true>(xf[z][m], sx2, vx, xo, m * 256);
    xo += 8192u; }
  s16x4 wl[2][4], wh[2][4];
#define G2_RW(z, off, c) do { LAS const char* p_ = LP(wa[c]) + (off); wl[z][c] = ldstr(p_); wh[z][c] = ldstr(p_ + 2048); } while (0)
#pragma unroll
  for (int c = 0; c < 4; ++c) G2_RW(0, 0, c);
  SBAR();
#define G2_KB(sl, WS_, RD, X0, W0, X1, W1, W2) G2_KBX(sl, WS_, RD, (((sl) + 1) & 3) * 4096, X0, W0, X1, W1, W2, 0, NOP_)
#define G2_KBX(sl, WS_, RD, ROFF, X0, W0, X1, W1, W2, EXTRA, HOOK)                                                                \
  do {                                                                                                       \
    constexpr int z = (sl) & 1;                                                                              \
    _Pragma("unroll") for (int m = 0; m < 8; ++m) {                                                          \
      switch (m) {                                                                                           \
        case 0: mfma16w<g2_cnt(0, X0, W0, X1, W1, W2) + (EXTRA)>(acc[m][0], cat8(wl[z][0], wh[z][0]), xf[z][m]); break; \
        case 1: mfma16w<g2_cnt(1, X0, W0, X1, W1, W2) + (EXTRA)>(acc[m][0], cat8(wl[z][0], wh[z][0]), xf[z][m]); break; \
        case 2: mfma16w<g2_cnt(2, X0, W0, X1, W1, W2) + (EXTRA)>(acc[m][0], cat8(wl[z][0], wh[z][0]), xf[z][m]); break; \
        case 3: mfma16w<g2_cnt(3, X0, W0, X1, W1, W2) + (EXTRA)>(acc[m][0], cat8(wl[z][0], wh[z][0]), xf[z][m]); break; \
        case 4: mfma16w<g2_cnt(4, X0, W0, X1, W1, W2) + (EXTRA)>(acc[m][0], cat8(wl[z][0], wh[z][0]), xf[z][m]); break; \
        case 5: mfma16w<g2_cnt(5, X0, W0, X1, W1, W2) + (EXTRA)>(acc[m][0], cat8(wl[z][0], wh[z][0]), xf[z][m]); break; \
        case 6: mfma16w<g2_cnt(6, X0, W0, X1, W1, W2) + (EXTRA)>(acc[m][0], cat8(wl[z][0], wh[z][0]), xf[z][m]); break; \
        default: mfma16w<g2_cnt(7, X0, W0, X1, W1, W2) + (EXTRA)>(acc[m][0], cat8(wl[z][0], wh[z][0]), xf[z][m]); break; \
      }                                                                                                      \
      SBAR();                                                                                                \
      if (RD && m >= 2 && m < 6) G2_RW(z ^ 1, ROFF, m - 2);                                \
      SBAR();                                                                                                \
      mfma16a(acc[m][1], cat8(wl[z][1], wh[z][1]), xf[z][m]);                                                 \
      mfma16a(acc[m][2], cat8(wl[z][2], wh[z][2]), xf[z][m]);                                                 \
      mfma16a(acc[m][3], cat8(wl[z][3], wh[z][3]), xf[z][m]); SBAR();                                         \
      HOOK;                                                                                                  \
      if (X0) ld16_inplace(xf[z][m], sx2, vx, xo, m * 256);                                                  \
      if (W0 && !(m & 1)) dma16<false>(WS_, vw, wo_ + (m >> 1) * 16384u, ldsw + (sl) * 4096u + (m >> 1) * 1024u); \
      SBAR();                                                                                                \
    }                                                                                                        \
    xo += 8192u; wo_ += 65536u;                                                                              \
  } while (0)
#define YES true
#define NO false
  G2_KB(0, swd, YES, YES, YES, YES, NO, NO); G2_KB(1, swd, YES, YES, YES, YES, YES, NO); G2_KB(2, swd, YES, YES, YES, YES, YES, YES); G2_KB(3, swd, YES, YES, YES, YES, YES, YES);
  for (int i = 4; i < 24; i += 4) {
    dma16<false>(scode, vx, (unsigned)((mt * 8192) + ((j >> 3) & 1) * 1024 + kq * 256), (unsigned)(uintptr_t)lds + LDS_DUMP + (4 + kq) * 1024u);
    G2_KB(0, swd, YES, YES, YES, YES, YES, YES); G2_KB(1, swd, YES, YES, YES, YES, YES, YES); G2_KB(2, swd, YES, YES, YES, YES, YES, YES); G2_KB(3, swd, YES, YES, YES, YES, YES, YES);
  }
  wo_ = (unsigned)(8 * kq) * 65536u;
  G2_KB(0, swo, YES, YES, YES, YES, YES, YES); G2_KB(1, swo, YES, YES, YES, YES, YES, YES);
  G2_KB(2, swo, YES, NO, YES, YES, YES, YES); G2_KB(3, swo, YES, NO, YES, NO, YES, YES);
  asm volatile("" : "+v"(wl[0][0]), "+v"(wh[0][0]), "+v"(wl[0][1]), "+v"(wh[0][1]), "+v"(wl[0][2]), "+v"(wh[0][2]), "+v"(wl[0][3]), "+v"(wh[0][3]));
#pragma unroll
  for (int p = 0; p < 4; ++p) dma16<false>(swo, vw, wo_ + p * 16384u, ldsw + p * 1024u);
  lds_meet(0u, 1u, (unsigned)(uintptr_t)lds + LDS_CTL + (8 + kq) * 4u);
  xo = (unsigned)(112 + 8 * kq) * 8192u;
#pragma unroll
  for (int z = 0; z < 2; ++z) {
#pragma unroll
    for (int m = 0; m < 8; ++m) ld16_inplace(xf[z][m], sx2, vx, xo, m * 256);
    xo += 8192u; }
  G2_KB(0, swo, YES, YES, NO, YES, NO, NO); G2_KB(1, swo, YES, YES, NO, YES, NO, NO); G2_KB(2, swo, YES, YES, NO, YES, NO, NO); G2_KB(3, swo, YES, YES, NO, YES, NO, NO);
#pragma unroll
  for (int c = 0; c < 4; ++c) wa[c] -= P2_G2RING + kq * 12288u;
  G2_KBX(0, swo, YES, 0, YES, NO, YES, NO, NO, 0, NOP_); G2_KBX(1, swo, YES, 16384, YES, NO, YES, NO, NO, 0, NOP_);
  G2_KBX(2, swo, YES, 32768, NO, NO, YES, NO, NO, 0, NOP_);
  const int ty = lane_id();
  { const srd_t sxi = mksrd(x, S * D * 2);
    const unsigned xv = (unsigned)((ty & 63) >> 3) * 2048u + (ty & 7) * 16u, xs = (unsigned)(tg * 128 + (4 + kq) * 16) * 2048u + cg * 128u;
    ld16<false>(xr[0], sxi, xv, xs, 0); ld16<false>(xr[1], sxi, xv, xs + 16384u, 0); }
  unsigned pa[4];
#pragma unroll
  for (int c = 0; c < 4; ++c) pa[c] = (unsigned)(uintptr_t)lds + kq * 4096u + (ty & 15) * 256u + (((4 * c + ((ty >> 4) & 3)) ^ (ty & 15)) << 4);
  const unsigned pown = P2_G2RING + kq * 12288u;
#define G2_PUT(mm) do { _Pragma("unroll") for (int c = 0; c < 4; ++c) *(LAS f32x4*)((LAS char*)(uintptr_t)((mm) < 4 ? pa[c] : pa[c] + pown) + ((mm) < 4 ? (mm) * 16384 : ((mm) - 4) * 4096)) = acc[mm][c]; } while (0)
  G2_KBX(3, swo, NO, 0, NO, NO, NO, NO, NO, 2, if (m >= 2) G2_PUT(m >= 2 ? m - 2 : 0));
  asm volatile("s_nop 7" ::: "memory");
  G2_PUT(6); G2_PUT(7);
  asm volatile("s_waitcnt vmcnt(0)" : "+v"(xr[0]), "+v"(xr[1]));
}

__global__ __launch_bounds__(512, 2) void fused_kernel(const u16* x, const u16* wqkv, const u16* wo, const u16* wg, const u16* wu, const u16* wd, char* ws, u16* out) {
  extern __shared__ __attribute__((aligned(16))) char smem[];
  LAS char* lds = (LAS char*)smem;
  const int b = blockIdx.x, xcd = b & 7, r = b >> 3, wid = rfl(threadIdx.x >> 6);
  u64 tag = *(const __attribute__((address_space(4))) u64*)(ws + WS_FLG);
  ((LAS unsigned*)(lds + LDS_CTL))[threadIdx.x & 31] = 0u;
  const int tg = (xcd >> 1) * 4 + (r & 3), cg = (xcd & 1) * 8 + (r >> 2);
  const int mt = r & 7, j = xcd + 8 * (r >> 3);
  srd_t scode;
  { u64 pc; unsigned len;
    asm volatile("s_getpc_b64 %0\n1:\n\ts_mov_b32 %1, .Lfk_code_end-1b" : "=s"(pc), "=s"(len));
    scode = mksrd((const void*)(pc & ~127ull), len); }
  ln_row(x, ws, mt * 256 + j * 8 + wid, scode, (unsigned)((r * 8 + wid) & 127) * 1024u, (unsigned)(uintptr_t)lds + LDS_DUMP + wid * 1024u);
  asm volatile("s_waitcnt vmcnt(0)" : "+s"(tag)::"memory");
  tag = tag * 6364136223846793005ull + 1442695040888963407ull;
  asm volatile("s_barrier" ::: "memory");
  if (wid == 0) publish(ws, FLG_F1 + mt * 32 + j, tag, lane_id());
  const int head = xcd * 4 + (r >> 3);
  f32x4 accb[4][4];
  u32x4 ha[4];
  if (j < 16) g1_body<0>(head, wd, cg, wqkv, wg, wu, ws, lds, wid, mt, j, scode, tag, accb, ha);
  else if (j < 24) g1_body<1>(head, wd, cg, wqkv, wg, wu, ws, lds, wid, mt, j, scode, tag, accb, ha);
  else g1_body<2>(head, wd, cg, wqkv, wg, wu, ws, lds, wid, mt, j, scode, tag, accb, ha);
  u32x4 hp[4];
  glu_pack(accb, hp);
  u32x4 xr[2];
  if (wid >= 4) {
    g2_role(x, xr, wo, wd, ws, lds, tg, cg, wid - 4, tag, hp, ha, mt, j, scode);
  } else {
    att_role(wo, ws, lds, head, mt, tg, cg, wid, tag, hp, ha);
    const int ty = lane_id();
#pragma unroll
    for (int e = 0; e < 2; ++e) xr[e] = *(const u32x4*)(x + (size_t)(tg * 128 + wid * 16 + e * 8 + ((ty & 63) >> 3)) * D + cg * 64 + (ty & 7) * 8);
  }
  const int ln = lane_id();
  unsigned ra[4][4], vo = (unsigned)((tg * 128 + wid * 16 + (ln >> 3)) * D + cg * 64 + (ln & 7) * 8) * 2u;
  f32x4 sum[4];
#pragma unroll
  for (int i = 0; i < 4; ++i) {
    const unsigned tk = (i >> 1) * 8 + (ln >> 3);
#pragma unroll
    for (int k = 0; k < 4; ++k) { ra[i][k] = (unsigned)(uintptr_t)lds + (wid < 4 ? wid * 16384 + k * 4096 : P2_G2RING + (wid - 4) * 4096 + k * 16384) + tk * 256 + (((2 * (ln & 7) + (i & 1)) ^ tk) << 4); PIN(ra[i][k]); }
    sum[i] = f32x4{blo(xr[i >> 1][2 * (i & 1)]), bhi(xr[i >> 1][2 * (i & 1)]), blo(xr[i >> 1][2 * (i & 1) + 1]), bhi(xr[i >> 1][2 * (i & 1) + 1])};
    PIN(sum[i]);
  }
  PIN(vo);
  asm volatile("s_waitcnt lgkmcnt(0)\n\ts_barrier" ::: "memory");
  f32x4 part[4][4];
#pragma unroll
  for (int i = 0; i < 4; ++i)
#pragma unroll
    for (int k = 0; k < 4; ++k) part[i][k] = *(LAS const f32x4*)LP(ra[i][k]);
  SBAR();
#pragma unroll
  for (int i = 0; i < 4; ++i) sum[i] += (part[i][0] + part[i][1]) + (part[i][2] + part[i][3]);
  st16_sc1(mksrd(out, S * D * 2), vo, 0u, pack44(sum[0], sum[1], 1.f));
  st16_sc1(mksrd(out, S * D * 2), vo, 8u * D * 2u, pack44(sum[2], sum[3], 1.f));
  asm volatile(".Lfk_code_end:");
}

extern "C" void kernel_launch(void* const* d_in, const int* in_sizes, int n_in, void* d_out, int out_size, void* d_ws, size_t ws_size, hipStream_t stream) {
  (void)hipFuncSetAttribute((const void*)fused_kernel, hipFuncAttributeMaxDynamicSharedMemorySize, LDS_BYTES);
  fused_kernel<<<256, 512, LDS_BYTES, stream>>>((const u16*)d_in[0], (const u16*)d_in[1], (const u16*)d_in[2], (const u16*)d_in[4], (const u16*)d_in[5], (const u16*)d_in[6], (char*)d_ws, (u16*)d_out);
}
```

```cpp
#include <hip/hip_runtime.h>
#include <hip/hip_bf16.h>
#include <stdint.h>

typedef unsigned short u16;
typedef unsigned long long u64;
typedef short bf16x8 __attribute__((ext_vector_type(8)));
typedef short s16x4 __attribute__((ext_vector_type(4)));
typedef float f32x16 __attribute__((ext_vector_type(16)));
typedef float f32x4 __attribute__((ext_vector_type(4)));
typedef float f32x2 __attribute__((ext_vector_type(2)));
typedef unsigned u32x4 __attribute__((ext_vector_type(4)));
typedef unsigned u32x2 __attribute__((ext_vector_type(2)));
typedef __bf16 bf16x2_t __attribute__((ext_vector_type(2)));
typedef short v4i16_t __attribute__((ext_vector_type(4)));
typedef __amdgpu_buffer_rsrc_t srd_t;
typedef __attribute__((address_space(1))) u64 gu64;
#define LAS __attribute__((address_space(3)))
#define DI __device__ __forceinline__
#define SBAR() __builtin_amdgcn_sched_barrier(0)
#define PIN(x) asm volatile("" : "+v"(x))
#define NOP_ ((void)0)
#define LP(a) ((LAS const char*)(uintptr_t)(a))
#define MFMA32(a, b, c) __builtin_amdgcn_mfma_f32_32x32x16_bf16((a), (b), (c), 0, 0, 0)
#define MFMA16(a, b, c) __builtin_amdgcn_mfma_f32_16x16x32_bf16((a), (b), (c), 0, 0, 0)
#define EX2(v) __builtin_amdgcn_exp2f(v)
#define BF(x) __builtin_bit_cast(bf16x8, x)
#define RLX_AGENT __ATOMIC_RELAXED, __HIP_MEMORY_SCOPE_AGENT

constexpr int S = 2048, D = 1024, F = 3584;
constexpr int NKB = (F + D) / 32;
constexpr float LOG2E = 1.4426950408889634f;
constexpr float QSCALE = 0.17677669529663687f * LOG2E;

constexpr unsigned WS_FLG = 0;
constexpr unsigned FLG_F1 = 256, FLG_F2 = FLG_F1 + 2048, FLG_F3 = FLG_F2 + 2048, FLG_F4 = FLG_F3 + 2048;
constexpr unsigned FST = 16;
constexpr unsigned WS_XN = 2097152;
constexpr unsigned WS_Q = WS_XN + S * D * 2;
constexpr unsigned WS_KV = WS_Q + S * D * 2;
constexpr unsigned WS_X2 = WS_KV + S * D * 4;
constexpr unsigned WS_END = WS_X2 + S * (F + D) * 2;
constexpr int G1_XS = 32768, G1_WB = 65536, G1_WS = 40960;
constexpr int P2_G2RING = 65536;
constexpr int LDS_CTL = G1_WB + 2 * G1_WS, LDS_DUMP = LDS_CTL + 1024, LDS_BYTES = LDS_DUMP + 8192;
#define WPF 2
#define PFD 6u
constexpr unsigned SPIN_CAP = 1u << 20;

DI unsigned rfl(unsigned x) { return __builtin_amdgcn_readfirstlane(x); }
DI int lane_id() { int l; asm volatile("v_mbcnt_lo_u32_b32 %0, -1, 0\n\tv_mbcnt_hi_u32_b32 %0, -1, %0" : "=v"(l)); return l; }
DI unsigned cvtpk(float lo, float hi) { f32x2 v = {lo, hi}; bf16x2_t b = __builtin_convertvector(v, bf16x2_t); return __builtin_bit_cast(unsigned, b); }
DI float rbf(float f) { return __uint_as_float(cvtpk(f, f) << 16); }
DI float blo(unsigned u) { return __uint_as_float(u << 16); }
DI float bhi(unsigned u) { return __uint_as_float(u & 0xffff0000u); }
DI srd_t mksrd(const void* p, unsigned bytes) { return __builtin_amdgcn_make_buffer_rsrc((void*)p, 0, bytes, 0x00020000); }
template <bool SC1> DI void dma16(srd_t srd, unsigned voff, unsigned soff, unsigned m0v) {
  if (SC1) asm volatile("s_mov_b32 m0, %0\n\ts_nop 0\n\tbuffer_load_dwordx4 %1, %2, %3 offen sc1 lds" ::"s"(m0v), "v"(voff), "s"(srd), "s"(soff) : "memory");
  else asm volatile("s_mov_b32 m0, %0\n\ts_nop 0\n\tbuffer_load_dwordx4 %1, %2, %3 offen lds" ::"s"(m0v), "v"(voff), "s"(srd), "s"(soff) : "memory");
}
template <bool SC1, class T> DI void ld16(T& d, srd_t srd, unsigned voff, unsigned soff, int imm) {
  if (SC1) asm volatile("buffer_load_dwordx4 %0, %1, %2, %3 offen offset:%4 sc1" : "=v"(d) : "v"(voff), "s"(srd), "s"(soff), "i"(imm) : "memory");
  else asm volatile("buffer_load_dwordx4 %0, %1, %2, %3 offen offset:%4" : "=v"(d) : "v"(voff), "s"(srd), "s"(soff), "i"(imm) : "memory");
}
template <class T> DI void ld16_inplace(T& d, srd_t srd, unsigned voff, unsigned soff, int imm) {
  asm volatile("buffer_load_dwordx4 %0, %1, %2, %3 offen offset:%4 sc1" : "+v"(d) : "v"(voff), "s"(srd), "s"(soff), "i"(imm) : "memory");
}
DI void st16_sc1(srd_t srd, unsigned voff, unsigned soff, u32x4 v) {
  asm volatile("buffer_store_dwordx4 %0, %1, %2, %3 offen sc1\n\ts_nop 1" ::"v"(v), "v"(voff), "s"(srd), "s"(soff) : "memory");
}
DI s16x4 ldstr(LAS const char* p) { return __builtin_bit_cast(s16x4, __builtin_amdgcn_ds_read_tr16_b64_v4i16((LAS v4i16_t*)p)); }
DI bf16x8 lds128(LAS const char* p) { return *(LAS const bf16x8*)p; }
DI bf16x8 cat8(s16x4 lo, s16x4 hi) { return __builtin_shufflevector(lo, hi, 0, 1, 2, 3, 4, 5, 6, 7); }
DI u32x4 pack8(const f32x16& v, int s, float sc) {
  u32x4 w;
  w[0] = cvtpk(v[8 * s + 0] * sc, v[8 * s + 1] * sc); w[1] = cvtpk(v[8 * s + 2] * sc, v[8 * s + 3] * sc);
  w[2] = cvtpk(v[8 * s + 4] * sc, v[8 * s + 5] * sc); w[3] = cvtpk(v[8 * s + 6] * sc, v[8 * s + 7] * sc);
  return w;
}
DI u32x4 pack44(const f32x4& a, const f32x4& b, float sc) {
  u32x4 w; w[0] = cvtpk(a[0] * sc, a[1] * sc); w[1] = cvtpk(a[2] * sc, a[3] * sc); w[2] = cvtpk(b[0] * sc, b[1] * sc); w[3] = cvtpk(b[2] * sc, b[3] * sc); return w;
}
DI void mfma16a(f32x4& c, const bf16x8& a, const bf16x8& b) { asm volatile("v_mfma_f32_16x16x32_bf16 %0, %1, %2, %0" : "+v"(c) : "v"(a), "v"(b)); }
template <int N> DI void mfma16w(f32x4& c, const bf16x8& a, const bf16x8& b) { asm volatile("s_waitcnt vmcnt(%3)\n\tv_mfma_f32_16x16x32_bf16 %0, %1, %2, %0" : "+v"(c) : "v"(a), "v"(b), "i"(N)); }
template <int CTRL, int RM> DI float dppadd(float v) { return v + __builtin_bit_cast(float, __builtin_amdgcn_update_dpp(0, __builtin_bit_cast(int, v), CTRL, RM, 0xf, true)); }
DI float wave_sum(float v) {
  v = dppadd<0x111, 0xf>(v); v = dppadd<0x112, 0xf>(v); v = dppadd<0x114, 0xf>(v); v = dppadd<0x118, 0xf>(v);
  v = dppadd<0x142, 0xa>(v); v = dppadd<0x143, 0xc>(v);
  return __builtin_bit_cast(float, __builtin_amdgcn_readlane(__builtin_bit_cast(int, v), 63));
}
DI void publish(char* ws, unsigned idx, u64 tag, int lane) {
  const u32x2 tv = {(unsigned)tag, (unsigned)(tag >> 32)};
  asm volatile("buffer_store_dwordx2 %0, %1, %2, %3 offen sc1" ::"v"(tv), "v"(lane == 0 ? 0u : 0x7ff00000u), "s"(mksrd(ws + WS_FLG, WS_XN)), "s"(idx * (8u * FST)) : "memory");
}
DI void publish_last(char* ws, unsigned cnt_addr, unsigned n, unsigned idx, u64 tag, int lane) {
  unsigned old;
  asm volatile("ds_add_rtn_u32 %0, %1, %2\n\ts_waitcnt lgkmcnt(0)" : "=v"(old) : "v"(cnt_addr), "v"(lane == 0 ? 1u : 0u) : "memory");
  if (rfl(old) == n - 1) publish(ws, idx, tag, lane);
}
DI void await(char* ws, unsigned idx, u64 tag) {
  gu64* p = (gu64*)(ws + WS_FLG) + idx * FST;
  for (unsigned spins = 0; spins < SPIN_CAP; ++spins) {
    if (__all(__hip_atomic_load(p, RLX_AGENT) == tag)) break;
    __builtin_amdgcn_s_sleep(1);
  }
  asm volatile("" ::: "memory");
}

#define STW wid
#define STAMP(i) do {} while (0)

DI unsigned g2_vw(int lane, int cg) { const unsigned l3 = lane >> 3, l7 = lane & 7; return l3 * 2048u + ((l7 ^ (((l3 >> 1) & 3) << 1)) << 4) + cg * 128u; }

DI void ln_row(const u16* x, char* ws, int row, srd_t scode, unsigned code_off, unsigned dump) {
  const int lane = lane_id();
  const srd_t sxi = mksrd(x, S * D * 2), sx = mksrd(ws + WS_XN, S * D * 2);
  u32x4 a, b;
  ld16<false>(a, sxi, lane * 16u, row * 2048u, 0); ld16<false>(b, sxi, lane * 16u, row * 2048u, 1024);
  dma16<false>(scode, lane * 16u, code_off, dump);
  asm volatile("s_waitcnt vmcnt(1)" : "+v"(a), "+v"(b));
  f32x2 v[8];
#pragma unroll
  for (int i = 0; i < 4; ++i) { v[i] = f32x2{blo(a[i]), bhi(a[i])}; v[4 + i] = f32x2{blo(b[i]), bhi(b[i])}; }
  const f32x2 s2 = ((v[0] + v[1]) + (v[2] + v[3])) + ((v[4] + v[5]) + (v[6] + v[7]));
  const float mean = wave_sum(s2[0] + s2[1]) * (1.f / D);
  f32x2 q[4];
#pragma unroll
  for (int i = 0; i < 4; ++i) { const f32x2 d0 = v[i] - mean, d1 = v[4 + i] - mean; q[i] = __builtin_elementwise_fma(d1, d1, d0 * d0); }
  const f32x2 q2 = (q[0] + q[1]) + (q[2] + q[3]);
  const float mb = rbf(mean);
  unsigned dn[8];
#pragma unroll
  for (int i = 0; i < 8; ++i) { const f32x2 d = v[i] - mb; dn[i] = cvtpk(d[0], d[1]); }
  const float vb = rbf(wave_sum(q2[0] + q2[1]) * (1.f / D));
  const float inv = __builtin_amdgcn_rcpf(rbf(__builtin_amdgcn_sqrtf(rbf(vb + 1e-5f))));
  u32x4 oa, ob;
#pragma unroll
  for (int i = 0; i < 4; ++i) { oa[i] = cvtpk(blo(dn[i]) * inv, bhi(dn[i]) * inv); ob[i] = cvtpk(blo(dn[4 + i]) * inv, bhi(dn[4 + i]) * inv); }
  st16_sc1(sx, lane * 16u, row * 2048u, oa); st16_sc1(sx, lane * 16u + 1024u, row * 2048u, ob);
}

DI f32x2 glu2(float g0, float g1, float u0, float u1) { return f32x2{u0 * __builtin_amdgcn_rcpf(1.f + EX2(g0 * -LOG2E)), u1 * __builtin_amdgcn_rcpf(1.f + EX2(g1 * -LOG2E))}; }
DI unsigned h_base(int mt, int pair, int wr, int wc) { return WS_X2 + ((mt * 2 + (wr >> 1)) * NKB + 2 * pair + wc) * 8192u + (wr & 1) * 1024u; }
DI int unit_owner(int a) { return a >= 32 ? a - 32 : (a < 8 ? 16 + a : 24 + (a - 8) / 3); }
DI unsigned att_dep(int head, int qb, int lane) {
  const int jq = unit_owner(head >> 1), jk = unit_owner(16 + (head >> 1)), jv = unit_owner(32 + (head >> 1));
  unsigned ikv = (lane & 7) * 32 + ((lane & 8) ? jk : jv); PIN(ikv);
  return FLG_F2 + (lane < 16 ? ikv : qb * 32 + jq);
}
template <int KIND>
DI void g1_body(int head, const u16* wo, const u16* wd, int cg, const u16* wqkv, const u16* wg, const u16* wu, char* ws, LAS char* lds, int wid, int mt, int j, srd_t scode, u64 tag, f32x4 (&accb)[4][4], u32x4 (&ha)[4]) {
  const int lane = lane_id(), wr = wid & 3, wc = wid >> 2;
  constexpr bool SWAP0 = KIND == 0, isY = KIND == 2;
  const int a0 = KIND == 0 ? 32 + j : (KIND == 1 ? j - 16 : 3 * j - 64);
  const srd_t sx = mksrd(ws + WS_XN, S * D * 2), sq = mksrd(wqkv, D * 3 * D * 2), sg = mksrd(wg, D * F * 2), su = mksrd(wu, D * F * 2);
  const srd_t sa = isY ? sq : sg, sb = isY ? sq : su;
  const unsigned ld1 = isY ? 6144u : 7168u;
  const unsigned c0 = a0 * 128u, c1 = isY ? c0 + 128u : j * 256u, c2 = isY ? c0 + 256u : j * 256u, c3 = isY ? (24 + j) * 128u : j * 256u + 128u;
  const unsigned l3 = lane >> 3, l7 = lane & 7, l4 = lane >> 4;
  const unsigned vx0 = l3 * 2048u + ((l7 ^ l4) << 4), vx1 = vx0 ^ 64u;
  const unsigned wsw = (l7 ^ (((l4 & 1) | ((wid & 1) << 1)) << 1)) << 4;
  const unsigned vw0 = l3 * 6144u + wsw, vw2 = l3 * 7168u + wsw;
  const unsigned vw1 = isY ? vw0 : vw2;
  const unsigned ldsb = (unsigned)(uintptr_t)lds;
  unsigned sox = (unsigned)(mt * 256 + wid * 32) * 2048u;
  unsigned so0 = wid * 8 * 6144u + c0, so1 = wid * 8 * ld1, so2 = wid * 8 * 7168u + c3;
  const unsigned m0x = ldsb + wid * 4096u, m0w = ldsb + G1_WB + wid * 1024u;
#define G1_DX(i, st) dma16<true>(sx, ((i) & 1) ? vx1 : vx0, sox + (i) * 16384u, m0x + (st) * G1_XS + (i) * 1024u)
#define G1_DW(i, st)                                                                             \
  do {                                                                                           \
    if ((i) == 0) dma16<false>(sq, vw0, so0, m0w + (st) * G1_WS);                                 \
    if ((i) == 1) dma16<false>(sa, vw1, so1 + c1, m0w + (st) * G1_WS + 8192u);                    \
    if ((i) == 2) dma16<false>(sb, vw1, so1 + c2, m0w + (st) * G1_WS + 16384u);                   \
    if ((i) == 3) dma16<false>(sg, vw2, so2, m0w + (st) * G1_WS + 24576u);                        \
    if ((i) == 4) dma16<false>(su, vw2, so2, m0w + (st) * G1_WS + 32768u);                        \
  } while (0)
#define G1_ADVX() do { sox += 128u; } while (0)
#define G1_ADVW() do { so0 += 64u * 6144u; so1 += 64u * ld1; so2 += 64u * 7168u; } while (0)
#define G1_CODE() dma16<false>(scode, vx0, (unsigned)(mt * 16384 + ((j >> 3) & 1) * 1024 + wid * 128), ldsb + LDS_DUMP + wid * 1024u)
  f32x4 acc[4][10];
  G1_DW(0, 0); G1_DW(1, 0); G1_DW(2, 0); G1_DW(3, 0); G1_DW(4, 0); G1_ADVW();
#pragma unroll
  for (int m = 0; m < 4; ++m)
#pragma unroll
    for (int n = 0; n < 10; ++n) { acc[m][n] = f32x4{}; PIN(acc[m][n]); }
  await(ws, FLG_F1 + mt * 32 + wid * 4 + (lane & 3), tag);
  STAMP(2);
  G1_DX(0, 0); G1_DX(1, 0); G1_DX(2, 0); G1_DX(3, 0); G1_ADVX();
  G1_DX(0, 1); G1_DX(1, 1); G1_DX(2, 1); G1_DX(3, 1); G1_ADVX();
  G1_DW(0, 1); G1_DW(1, 1); G1_DW(2, 1); G1_DW(3, 1); G1_DW(4, 1); G1_ADVW();
  const int t16 = lane & 15, g = lane >> 4, q4 = t16 >> 2, p4 = lane & 3;
  unsigned xa0 = ldsb + (wr * 64 + t16) * 128 + ((g ^ ((t16 >> 1) & 7)) << 4), xa1 = xa0 ^ 64u;
  unsigned wa00 = ldsb + G1_WB + (8 * g + q4) * 128 + (((2 * wc) ^ (((q4 >> 1) & 1) | ((g & 1) << 1))) << 5) + p4 * 8;
  unsigned wa01 = wa00 ^ 32u, wa10 = wa00 + G1_WS, wa11 = wa01 + G1_WS;
  asm volatile("" : "+v"(xa0), "+v"(xa1), "+v"(wa00), "+v"(wa01), "+v"(wa10), "+v"(wa11));
  bf16x8 xf[2][4];
  s16x4 wl[10], wh[10];
#define G1_RX(st, s, m) xf[s][m] = lds128(LP((s) ? xa1 : xa0) + (st) * G1_XS + (m) * 2048)
#define G1_RW(st, s, n)                                                                                      \
  do {                                                                                                       \
    LAS const char* p_ = LP((st) ? (((n) & 1) ? wa11 : wa10) : (((n) & 1) ? wa01 : wa00)) + ((n) >> 1) * 8192 + (s) * 4096; \
    wl[n] = ldstr(p_); wh[n] = ldstr(p_ + 512);                                                              \
  } while (0)
#define G1_MM(s, n, m)                                                                                       \
  do {                                                                                                       \
    if (SWAP0 && (n) < 2) mfma16a(acc[m][n], xf[s][m], cat8(wl[n], wh[n]));                                   \
    else mfma16a(acc[m][n], cat8(wl[n], wh[n]), xf[s][m]);                                                    \
  } while (0)
#define G1_GRP(s, n, A, B, C) do { G1_MM(s, n, 0); SBAR(); A; SBAR(); G1_MM(s, n, 1); SBAR(); B; SBAR(); G1_MM(s, n, 2); SBAR(); C; SBAR(); G1_MM(s, n, 3); SBAR(); } while (0)
#define G1_WAITBAR() G1_WAITN(0)
#define G1_WAITN(N) asm volatile("s_waitcnt vmcnt(%0) lgkmcnt(0)\n\ts_barrier" ::"i"(N) : "memory")
#define G1_HEAD(st)                                                                                          \
  do {                                                                                                       \
    G1_GRP(0, 0, G1_RW(st, 0, 9), NOP_, NOP_);                                                               \
    G1_GRP(0, 1, G1_RW(st, 1, 0), G1_RX(st, 1, 0), NOP_);                                                    \
    G1_GRP(0, 2, G1_RW(st, 1, 1), G1_RX(st, 1, 1), NOP_);                                                    \
    G1_GRP(0, 3, G1_RW(st, 1, 2), G1_RX(st, 1, 2), NOP_);                                                    \
    G1_GRP(0, 4, G1_RW(st, 1, 3), G1_RX(st, 1, 3), NOP_);                                                    \
    G1_GRP(0, 5, G1_RW(st, 1, 4), NOP_, NOP_);                                                               \
    G1_GRP(0, 6, G1_RW(st, 1, 5), NOP_, NOP_);                                                               \
    G1_GRP(0, 7, G1_RW(st, 1, 6), NOP_, NOP_);                                                               \
    G1_GRP(0, 8, G1_RW(st, 1, 7), NOP_, NOP_);                                                               \
    G1_GRP(0, 9, G1_RW(st, 1, 8), NOP_, NOP_);                                                               \
    G1_GRP(1, 0, G1_RW(st, 1, 9), NOP_, NOP_);                                                               \
    G1_GRP(1, 1, NOP_, NOP_, NOP_);                                                                          \
    G1_GRP(1, 2, NOP_, NOP_, NOP_);                                                                          \
    G1_WAITN((st) ? NWARM : 0);         \
  } while (0)
#define G1_TILE(st, DM)                                                                                      \
  do {                                                                                                       \
    G1_HEAD(st);                                                                                             \
    G1_GRP(1, 3, G1_RX(st ^ 1, 0, 0), G1_RW(st ^ 1, 0, 0), if (DM) G1_DX(0, st));                             \
    G1_GRP(1, 4, G1_RX(st ^ 1, 0, 1), G1_RW(st ^ 1, 0, 1), if (DM) G1_DW(0, st));                             \
    G1_GRP(1, 5, G1_RX(st ^ 1, 0, 2), G1_RW(st ^ 1, 0, 2), if (DM) G1_DX(1, st));                             \
    G1_GRP(1, 6, G1_RX(st ^ 1, 0, 3), G1_RW(st ^ 1, 0, 3), if (DM) G1_DW(1, st));                             \
    G1_GRP(1, 7, G1_RW(st ^ 1, 0, 4), G1_RW(st ^ 1, 0, 5), if (DM) { G1_DX(2, st); G1_DW(2, st); });          \
    G1_GRP(1, 8, G1_RW(st ^ 1, 0, 6), G1_RW(st ^ 1, 0, 7), if (DM) { G1_DX(3, st); G1_DW(3, st); });          \
    G1_GRP(1, 9, G1_RW(st ^ 1, 0, 8), NOP_, if (DM) { G1_DW(4, st); G1_ADVX(); G1_ADVW(); });                 \
  } while (0)
  asm volatile("s_waitcnt vmcnt(9)\n\ts_barrier" ::: "memory");
  G1_RX(0, 0, 0); G1_RW(0, 0, 0); G1_RX(0, 0, 1); G1_RX(0, 0, 2); G1_RX(0, 0, 3);
  G1_RW(0, 0, 1); G1_RW(0, 0, 2); G1_RW(0, 0, 3); G1_RW(0, 0, 4); G1_RW(0, 0, 5); G1_RW(0, 0, 6); G1_RW(0, 0, 7); G1_RW(0, 0, 8);
  SBAR();
  STAMP(3);
  constexpr int NWARM = 1 + (WPF & 1) + ((WPF >> 1) & 1);
  const srd_t sp = wid == 0 ? sq : (wid == 1 ? sa : (wid == 2 ? sb : (wid == 3 ? sg : su)));
  const unsigned ldp = wid == 0 ? 6144u : (wid < 3 ? ld1 : 7168u), cp = wid == 0 ? c0 : (wid == 1 ? c1 : (wid == 2 ? c2 : (wid < 5 ? c3 : 0x7ff00000u)));
  for (int t = 0; t < 14; t += 2) {
    G1_TILE(0, true);
    G1_CODE();
    if (WPF & 1) { const unsigned l = lane_id(), id = (mt * 32 + j) * 8 + wid, ln = (t >> 1) * 16384u + id * 8u + (l & 7);
      asm volatile("s_mov_b32 m0, %0\n\ts_nop 0\n\tbuffer_load_dword %1, %2, 0 offen lds" ::"s"(ldsb + LDS_DUMP + wid * 1024u), "v"(l < 8 ? (t < 8 ? ln : ln - 65536u) * 128u : 0x7ff00000u), "s"(t < 8 ? mksrd(wd, F * D * 2) : mksrd(wo, D * D * 2)) : "memory"); }
    if (WPF & 2) { const unsigned l = lane_id(), tl = (t == 0 ? 3u : t + PFD) + (l >> 3);
      asm volatile("s_mov_b32 m0, %0\n\ts_nop 0\n\tbuffer_load_dword %1, %2, %3 offen lds" ::"s"(ldsb + LDS_DUMP + wid * 1024u), "v"(tl < t + PFD + 2u ? (tl * 64u + mt * 8u + (l & 7)) * ldp : 0x7ff00000u), "s"(sp), "s"(cp) : "memory"); }
    G1_TILE(1, true);
  }
  STAMP(4);
  {
    const srd_t sw = mksrd(ws, WS_END);
    unsigned vq, vk;
    auto store_addr = [&]() { const unsigned l = lane_id(); vq = (l >> 4) * 512u + (l & 15) * 16u; vk = (l >> 4) * 512u + (2 * (l & 8) + (l & 7)) * 16u; };
    const unsigned T32 = mt * 8 + wr * 2;
    auto store_u = [&](int u, int i) {
      const int a = a0 + u; const unsigned head = 2 * (a & 15) + wc; const bool isq = a < 16;
      if (SWAP0) st16_sc1(sw, vk, WS_KV + (head * 64 + T32 + (i >> 1)) * 4096u + 2048u + (i & 1) * 128u, pack44(acc[2 * (i >> 1)][i & 1], acc[2 * (i >> 1) + 1][i & 1], 1.f));
      else st16_sc1(sw, isq ? vq : vk, isq ? WS_Q + (head * 64 + T32 + (i >> 1)) * 2048u + (i & 1) * 256u : WS_KV + (head * 64 + T32 + (i >> 1)) * 4096u + (i & 1) * 128u,
                    pack44(acc[i][2 * u], acc[i][2 * u + 1], isq ? QSCALE : 1.f));
    };
#define G1_HA(k) do { if (!isY) { const f32x2 h_ = glu2(acc[(k) >> 2][2 + (((k) >> 1) & 1)][2 * ((k) & 1)], acc[(k) >> 2][2 + (((k) >> 1) & 1)][2 * ((k) & 1) + 1], acc[(k) >> 2][4 + (((k) >> 1) & 1)][2 * ((k) & 1)], acc[(k) >> 2][4 + (((k) >> 1) & 1)][2 * ((k) & 1) + 1]); \
      unsigned w_ = cvtpk(h_[0], h_[1]); PIN(w_); ha[(k) >> 2][(k) & 3] = w_; } } while (0)
#define G1_SY(u, i) do { if (isY) store_u(u, i); } while (0)
#define G1_RD(d, st, s, n)                                                                                   \
  do {                                                                                                       \
    LAS const char* p_ = LP((st) ? (((n) & 1) ? wa11 : wa10) : (((n) & 1) ? wa01 : wa00)) + ((n) >> 1) * 8192 + (s) * 4096; \
    wl[d] = ldstr(p_); wh[d] = ldstr(p_ + 512);                                                              \
  } while (0)
#define G1_MD(d, s, n, m)                                                                                    \
  do {                                                                                                       \
    if (SWAP0 && (n) < 2) mfma16a(acc[m][n], xf[s][m], cat8(wl[d], wh[d]));                                   \
    else mfma16a(acc[m][n], cat8(wl[d], wh[d]), xf[s][m]);                                                    \
  } while (0)
#define G1_GD(d, s, n, A, B, C) do { G1_MD(d, s, n, 0); SBAR(); A; SBAR(); G1_MD(d, s, n, 1); SBAR(); B; SBAR(); G1_MD(d, s, n, 2); SBAR(); C; SBAR(); G1_MD(d, s, n, 3); SBAR(); } while (0)
    G1_GD(0, 0, 0, G1_RD(9, 0, 1, 0), G1_RX(0, 1, 0), NOP_);
    G1_GD(1, 0, 1, G1_RD(0, 0, 1, 1), G1_RX(0, 1, 1), NOP_);
    G1_GD(2, 0, 2, G1_RD(1, 0, 1, 2), G1_RX(0, 1, 2), NOP_);
    G1_GD(3, 0, 3, G1_RD(2, 0, 1, 3), G1_RX(0, 1, 3), NOP_);
    G1_GD(4, 0, 4, G1_RD(3, 0, 1, 4), NOP_, NOP_);
    G1_GD(5, 0, 5, G1_RD(4, 0, 1, 5), NOP_, NOP_);
    G1_GD(9, 1, 0, NOP_, NOP_, NOP_);
    asm volatile("s_waitcnt vmcnt(0)\n\ts_barrier" ::: "memory");
    G1_GD(0, 1, 1, G1_RX(1, 0, 0), G1_RD(5, 1, 0, 0), G1_RX(1, 0, 1));
    G1_GD(1, 1, 2, G1_RX(1, 0, 2), G1_RD(6, 1, 0, 1), G1_RX(1, 0, 3));
    G1_GD(2, 1, 3, G1_RD(7, 1, 1, 0), G1_RD(8, 1, 1, 1), NOP_);
    G1_GD(3, 1, 4, G1_RD(9, 1, 0, 2), G1_RD(0, 1, 0, 3), NOP_);
    G1_GD(4, 1, 5, G1_RD(1, 1, 1, 2), G1_RD(2, 1, 1, 3), NOP_);
    G1_GD(5, 0, 0, { G1_RX(1, 1, 0); G1_RX(1, 1, 1); }, { G1_RX(1, 1, 2); G1_RX(1, 1, 3); }, G1_RD(3, 1, 0, 4));
    G1_GD(6, 0, 1, G1_RD(4, 1, 0, 5), NOP_, NOP_);
    G1_GD(9, 0, 2, G1_RD(5, 1, 1, 4), NOP_, NOP_);
    G1_GD(7, 1, 0, G1_RD(6, 1, 1, 5), NOP_, NOP_);
    G1_GD(8, 1, 1, G1_RD(9, 1, 0, 6), NOP_, NOP_);
    G1_GD(0, 0, 3, G1_RD(7, 1, 0, 7), NOP_, NOP_);
    G1_GD(1, 1, 2, G1_RD(8, 1, 0, 8), NOP_, store_addr());
    G1_GD(2, 1, 3, G1_RD(0, 1, 0, 9), store_u(0, 0), store_u(0, 1));
    G1_GD(3, 0, 4, G1_RD(1, 1, 1, 6), store_u(0, 2), store_u(0, 3));
    G1_GD(4, 0, 5, G1_RD(2, 1, 1, 7), NOP_, NOP_);
    G1_GD(5, 1, 4, G1_RD(3, 1, 1, 8), G1_SY(1, 0), G1_SY(1, 1));
    G1_GD(6, 1, 5, G1_RD(4, 1, 1, 9), G1_SY(1, 2), G1_SY(1, 3));
    G1_GD(9, 0, 6, G1_RD(5, 0, 0, 6), NOP_, NOP_);
    G1_GD(7, 0, 7, G1_RD(6, 0, 0, 7), NOP_, G1_HA(0));
    G1_GD(8, 0, 8, G1_RD(9, 0, 0, 8), { G1_SY(2, 0); G1_HA(1); }, G1_SY(2, 1));
    G1_GD(0, 0, 9, G1_RD(7, 0, 0, 9), { G1_SY(2, 2); G1_HA(2); }, G1_SY(2, 3));
    G1_GD(1, 1, 6, { G1_RX(0, 0, 0); G1_RX(0, 0, 1); }, G1_RD(8, 0, 1, 6), G1_HA(3));
    G1_GD(2, 1, 7, { G1_RX(0, 0, 2); G1_RX(0, 0, 3); }, G1_RD(0, 0, 1, 7), G1_HA(4));
    G1_GD(3, 1, 8, G1_RD(1, 0, 1, 8), G1_HA(5), G1_HA(6));
    G1_GD(4, 1, 9, G1_RD(2, 0, 1, 9), G1_HA(7), NOP_);
    G1_GD(5, 0, 6, { G1_RX(0, 1, 0); G1_RX(0, 1, 1); }, { G1_RX(0, 1, 2); G1_RX(0, 1, 3); }, G1_HA(8));
    G1_GD(6, 0, 7, G1_HA(9), NOP_, NOP_);
    G1_GD(9, 0, 8, G1_HA(10), NOP_, NOP_);
    G1_GD(7, 0, 9, G1_HA(11), NOP_, NOP_);
    G1_WAITBAR();
    G1_GD(8, 1, 6, if (wid == 7) publish(ws, FLG_F2 + mt * 32 + j, tag, lane_id()), G1_HA(12), NOP_);
#define G1_PF(k) do { if (wc) { const unsigned vw_ = g2_vw(lane_id(), cg); _Pragma("unroll") for (int i = 4 * (k); i < 4 * (k) + 8; ++i) dma16<false>(mksrd(wd, F * D * 2), vw_, (unsigned)(28 * wr) * 65536u + i * 16384u, ldsb + P2_G2RING + wr * 16384u + i * 1024u); } } while (0)
    G1_GD(0, 1, 7, G1_HA(13), G1_PF(0), NOP_);
    G1_GD(1, 1, 8, G1_HA(14), G1_PF(2), NOP_);
    G1_GD(2, 1, 9, G1_HA(15), NOP_, NOP_);
    if (wc == 0) { __builtin_amdgcn_s_setprio(1); dma16<true>(mksrd(ws, WS_XN), att_dep(head, mt, lane_id()) * (8u * FST), 0u, ldsb + LDS_DUMP + wid * 1024u); }
  }
  asm volatile("s_nop 7" ::: "memory"); SBAR();
#pragma unroll
  for (int m = 0; m < 4; ++m)
#pragma unroll
    for (int n = 0; n < 4; ++n) accb[m][n] = acc[m][6 + n];
  STAMP(5);
}

DI void att_slow(const char* kv, const bf16x8 (&qf)[2][2], f32x16 (&o)[2], float (&linv)[2], int lane) {
  __builtin_amdgcn_fence(__ATOMIC_ACQUIRE, "agent");
#pragma unroll
  for (int m = 0; m < 2; ++m) {
    f32x16 oo = f32x16{}; float mref = -INFINITY, l = 0.f;
    for (int kt = 0; kt < 64; ++kt) {
      const char* p = kv + kt * 4096 + lane * 16;
      const bf16x8 k0 = *(const bf16x8*)p, k1 = *(const bf16x8*)(p + 1024), v0 = *(const bf16x8*)(p + 2048), v1 = *(const bf16x8*)(p + 3072);
      f32x16 s = MFMA32(k0, qf[m][0], f32x16{});
      s = MFMA32(k1, qf[m][1], s);
      float mx = s[0];
#pragma unroll
      for (int r = 1; r < 16; ++r) mx = fmaxf(mx, s[r]);
      mx = fmaxf(fmaxf(mx, __shfl_xor(mx, 32)), mref);
      const float f = EX2(mref - mx);
      mref = mx;
      float sum = 0.f;
#pragma unroll
      for (int r = 0; r < 16; ++r) { s[r] = EX2(s[r] - mref); sum += s[r]; oo[r] *= f; }
      l = l * f + sum;
      oo = MFMA32(v0, BF(pack8(s, 0, 1.f)), oo);
      oo = MFMA32(v1, BF(pack8(s, 1, 1.f)), oo);
    }
    o[m] = oo; linv[m] = 1.f / (l + __shfl_xor(l, 32));
  }
}
DI void lds_meet(unsigned v, unsigned need, unsigned addr) {
  unsigned left = SPIN_CAP;
  asm volatile("1:\n\tv_cmp_le_u32 vcc, %2, %0\n\ts_cmp_eq_u64 vcc, exec\n\ts_cbranch_scc1 2f\n\ts_sleep 1\n\tds_read_b32 %0, %3\n\ts_sub_u32 %1, %1, 1\n\ts_waitcnt lgkmcnt(0)\n\ts_cbranch_scc0 1b\n2:"
               : "+v"(v), "+s"(left) : "s"(need), "v"(addr) : "vcc", "scc", "memory");
}
DI void lds_meet_fast(unsigned v, unsigned need, unsigned addr) {
  unsigned left;
  asm volatile("v_cmp_le_u32 vcc, %2, %0\n\ts_cmp_eq_u64 vcc, exec\n\ts_cbranch_scc0 3f\n2:\n\t.subsection 1\n3:\n\ts_mov_b32 %1, 0x100000\n4:\n\ts_sleep 1\n\tds_read_b32 %0, %3\n\ts_waitcnt lgkmcnt(0)\n\t"
               "v_cmp_le_u32 vcc, %2, %0\n\ts_cmp_eq_u64 vcc, exec\n\ts_cbranch_scc1 2b\n\ts_sub_u32 %1, %1, 1\n\ts_cbranch_scc0 4b\n\ts_branch 2b\n\t.subsection 0"
               : "+v"(v), "=&s"(left) : "s"(need), "v"(addr) : "vcc", "scc", "memory");
}
DI void glu_pack(const f32x4 (&a)[4][4], u32x4 (&hp)[4]) {
#pragma unroll
  for (int m = 0; m < 4; ++m) {
#pragma unroll
    for (int k = 0; k < 4; ++k) { const f32x2 h = glu2(a[m][k >> 1][2 * (k & 1)], a[m][k >> 1][2 * (k & 1) + 1], a[m][2 + (k >> 1)][2 * (k & 1)], a[m][2 + (k >> 1)][2 * (k & 1) + 1]); hp[m][k] = cvtpk(h[0], h[1]); }
    PIN(hp[m]);
  }
}

DI void dma16x4_sc1(srd_t srd, unsigned voff, unsigned soff, unsigned m0v) {
  asm volatile("s_mov_b32 m0, %0\n\ts_nop 0\n\tbuffer_load_dwordx4 %1, %2, %3 offen sc1 lds\n\tbuffer_load_dwordx4 %1, %2, %3 offen offset:1024 sc1 lds\n\t"
               "buffer_load_dwordx4 %1, %2, %3 offen offset:2048 sc1 lds\n\tbuffer_load_dwordx4 %1, %2, %3 offen offset:3072 sc1 lds" ::"s"(m0v), "v"(voff), "s"(srd), "s"(soff) : "memory");
}
DI void att_role(const u16* wo, char* ws, LAS char* lds, int head, int qb, int tg, int cg, int wid, u64 tag, const u32x4 (&hp)[4], const u32x4 (&ha)[4]) {
  const int tz = lane_id();
  const int lane = tz & 63;
  const char* kvg = ws + WS_KV + (size_t)head * 262144;
  const srd_t skv = mksrd(kvg, 262144), sw = mksrd(ws, WS_END);
  const unsigned ldsb = (unsigned)(uintptr_t)lds;
  const unsigned voff = lane * 16u;
  volatile LAS unsigned* ctl = (volatile LAS unsigned*)(lds + LDS_CTL);
  const unsigned ctla = ldsb + LDS_CTL + (lane & 7) * 4u;
  {
#pragma unroll
    for (int i = 0; i < 4; ++i) { *(LAS u32x4*)(lds + 131072 + wid * 4096 + i * 1024 + lane * 16) = hp[i]; *(LAS u32x4*)(lds + 49152 + wid * 4096 + i * 1024 + lane * 16) = ha[i]; }
    asm volatile("s_waitcnt lgkmcnt(0)" ::: "memory");
    ctl[12 + wid] = 1u;
  }
#define AT_ARRIVE(k) ctl[wid] = (unsigned)((k) + 2)
#define AT_MEET(v, k) lds_meet(v, (unsigned)((k) + 2), ctla)
  f32x16 o[2] = {f32x16{}, f32x16{}};
  f32x4 ls[2] = {f32x4{}, f32x4{}};
  bf16x8 sel;
  { const short one = ((lane & 1) == ((lane >> 4) & 1) && (lane & 2) == 0) ? (short)0x3f80 : (short)0;
#pragma unroll
    for (int e = 0; e < 8; ++e) sel[e] = one; }
  bf16x8 kf[2][2], vf[2][2], qf[2][2];
  f32x16 sa, sb, negm[2];
  u32x4 pa0, pa1, pb0 = u32x4{}, pb1 = u32x4{};
  vf[1][0] = bf16x8{}; vf[1][1] = bf16x8{};
  PIN(sel); PIN(o[0]); PIN(o[1]); PIN(ls[0]); PIN(ls[1]); PIN(pb0); PIN(pb1); PIN(vf[1][0]); PIN(vf[1][1]);
  const unsigned kp = ldsb + lane * 16u;
  {
    asm volatile("s_waitcnt vmcnt(0)" ::: "memory");
    const u32x2 f = *(volatile LAS const u32x2*)(lds + LDS_DUMP + wid * 1024 + lane * 16);
    if (!__all(f[0] == (unsigned)tag && f[1] == (unsigned)(tag >> 32))) await(ws, att_dep(head, qb, lane), tag);
  }
  STAMP(6);
  dma16x4_sc1(skv, voff, wid * 4096u, ldsb + wid * 4096u);
  dma16x4_sc1(sw, voff, WS_Q + (head * 64 + qb * 8 + wid * 2) * 2048u, ldsb + 32768u + wid * 4096u);
  dma16x4_sc1(skv, voff, 16384u + wid * 4096u, ldsb + 16384u + wid * 4096u);
  asm volatile("s_waitcnt vmcnt(0)" ::: "memory");
  AT_ARRIVE(-1);
#pragma unroll
  for (int i = 0; i < 4; ++i) qf[i >> 1][i & 1] = lds128(LP(kp) + 32768 + wid * 4096 + i * 1024);
  asm volatile("" : "+v"(qf[0][0]), "+v"(qf[0][1]), "+v"(qf[1][0]), "+v"(qf[1][1]));
  lds_meet(ctl[lane & 3], 1u, ldsb + LDS_CTL + (lane & 3) * 4u);
  kf[0][0] = lds128(LP(kp)); kf[0][1] = lds128(LP(kp) + 1024);
  {
    sa = MFMA32(kf[0][0], qf[0][0], f32x16{}); sa = MFMA32(kf[0][1], qf[0][1], sa);
    sb = MFMA32(kf[0][0], qf[1][0], f32x16{}); sb = MFMA32(kf[0][1], qf[1][1], sb);
    dma16x4_sc1(skv, voff, 32768u + wid * 4096u, ldsb + 32768u + wid * 4096u);
    float ma = sa[0], mb = sb[0];
#pragma unroll
    for (int r = 1; r < 16; ++r) { ma = fmaxf(ma, sa[r]); mb = fmaxf(mb, sb[r]); }
    { const auto ra = __builtin_amdgcn_permlane32_swap(__float_as_uint(ma), __float_as_uint(ma), false, false), rb = __builtin_amdgcn_permlane32_swap(__float_as_uint(mb), __float_as_uint(mb), false, false);
      ma = fmaxf(__uint_as_float(ra[0]), __uint_as_float(ra[1])); mb = fmaxf(__uint_as_float(rb[0]), __uint_as_float(rb[1])); }
#pragma unroll
    for (int r = 0; r < 16; ++r) { negm[0][r] = -ma; negm[1][r] = -mb; sa[r] -= ma; }
  }
  PIN(negm[0]); PIN(negm[1]);
#define E1(S_, i) S_[i] = EX2(S_[i])
#define CV(P_, i, S_, r) P_[i] = cvtpk(S_[r], S_[(r) + 1])
#define AT_UNIT(SC, PC0, PC1, SN, kz, mn, PP0, PP1, vz, X0, X1, X2, X3)                                       \
  do {                                                                                                       \
    SN = MFMA32(kf[kz][0], qf[mn][0], negm[mn]); SBAR(); X0;                                                  \
    E1(SC, 0); E1(SC, 1); E1(SC, 2); E1(SC, 3); PIN(SC); SBAR();                                               \
    SN = MFMA32(kf[kz][1], qf[mn][1], SN); SBAR(); X1;                                                        \
    CV(PC0, 0, SC, 0); CV(PC0, 1, SC, 2); E1(SC, 4); E1(SC, 5); E1(SC, 6); PIN(SC); PIN(PC0); SBAR();           \
    o[mn] = MFMA32(vf[vz][0], BF(PP0), o[mn]); SBAR(); X2;                                                    \
    E1(SC, 7); CV(PC0, 2, SC, 4); E1(SC, 8); CV(PC0, 3, SC, 6); E1(SC, 9); PIN(SC); PIN(PC0); SBAR();           \
    ls[mn] = MFMA16(sel, BF(PP0), ls[mn]); SBAR();                                                            \
    E1(SC, 10); E1(SC, 11); PIN(SC); SBAR();                                                                  \
    o[mn] = MFMA32(vf[vz][1], BF(PP1), o[mn]); SBAR(); X3;                                                    \
    CV(PC1, 0, SC, 8); E1(SC, 12); CV(PC1, 1, SC, 10); E1(SC, 13); E1(SC, 14); E1(SC, 15); PIN(SC); PIN(PC1); SBAR(); \
    ls[mn] = MFMA16(sel, BF(PP1), ls[mn]); SBAR();                                                            \
    CV(PC1, 2, SC, 12); CV(PC1, 3, SC, 14); PIN(PC1); SBAR();                                                  \
  } while (0)
#define AT_KT(z, KADDR, VADDR, D0, D1)                                                                        \
  do {                                                                                                       \
    AT_UNIT(sa, pa0, pa1, sb, z, 1, pb0, pb1, z ^ 1, kf[z ^ 1][0] = lds128(KADDR), kf[z ^ 1][1] = lds128((KADDR) + 1024), vf[z][0] = lds128(VADDR), vf[z][1] = lds128((VADDR) + 1024)); \
    asm volatile("" : "+v"(kf[z ^ 1][0]), "+v"(kf[z ^ 1][1]), "+v"(vf[z][0]), "+v"(vf[z][1]));   \
    AT_UNIT(sb, pb0, pb1, sa, z ^ 1, 0, pa0, pa1, z, D0, NOP_, D1, NOP_);                                      \
  } while (0)
  const unsigned vwo = g2_vw(lane, cg), m0b = ldsb + wid * 4096u;
  unsigned arr = 2u, ctlw = ldsb + LDS_CTL + wid * 4u;
  PIN(arr); PIN(ctlw);
#define AT_INT(p, SRD, VO, SO, ST)                                                                            \
  do {                                                                                                       \
    unsigned pv;                                                                                             \
    AT_KT(0, LP(kp) + (p) * 16384 + 4096, LP(kp) + (p) * 16384 + 2048, NOP_, NOP_);                           \
    AT_KT(1, LP(kp) + (p) * 16384 + 8192, LP(kp) + (p) * 16384 + 6144, pv = ctl[lane & 7], NOP_);             \
    lds_meet_fast(pv, 4 * it + (p) + 1, ctla);     \
    SBAR();                                                                                                  \
    AT_KT(0, LP(kp) + (p) * 16384 + 12288, LP(kp) + (p) * 16384 + 10240, dma16<true>(SRD, VO, (SO), m0b + (((p) + 3) & 3) * 16384u), dma16<true>(SRD, VO, (SO) + (ST), m0b + (((p) + 3) & 3) * 16384u + 1024u)); \
    AT_KT(1, LP(kp) + (((p) + 1) & 3) * 16384, LP(kp) + (p) * 16384 + 14336, dma16<true>(SRD, VO, (SO) + 2 * (ST), m0b + (((p) + 3) & 3) * 16384u + 2048u), dma16<true>(SRD, VO, (SO) + 3 * (ST), m0b + (((p) + 3) & 3) * 16384u + 3072u)); \
    asm volatile("s_waitcnt vmcnt(4)\n\tds_write_b32 %1, %0\n\tv_add_u32 %0, 1, %0" : "+v"(arr) : "v"(ctlw) : "memory");     \
  } while (0)
  STAMP(7);
  for (int it = 0; it < 4; ++it) {
    const bool kvq = it < 3;
    const srd_t sq_ = mksrd(kvq ? (const void*)kvg : (const void*)wo, kvq ? 262144u : D * D * 2u);
    const unsigned sk_ = (4 * it + 3) * 16384u + wid * 4096u;
    const unsigned vq_ = kvq ? voff : vwo, so_ = kvq ? sk_ + 16384u : (8 * wid + 5) * 65536u, st_ = kvq ? 1024u : 16384u, sn_ = kvq ? 16384u : 65536u;
    AT_INT(0, skv, voff, sk_, 1024u);
    AT_INT(1, sq_, vq_, so_, st_);
    AT_INT(2, sq_, vq_, so_ + sn_, st_);
    AT_INT(3, sq_, vq_, so_ + 2 * sn_, st_);
  }
  STAMP(8);
  o[1] = MFMA32(vf[1][0], BF(pb0), o[1]); ls[1] = MFMA16(sel, BF(pb0), ls[1]);
  o[1] = MFMA32(vf[1][1], BF(pb1), o[1]); ls[1] = MFMA16(sel, BF(pb1), ls[1]);
  auto store_o = [&](int m, float inv) {
    const unsigned T = qb * 8 + wid * 2 + m;
    const unsigned so = WS_X2 + ((T >> 2) * NKB + 112 + head) * 8192u + (T & 3) * 512u, vo = (lane >> 5) * 2048u + (lane & 31) * 16u;
    st16_sc1(sw, vo, so, pack8(o[m], 0, inv));
    st16_sc1(sw, vo, so + 4096u, pack8(o[m], 1, inv));
  };
  float lt[2];
#pragma unroll
  for (int m = 0; m < 2; ++m) { lt[m] = (lane & 16) ? ls[m][1] : ls[m][0]; store_o(m, __builtin_amdgcn_rcpf(lt[m])); }
  bool bad = false;
#pragma unroll
  for (int m = 0; m < 2; ++m) {
    float chk = lt[m], nanp = lt[m];
#pragma unroll
    for (int r = 0; r < 16; ++r) { chk = fmaxf(chk, fabsf(o[m][r])); nanp += o[m][r]; }
    bad = bad || !(chk < 1e30f) || (nanp != nanp);
  }
  if (__builtin_expect(__any(bad), 0)) {
    float linv[2];
    asm volatile("s_waitcnt vmcnt(0)" ::: "memory");
    att_slow(kvg, qf, o, linv, lane);
    store_o(0, linv[0]); store_o(1, linv[1]);
  }
  asm volatile("s_waitcnt vmcnt(0)" ::: "memory");
  publish_last(ws, ldsb + LDS_CTL + 17 * 4u, 4u, FLG_F4 + qb * 32 + head, tag, lane);
  STAMP(9);
  await(ws, FLG_F4 + (tg >> 1) * 32 + 8 * wid + (lane & 7), tag);
  { unsigned pv = 0; AT_MEET(pv, 15); }
  ctl[8 + wid] = 1u;
  STAMP(10);
}

constexpr int g2_cnt(int m, bool x0, bool w0, bool x1, bool w1, bool w2) {
  return (7 - m) + (w2 ? (8 - m) >> 1 : 0) + (x1 ? 8 : 0) + (w1 ? 4 : 0) + (x0 ? m : 0) + (w0 ? (m + 1) >> 1 : 0);
}
#undef STW
#define STW (kq + 4)
DI void g2_role(const u16* x, u32x4 (&xr)[2], const u16* wo, const u16* wd, char* ws, LAS char* lds, int tg, int cg, int kq, u64 tag, const u32x4 (&hp)[4], const u32x4 (&ha)[4], int mt, int j, srd_t scode) {
  const int tz = lane_id();
  const int lane = tz & 63;
  const srd_t sx2 = mksrd(ws + WS_X2 + (size_t)tg * NKB * 8192, NKB * 8192);
  const srd_t swd = mksrd(wd, F * D * 2), swo = mksrd(wo, D * D * 2);
  const unsigned ldsw = (unsigned)(uintptr_t)lds + P2_G2RING + kq * 16384u;
  const unsigned vw = g2_vw(lane, cg);
  const int t16 = lane & 15, g = lane >> 4, q4 = t16 >> 2, p4 = lane & 3;
  const unsigned vx = g * 2048u + t16 * 16u;
  volatile LAS unsigned* ctl = (volatile LAS unsigned*)(lds + LDS_CTL);
  unsigned wa[4];
  f32x4 acc[8][4];
  {
    u32x4 hq[8];
    const srd_t sw = mksrd(ws, WS_END);
    const unsigned cb = (unsigned)(uintptr_t)lds + LDS_CTL;
    lds_meet(0u, 1u, cb + (12 + kq) * 4u);
#pragma unroll
    for (int i = 0; i < 8; ++i) hq[i] = *(LAS const u32x4*)(lds + (i < 4 ? 131072 : 49152 - 4096) + kq * 4096 + i * 1024 + lane * 16);
#pragma unroll
    for (int i = 0; i < 8; ++i) PIN(hq[i]);
    ctl[4 + kq] = ~0u;
#pragma unroll
    for (int c = 0; c < 4; ++c) { wa[c] = ldsw + (4 * g + q4) * 128 + ((c ^ ((2 * g + (q4 >> 1)) & 3)) << 5) + p4 * 8; PIN(wa[c]); }
#pragma unroll
    for (int m = 0; m < 8; ++m)
#pragma unroll
      for (int c = 0; c < 4; ++c) { acc[m][c] = f32x4{}; PIN(acc[m][c]); }
    lds_meet(0u, 1u, cb + (lane & 3) * 4u);
    const int pair = j < 24 ? 2 * j + 1 : 24 + j;
#pragma unroll
    for (int m = 0; m < 4; ++m) { st16_sc1(sw, vx, h_base(mt, pair, kq, 1) + m * 256u, hp[m]); st16_sc1(sw, vx, h_base(mt, pair, kq, 0) + m * 256u, hq[m]); }
    if (j < 24) {
#pragma unroll
      for (int m = 0; m < 4; ++m) { st16_sc1(sw, vx, h_base(mt, 2 * j, kq, 1) + m * 256u, ha[m]); st16_sc1(sw, vx, h_base(mt, 2 * j, kq, 0) + m * 256u, hq[4 + m]); }
    }
    asm volatile("s_waitcnt vmcnt(0)" ::: "memory");
    publish_last(ws, cb + 16 * 4u, 4u, FLG_F3 + mt * 32 + j, tag, lane);
  }
  if (blockIdx.x == 0 && kq == 0) {
    for (int i = 0; i < 4; ++i) await(ws, FLG_F1 + i * 64 + lane, tag);
    publish(ws, 0, tag, lane);
  }
  await(ws, FLG_F3 + (tg >> 1) * 32 + 7 * kq + (lane < (kq == 3 ? 11 : 7) ? lane : 0), tag);
  STAMP(6);
  bf16x8 xf[2][8];
  unsigned xo = (unsigned)(28 * kq) * 8192u, wo_ = (unsigned)(28 * kq + 4) * 65536u;
#pragma unroll
  for (int z = 0; z < 2; ++z) {
#pragma unroll
    for (int m = 0; m < 8; ++m) ld16<true>(xf[z][m], sx2, vx, xo, m * 256);
    xo += 8192u; }
  s16x4 wl[2][4], wh[2][4];
#define G2_RW(z, off, c) do { LAS const char* p_ = LP(wa[c]) + (off); wl[z][c] = ldstr(p_); wh[z][c] = ldstr(p_ + 2048); } while (0)
#pragma unroll
  for (int c = 0; c < 4; ++c) G2_RW(0, 0, c);
  SBAR();
#define G2_KB(sl, WS_, RD, X0, W0, X1, W1, W2) G2_KBX(sl, WS_, RD, (((sl) + 1) & 3) * 4096, X0, W0, X1, W1, W2, 0, NOP_)
#define G2_KBX(sl, WS_, RD, ROFF, X0, W0, X1, W1, W2, EXTRA, HOOK)                                                                \
  do {                                                                                                       \
    constexpr int z = (sl) & 1;                                                                              \
    _Pragma("unroll") for (int m = 0; m < 8; ++m) {                                                          \
      switch (m) {                                                                                           \
        case 0: mfma16w<g2_cnt(0, X0, W0, X1, W1, W2) + (EXTRA)>(acc[m][0], cat8(wl[z][0], wh[z][0]), xf[z][m]); break; \
        case 1: mfma16w<g2_cnt(1, X0, W0, X1, W1, W2) + (EXTRA)>(acc[m][0], cat8(wl[z][0], wh[z][0]), xf[z][m]); break; \
        case 2: mfma16w<g2_cnt(2, X0, W0, X1, W1, W2) + (EXTRA)>(acc[m][0], cat8(wl[z][0], wh[z][0]), xf[z][m]); break; \
        case 3: mfma16w<g2_cnt(3, X0, W0, X1, W1, W2) + (EXTRA)>(acc[m][0], cat8(wl[z][0], wh[z][0]), xf[z][m]); break; \
        case 4: mfma16w<g2_cnt(4, X0, W0, X1, W1, W2) + (EXTRA)>(acc[m][0], cat8(wl[z][0], wh[z][0]), xf[z][m]); break; \
        case 5: mfma16w<g2_cnt(5, X0, W0, X1, W1, W2) + (EXTRA)>(acc[m][0], cat8(wl[z][0], wh[z][0]), xf[z][m]); break; \
        case 6: mfma16w<g2_cnt(6, X0, W0, X1, W1, W2) + (EXTRA)>(acc[m][0], cat8(wl[z][0], wh[z][0]), xf[z][m]); break; \
        default: mfma16w<g2_cnt(7, X0, W0, X1, W1, W2) + (EXTRA)>(acc[m][0], cat8(wl[z][0], wh[z][0]), xf[z][m]); break; \
      }                                                                                                      \
      SBAR();                                                                                                \
      if (RD && m >= 2 && m < 6) G2_RW(z ^ 1, ROFF, m - 2);                                \
      SBAR();                                                                                                \
      mfma16a(acc[m][1], cat8(wl[z][1], wh[z][1]), xf[z][m]);                                                 \
      mfma16a(acc[m][2], cat8(wl[z][2], wh[z][2]), xf[z][m]);                                                 \
      mfma16a(acc[m][3], cat8(wl[z][3], wh[z][3]), xf[z][m]); SBAR();                                         \
      HOOK;                                                                                                  \
      if (X0) ld16_inplace(xf[z][m], sx2, vx, xo, m * 256);                                                  \
      if (W0 && !(m & 1)) dma16<false>(WS_, vw, wo_ + (m >> 1) * 16384u, ldsw + (sl) * 4096u + (m >> 1) * 1024u); \
      SBAR();                                                                                                \
    }                                                                                                        \
    xo += 8192u; wo_ += 65536u;                                                                              \
  } while (0)
#define YES true
#define NO false
  STAMP(7);
  G2_KB(0, swd, YES, YES, YES, YES, NO, NO); G2_KB(1, swd, YES, YES, YES, YES, YES, NO); G2_KB(2, swd, YES, YES, YES, YES, YES, YES); G2_KB(3, swd, YES, YES, YES, YES, YES, YES);
  for (int i = 4; i < 24; i += 4) {
    dma16<false>(scode, vx, (unsigned)((mt * 8192) + ((j >> 3) & 1) * 1024 + kq * 256), (unsigned)(uintptr_t)lds + LDS_DUMP + (4 + kq) * 1024u);
    G2_KB(0, swd, YES, YES, YES, YES, YES, YES); G2_KB(1, swd, YES, YES, YES, YES, YES, YES); G2_KB(2, swd, YES, YES, YES, YES, YES, YES); G2_KB(3, swd, YES, YES, YES, YES, YES, YES);
  }
  wo_ = (unsigned)(8 * kq) * 65536u;
  G2_KB(0, swo, YES, YES, YES, YES, YES, YES); G2_KB(1, swo, YES, YES, YES, YES, YES, YES);
  G2_KB(2, swo, YES, NO, YES, YES, YES, YES); G2_KB(3, swo, YES, NO, YES, NO, YES, YES);
  asm volatile("" : "+v"(wl[0][0]), "+v"(wh[0][0]), "+v"(wl[0][1]), "+v"(wh[0][1]), "+v"(wl[0][2]), "+v"(wh[0][2]), "+v"(wl[0][3]), "+v"(wh[0][3]));
#pragma unroll
  for (int p = 0; p < 4; ++p) dma16<false>(swo, vw, wo_ + p * 16384u, ldsw + p * 1024u);
  STAMP(8);
  lds_meet(0u, 1u, (unsigned)(uintptr_t)lds + LDS_CTL + (8 + kq) * 4u);
  STAMP(9);
  xo = (unsigned)(112 + 8 * kq) * 8192u;
#pragma unroll
  for (int z = 0; z < 2; ++z) {
#pragma unroll
    for (int m = 0; m < 8; ++m) ld16_inplace(xf[z][m], sx2, vx, xo, m * 256);
    xo += 8192u; }
  G2_KB(0, swo, YES, YES, NO, YES, NO, NO); G2_KB(1, swo, YES, YES, NO, YES, NO, NO); G2_KB(2, swo, YES, YES, NO, YES, NO, NO); G2_KB(3, swo, YES, YES, NO, YES, NO, NO);
#pragma unroll
  for (int c = 0; c < 4; ++c) wa[c] -= P2_G2RING + kq * 12288u;
  G2_KBX(0, swo, YES, 0, YES, NO, YES, NO, NO, 0, NOP_); G2_KBX(1, swo, YES, 16384, YES, NO, YES, NO, NO, 0, NOP_);
  G2_KBX(2, swo, YES, 32768, NO, NO, YES, NO, NO, 0, NOP_);
  const int ty = lane_id();
  { const srd_t sxi = mksrd(x, S * D * 2);
    const unsigned xv = (unsigned)((ty & 63) >> 3) * 2048u + (ty & 7) * 16u, xs = (unsigned)(tg * 128 + (4 + kq) * 16) * 2048u + cg * 128u;
    ld16<false>(xr[0], sxi, xv, xs, 0); ld16<false>(xr[1], sxi, xv, xs + 16384u, 0); }
  unsigned pa[4];
#pragma unroll
  for (int c = 0; c < 4; ++c) pa[c] = (unsigned)(uintptr_t)lds + kq * 4096u + (ty & 15) * 256u + (((4 * c + ((ty >> 4) & 3)) ^ (ty & 15)) << 4);
  const unsigned pown = P2_G2RING + kq * 12288u;
#define G2_PUT(mm) do { _Pragma("unroll") for (int c = 0; c < 4; ++c) *(LAS f32x4*)((LAS char*)(uintptr_t)((mm) < 4 ? pa[c] : pa[c] + pown) + ((mm) < 4 ? (mm) * 16384 : ((mm) - 4) * 4096)) = acc[mm][c]; } while (0)
  G2_KBX(3, swo, NO, 0, NO, NO, NO, NO, NO, 2, if (m >= 2) G2_PUT(m >= 2 ? m - 2 : 0));
  asm volatile("s_nop 7" ::: "memory");
  G2_PUT(6); G2_PUT(7);
  asm volatile("s_waitcnt vmcnt(0)" : "+v"(xr[0]), "+v"(xr[1]));
  STAMP(10);
}

#undef STW
#define STW wid
__global__ __launch_bounds__(512, 2) void fused_kernel(const u16* x, const u16* wqkv, const u16* wo, const u16* wg, const u16* wu, const u16* wd, char* ws, u16* out) {
  extern __shared__ __attribute__((aligned(16))) char smem[];
  LAS char* lds = (LAS char*)smem;
  const int b = blockIdx.x, xcd = b & 7, r = b >> 3, wid = rfl(threadIdx.x >> 6);
  u64 tag = *(const __attribute__((address_space(4))) u64*)(ws + WS_FLG);
  ((LAS unsigned*)(lds + LDS_CTL))[threadIdx.x & 31] = 0u;
  const int tg = (xcd >> 1) * 4 + (r & 3), cg = (xcd & 1) * 8 + (r >> 2);
  const int mt = r & 7, j = xcd + 8 * (r >> 3);
  srd_t scode;
  { u64 pc; unsigned len;
    asm volatile("s_getpc_b64 %0\n1:\n\ts_mov_b32 %1, .Lfk_code_end-1b" : "=s"(pc), "=s"(len));
    scode = mksrd((const void*)(pc & ~127ull), len); }
  STAMP(0);
  ln_row(x, ws, mt * 256 + j * 8 + wid, scode, (unsigned)((r * 8 + wid) & 127) * 1024u, (unsigned)(uintptr_t)lds + LDS_DUMP + wid * 1024u);
  asm volatile("s_waitcnt vmcnt(0)" : "+s"(tag)::"memory");
  tag = tag * 6364136223846793005ull + 1442695040888963407ull;
  STAMP(1);
  asm volatile("s_barrier" ::: "memory");
  if (wid == 0) publish(ws, FLG_F1 + mt * 32 + j, tag, lane_id());
  const int head = xcd * 4 + (r >> 3);
  f32x4 accb[4][4];
  u32x4 ha[4];
  if (j < 16) g1_body<0>(head, wo, wd, cg, wqkv, wg, wu, ws, lds, wid, mt, j, scode, tag, accb, ha);
  else if (j < 24) g1_body<1>(head, wo, wd, cg, wqkv, wg, wu, ws, lds, wid, mt, j, scode, tag, accb, ha);
  else g1_body<2>(head, wo, wd, cg, wqkv, wg, wu, ws, lds, wid, mt, j, scode, tag, accb, ha);
  u32x4 hp[4];
  glu_pack(accb, hp);
  u32x4 xr[2];
  if (wid >= 4) {
    g2_role(x, xr, wo, wd, ws, lds, tg, cg, wid - 4, tag, hp, ha, mt, j, scode);
  } else {
    att_role(wo, ws, lds, head, mt, tg, cg, wid, tag, hp, ha);
    const int ty = lane_id();
#pragma unroll
    for (int e = 0; e < 2; ++e) xr[e] = *(const u32x4*)(x + (size_t)(tg * 128 + wid * 16 + e * 8 + ((ty & 63) >> 3)) * D + cg * 64 + (ty & 7) * 8);
  }
  const int ln = lane_id();
  unsigned ra[4][4], vo = (unsigned)((tg * 128 + wid * 16 + (ln >> 3)) * D + cg * 64 + (ln & 7) * 8) * 2u;
  f32x4 sum[4];
#pragma unroll
  for (int i = 0; i < 4; ++i) {
    const unsigned tk = (i >> 1) * 8 + (ln >> 3);
#pragma unroll
    for (int k = 0; k < 4; ++k) { ra[i][k] = (unsigned)(uintptr_t)lds + (wid < 4 ? wid * 16384 + k * 4096 : P2_G2RING + (wid - 4) * 4096 + k * 16384) + tk * 256 + (((2 * (ln & 7) + (i & 1)) ^ tk) << 4); PIN(ra[i][k]); }
    sum[i] = f32x4{blo(xr[i >> 1][2 * (i & 1)]), bhi(xr[i >> 1][2 * (i & 1)]), blo(xr[i >> 1][2 * (i & 1) + 1]), bhi(xr[i >> 1][2 * (i & 1) + 1])};
    PIN(sum[i]);
  }
  PIN(vo);
  asm volatile("s_waitcnt lgkmcnt(0)\n\ts_barrier" ::: "memory");
  STAMP(11);
  f32x4 part[4][4];
#pragma unroll
  for (int i = 0; i < 4; ++i)
#pragma unroll
    for (int k = 0; k < 4; ++k) part[i][k] = *(LAS const f32x4*)LP(ra[i][k]);
  SBAR();
#pragma unroll
  for (int i = 0; i < 4; ++i) sum[i] += (part[i][0] + part[i][1]) + (part[i][2] + part[i][3]);
  st16_sc1(mksrd(out, S * D * 2), vo, 0u, pack44(sum[0], sum[1], 1.f));
  st16_sc1(mksrd(out, S * D * 2), vo, 8u * D * 2u, pack44(sum[2], sum[3], 1.f));
  STAMP(12);
  asm volatile(".Lfk_code_end:");
}

extern "C" void kernel_launch(void* const* d_in, const int* in_sizes, int n_in, void* d_out, int out_size, void* d_ws, size_t ws_size, hipStream_t stream) {
  (void)hipFuncSetAttribute((const void*)fused_kernel, hipFuncAttributeMaxDynamicSharedMemorySize, LDS_BYTES);
  fused_kernel<<<256, 512, LDS_BYTES, stream>>>((const u16*)d_in[0], (const u16*)d_in[1], (const u16*)d_in[2], (const u16*)d_in[4], (const u16*)d_in[5], (const u16*)d_in[6], (char*)d_ws, (u16*)d_out);
}
```

```cpp
#include <hip/hip_runtime.h>
#include <hip/hip_bf16.h>
#include <stdint.h>

typedef unsigned short u16;
typedef unsigned long long u64;
typedef short bf16x8 __attribute__((ext_vector_type(8)));
typedef short s16x4 __attribute__((ext_vector_type(4)));
typedef float f32x16 __attribute__((ext_vector_type(16)));
typedef float f32x4 __attribute__((ext_vector_type(4)));
typedef float f32x2 __attribute__((ext_vector_type(2)));
typedef unsigned u32x4 __attribute__((ext_vector_type(4)));
typedef unsigned u32x2 __attribute__((ext_vector_type(2)));
typedef __bf16 bf16x2_t __attribute__((ext_vector_type(2)));
typedef short v4i16_t __attribute__((ext_vector_type(4)));
typedef __amdgpu_buffer_rsrc_t srd_t;
typedef __attribute__((address_space(1))) u64 gu64;
#define LAS __attribute__((address_space(3)))
#define DI __device__ __forceinline__
#define SBAR() __builtin_amdgcn_sched_barrier(0)
#define PIN(x) asm volatile("" : "+v"(x))
#define NOP_ ((void)0)
#define LP(a) ((LAS const char*)(uintptr_t)(a))
#define MFMA32(a, b, c) __builtin_amdgcn_mfma_f32_32x32x16_bf16((a), (b), (c), 0, 0, 0)
#define MFMA16(a, b, c) __builtin_amdgcn_mfma_f32_16x16x32_bf16((a), (b), (c), 0, 0, 0)
#define EX2(v) __builtin_amdgcn_exp2f(v)
#define BF(x) __builtin_bit_cast(bf16x8, x)
#define RLX_AGENT __ATOMIC_RELAXED, __HIP_MEMORY_SCOPE_AGENT

constexpr int S = 2048, D = 1024, F = 3584;
constexpr int NKB = (F + D) / 32;
constexpr float LOG2E = 1.4426950408889634f;
constexpr float QSCALE = 0.17677669529663687f * LOG2E;

constexpr unsigned WS_FLG = 0;
constexpr unsigned FLG_F1 = 256, FLG_F2 = FLG_F1 + 2048, FLG_F3 = FLG_F2 + 2048, FLG_F4 = FLG_F3 + 2048;
constexpr unsigned FST = 16;
constexpr unsigned WS_XN = 2097152;
constexpr unsigned WS_Q = WS_XN + S * D * 2;
constexpr unsigned WS_KV = WS_Q + S * D * 2;
constexpr unsigned WS_X2 = WS_KV + S * D * 4;
constexpr unsigned WS_END = WS_X2 + S * (F + D) * 2;
constexpr int G1_XS = 32768, G1_WB = 65536, G1_WS = 40960;
constexpr int P2_G2RING = 65536;
constexpr int LDS_CTL = G1_WB + 2 * G1_WS, LDS_DUMP = LDS_CTL + 1024, LDS_BYTES = LDS_DUMP + 8192;
#define PFD 4u
#define EARLY 1
constexpr unsigned SPIN_CAP = 1u << 20;

DI unsigned rfl(unsigned x) { return __builtin_amdgcn_readfirstlane(x); }
DI int lane_id() { int l; asm volatile("v_mbcnt_lo_u32_b32 %0, -1, 0\n\tv_mbcnt_hi_u32_b32 %0, -1, %0" : "=v"(l)); return l; }
DI unsigned cvtpk(float lo, float hi) { f32x2 v = {lo, hi}; bf16x2_t b = __builtin_convertvector(v, bf16x2_t); return __builtin_bit_cast(unsigned, b); }
DI float rbf(float f) { return __uint_as_float(cvtpk(f, f) << 16); }
DI float blo(unsigned u) { return __uint_as_float(u << 16); }
DI float bhi(unsigned u) { return __uint_as_float(u & 0xffff0000u); }
DI srd_t mksrd(const void* p, unsigned bytes) { return __builtin_amdgcn_make_buffer_rsrc((void*)p, 0, bytes, 0x00020000); }
template <bool SC1> DI void dma16(srd_t srd, unsigned voff, unsigned soff, unsigned m0v) {
  if (SC1) asm volatile("s_mov_b32 m0, %0\n\ts_nop 0\n\tbuffer_load_dwordx4 %1, %2, %3 offen sc1 lds" ::"s"(m0v), "v"(voff), "s"(srd), "s"(soff) : "memory");
  else asm volatile("s_mov_b32 m0, %0\n\ts_nop 0\n\tbuffer_load_dwordx4 %1, %2, %3 offen lds" ::"s"(m0v), "v"(voff), "s"(srd), "s"(soff) : "memory");
}
template <bool SC1, class T> DI void ld16(T& d, srd_t srd, unsigned voff, unsigned soff, int imm) {
  if (SC1) asm volatile("buffer_load_dwordx4 %0, %1, %2, %3 offen offset:%4 sc1" : "=v"(d) : "v"(voff), "s"(srd), "s"(soff), "i"(imm) : "memory");
  else asm volatile("buffer_load_dwordx4 %0, %1, %2, %3 offen offset:%4" : "=v"(d) : "v"(voff), "s"(srd), "s"(soff), "i"(imm) : "memory");
}
template <class T> DI void ld16_inplace(T& d, srd_t srd, unsigned voff, unsigned soff, int imm) {
  asm volatile("buffer_load_dwordx4 %0, %1, %2, %3 offen offset:%4 sc1" : "+v"(d) : "v"(voff), "s"(srd), "s"(soff), "i"(imm) : "memory");
}
DI void st16_sc1(srd_t srd, unsigned voff, unsigned soff, u32x4 v) {
  asm volatile("buffer_store_dwordx4 %0, %1, %2, %3 offen sc1\n\ts_nop 1" ::"v"(v), "v"(voff), "s"(srd), "s"(soff) : "memory");
}
DI s16x4 ldstr(LAS const char* p) { return __builtin_bit_cast(s16x4, __builtin_amdgcn_ds_read_tr16_b64_v4i16((LAS v4i16_t*)p)); }
DI bf16x8 lds128(LAS const char* p) { return *(LAS const bf16x8*)p; }
DI bf16x8 cat8(s16x4 lo, s16x4 hi) { return __builtin_shufflevector(lo, hi, 0, 1, 2, 3, 4, 5, 6, 7); }
DI u32x4 pack8(const f32x16& v, int s, float sc) {
  u32x4 w;
  w[0] = cvtpk(v[8 * s + 0] * sc, v[8 * s + 1] * sc); w[1] = cvtpk(v[8 * s + 2] * sc, v[8 * s + 3] * sc);
  w[2] = cvtpk(v[8 * s + 4] * sc, v[8 * s + 5] * sc); w[3] = cvtpk(v[8 * s + 6] * sc, v[8 * s + 7] * sc);
  return w;
}
DI u32x4 pack44(const f32x4& a, const f32x4& b, float sc) {
  u32x4 w; w[0] = cvtpk(a[0] * sc, a[1] * sc); w[1] = cvtpk(a[2] * sc, a[3] * sc); w[2] = cvtpk(b[0] * sc, b[1] * sc); w[3] = cvtpk(b[2] * sc, b[3] * sc); return w;
}
DI void mfma16a(f32x4& c, const bf16x8& a, const bf16x8& b) { asm volatile("v_mfma_f32_16x16x32_bf16 %0, %1, %2, %0" : "+v"(c) : "v"(a), "v"(b)); }
template <int N> DI void mfma16w(f32x4& c, const bf16x8& a, const bf16x8& b) { asm volatile("s_waitcnt vmcnt(%3)\n\tv_mfma_f32_16x16x32_bf16 %0, %1, %2, %0" : "+v"(c) : "v"(a), "v"(b), "i"(N)); }
template <int CTRL, int RM> DI float dppadd(float v) { return v + __builtin_bit_cast(float, __builtin_amdgcn_update_dpp(0, __builtin_bit_cast(int, v), CTRL, RM, 0xf, true)); }
DI float wave_sum(float v) {
  v = dppadd<0x111, 0xf>(v); v = dppadd<0x112, 0xf>(v); v = dppadd<0x114, 0xf>(v); v = dppadd<0x118, 0xf>(v);
  v = dppadd<0x142, 0xa>(v); v = dppadd<0x143, 0xc>(v);
  return __builtin_bit_cast(float, __builtin_amdgcn_readlane(__builtin_bit_cast(int, v), 63));
}
DI void publish(char* ws, unsigned idx, u64 tag, int lane) {
  const u32x2 tv = {(unsigned)tag, (unsigned)(tag >> 32)};
  asm volatile("buffer_store_dwordx2 %0, %1, %2, %3 offen sc1" ::"v"(tv), "v"(lane == 0 ? 0u : 0x7ff00000u), "s"(mksrd(ws + WS_FLG, WS_XN)), "s"(idx * (8u * FST)) : "memory");
}
DI void publish_last(char* ws, unsigned cnt_addr, unsigned n, unsigned idx, u64 tag, int lane) {
  unsigned old;
  asm volatile("ds_add_rtn_u32 %0, %1, %2\n\ts_waitcnt lgkmcnt(0)" : "=v"(old) : "v"(cnt_addr), "v"(lane == 0 ? 1u : 0u) : "memory");
  if (rfl(old) == n - 1) publish(ws, idx, tag, lane);
}
DI void await(char* ws, unsigned idx, u64 tag) {
  gu64* p = (gu64*)(ws + WS_FLG) + idx * FST;
  for (unsigned spins = 0; spins < SPIN_CAP; ++spins) {
    if (__all(__hip_atomic_load(p, RLX_AGENT) == tag)) break;
    __builtin_amdgcn_s_sleep(1);
  }
  asm volatile("" ::: "memory");
}

#define STW wid
#define STAMP(i) do {} while (0)

DI unsigned g2_vw(int lane, int cg) { const unsigned l3 = lane >> 3, l7 = lane & 7; return l3 * 2048u + ((l7 ^ (((l3 >> 1) & 3) << 1)) << 4) + cg * 128u; }

DI void ln_row(const u16* x, char* ws, int row, srd_t scode, unsigned code_off, unsigned dump) {
  const int lane = lane_id();
  const srd_t sxi = mksrd(x, S * D * 2), sx = mksrd(ws + WS_XN, S * D * 2);
  u32x4 a, b;
  ld16<false>(a, sxi, lane * 16u, row * 2048u, 0); ld16<false>(b, sxi, lane * 16u, row * 2048u, 1024);
  dma16<false>(scode, lane * 16u, code_off, dump);
  asm volatile("s_waitcnt vmcnt(1)" : "+v"(a), "+v"(b));
  f32x2 v[8];
#pragma unroll
  for (int i = 0; i < 4; ++i) { v[i] = f32x2{blo(a[i]), bhi(a[i])}; v[4 + i] = f32x2{blo(b[i]), bhi(b[i])}; }
  const f32x2 s2 = ((v[0] + v[1]) + (v[2] + v[3])) + ((v[4] + v[5]) + (v[6] + v[7]));
  const float mean = wave_sum(s2[0] + s2[1]) * (1.f / D);
  f32x2 q[4];
#pragma unroll
  for (int i = 0; i < 4; ++i) { const f32x2 d0 = v[i] - mean, d1 = v[4 + i] - mean; q[i] = __builtin_elementwise_fma(d1, d1, d0 * d0); }
  const f32x2 q2 = (q[0] + q[1]) + (q[2] + q[3]);
  const float mb = rbf(mean);
  unsigned dn[8];
#pragma unroll
  for (int i = 0; i < 8; ++i) { const f32x2 d = v[i] - mb; dn[i] = cvtpk(d[0], d[1]); }
  const float vb = rbf(wave_sum(q2[0] + q2[1]) * (1.f / D));
  const float inv = __builtin_amdgcn_rcpf(rbf(__builtin_amdgcn_sqrtf(rbf(vb + 1e-5f))));
  u32x4 oa, ob;
#pragma unroll
  for (int i = 0; i < 4; ++i) { oa[i] = cvtpk(blo(dn[i]) * inv, bhi(dn[i]) * inv); ob[i] = cvtpk(blo(dn[4 + i]) * inv, bhi(dn[4 + i]) * inv); }
  st16_sc1(sx, lane * 16u, row * 2048u, oa); st16_sc1(sx, lane * 16u + 1024u, row * 2048u, ob);
}

DI f32x2 glu2(float g0, float g1, float u0, float u1) { return f32x2{u0 * __builtin_amdgcn_rcpf(1.f + EX2(g0 * -LOG2E)), u1 * __builtin_amdgcn_rcpf(1.f + EX2(g1 * -LOG2E))}; }
DI unsigned h_base(int mt, int pair, int wr, int wc) { return WS_X2 + ((mt * 2 + (wr >> 1)) * NKB + 2 * pair + wc) * 8192u + (wr & 1) * 1024u; }
DI int unit_owner(int a) { return a >= 32 ? a - 32 : (a < 8 ? 16 + a : 24 + (a - 8) / 3); }
DI unsigned att_dep(int head, int qb, int lane) {
  const int jq = unit_owner(head >> 1), jk = unit_owner(16 + (head >> 1)), jv = unit_owner(32 + (head >> 1));
  unsigned ikv = (lane & 7) * 32 + ((lane & 8) ? jk : jv); PIN(ikv);
  return FLG_F2 + (lane < 16 ? ikv : qb * 32 + jq);
}
template <int KIND>
DI void g1_body(int head, const u16* wo, const u16* wd, int cg, const u16* wqkv, const u16* wg, const u16* wu, char* ws, LAS char* lds, int wid, int mt, int j, srd_t scode, u64 tag, f32x4 (&accb)[4][4], u32x4 (&ha)[4]) {
  const int lane = lane_id(), wr = wid & 3, wc = wid >> 2;
  constexpr bool SWAP0 = KIND == 0, isY = KIND == 2;
  const int a0 = KIND == 0 ? 32 + j : (KIND == 1 ? j - 16 : 3 * j - 64);
  const srd_t sx = mksrd(ws + WS_XN, S * D * 2), sq = mksrd(wqkv, D * 3 * D * 2), sg = mksrd(wg, D * F * 2), su = mksrd(wu, D * F * 2);
  const srd_t sa = isY ? sq : sg, sb = isY ? sq : su;
  const unsigned ld1 = isY ? 6144u : 7168u;
  const unsigned c0 = a0 * 128u, c1 = isY ? c0 + 128u : j * 256u, c2 = isY ? c0 + 256u : j * 256u, c3 = isY ? (24 + j) * 128u : j * 256u + 128u;
  const unsigned l3 = lane >> 3, l7 = lane & 7, l4 = lane >> 4;
  const unsigned vx0 = l3 * 2048u + ((l7 ^ l4) << 4), vx1 = vx0 ^ 64u;
  const unsigned wsw = (l7 ^ (((l4 & 1) | ((wid & 1) << 1)) << 1)) << 4;
  const unsigned vw0 = l3 * 6144u + wsw, vw2 = l3 * 7168u + wsw;
  const unsigned vw1 = isY ? vw0 : vw2;
  const unsigned ldsb = (unsigned)(uintptr_t)lds;
  unsigned sox = (unsigned)(mt * 256 + wid * 32) * 2048u;
  unsigned so0 = wid * 8 * 6144u + c0, so1 = wid * 8 * ld1, so2 = wid * 8 * 7168u + c3;
  const unsigned m0x = ldsb + wid * 4096u, m0w = ldsb + G1_WB + wid * 1024u;
#define G1_DX(i, st) dma16<true>(sx, ((i) & 1) ? vx1 : vx0, sox + (i) * 16384u, m0x + (st) * G1_XS + (i) * 1024u)
#define G1_DW(i, st)                                                                             \
  do {                                                                                           \
    if ((i) == 0) dma16<false>(sq, vw0, so0, m0w + (st) * G1_WS);                                 \
    if ((i) == 1) dma16<false>(sa, vw1, so1 + c1, m0w + (st) * G1_WS + 8192u);                    \
    if ((i) == 2) dma16<false>(sb, vw1, so1 + c2, m0w + (st) * G1_WS + 16384u);                   \
    if ((i) == 3) dma16<false>(sg, vw2, so2, m0w + (st) * G1_WS + 24576u);                        \
    if ((i) == 4) dma16<false>(su, vw2, so2, m0w + (st) * G1_WS + 32768u);                        \
  } while (0)
#define G1_ADVX() do { sox += 128u; } while (0)
#define G1_ADVW() do { so0 += 64u * 6144u; so1 += 64u * ld1; so2 += 64u * 7168u; } while (0)
#define G1_CODE() dma16<false>(scode, vx0, (unsigned)(mt * 16384 + ((j >> 3) & 1) * 1024 + wid * 128), ldsb + LDS_DUMP + wid * 1024u)
  const srd_t sp = wid == 0 ? sq : (wid == 1 ? sa : (wid == 2 ? sb : (wid == 3 ? sg : su)));
  const unsigned ldp = wid == 0 ? 6144u : (wid < 3 ? ld1 : 7168u), cp = wid == 0 ? c0 : (wid == 1 ? c1 : (wid == 2 ? c2 : (wid < 5 ? c3 : 0x7ff00000u)));
  const unsigned vwp = ldp == 6144u ? vw0 : vw2, idp = (mt * 32 + j) * 8 + wid;
  unsigned sop = cp + (PFD * 64u + mt * 8u) * ldp, rowd = (idp >> 4) * 8u;
#define G1_WARMW() do { dma16<false>(sp, vwp, sop, ldsb + LDS_DUMP + wid * 1024u); sop += 64u * ldp; } while (0)
#define G1_WARMD() do { const bool d_ = rowd < (unsigned)F; dma16<false>(mksrd(d_ ? wd : wo, d_ ? F * D * 2 : D * D * 2), vx0, (d_ ? rowd : rowd - F) * 2048u + (idp & 15) * 128u, ldsb + LDS_DUMP + wid * 1024u); rowd += 1024u; } while (0)
  f32x4 acc[4][10];
  G1_DW(0, 0); G1_DW(1, 0); G1_DW(2, 0); G1_DW(3, 0); G1_DW(4, 0); G1_ADVW();
  if (EARLY) asm volatile("s_mov_b32 m0, %0\n\ts_nop 0\n\tbuffer_load_dword %1, %2, %3 offen lds" ::"s"(ldsb + LDS_DUMP + wid * 1024u), "v"(l3 + 1u < PFD ? ((l3 + 1u) * 64u + mt * 8u + l7) * ldp : 0x7ff00000u), "s"(sp), "s"(cp) : "memory");
#pragma unroll
  for (int m = 0; m < 4; ++m)
#pragma unroll
    for (int n = 0; n < 10; ++n) { acc[m][n] = f32x4{}; PIN(acc[m][n]); }
  await(ws, FLG_F1 + mt * 32 + wid * 4 + (lane & 3), tag);
  STAMP(2);
  G1_DX(0, 0); G1_DX(1, 0); G1_DX(2, 0); G1_DX(3, 0); G1_ADVX();
  G1_DX(0, 1); G1_DX(1, 1); G1_DX(2, 1); G1_DX(3, 1); G1_ADVX();
  G1_DW(0, 1); G1_DW(1, 1); G1_DW(2, 1); G1_DW(3, 1); G1_DW(4, 1); G1_ADVW();
  const int t16 = lane & 15, g = lane >> 4, q4 = t16 >> 2, p4 = lane & 3;
  unsigned xa0 = ldsb + (wr * 64 + t16) * 128 + ((g ^ ((t16 >> 1) & 7)) << 4), xa1 = xa0 ^ 64u;
  unsigned wa00 = ldsb + G1_WB + (8 * g + q4) * 128 + (((2 * wc) ^ (((q4 >> 1) & 1) | ((g & 1) << 1))) << 5) + p4 * 8;
  unsigned wa01 = wa00 ^ 32u, wa10 = wa00 + G1_WS, wa11 = wa01 + G1_WS;
  asm volatile("" : "+v"(xa0), "+v"(xa1), "+v"(wa00), "+v"(wa01), "+v"(wa10), "+v"(wa11));
  bf16x8 xf[2][4];
  s16x4 wl[10], wh[10];
#define G1_RX(st, s, m) xf[s][m] = lds128(LP((s) ? xa1 : xa0) + (st) * G1_XS + (m) * 2048)
#define G1_RW(st, s, n)                                                                                      \
  do {                                                                                                       \
    LAS const char* p_ = LP((st) ? (((n) & 1) ? wa11 : wa10) : (((n) & 1) ? wa01 : wa00)) + ((n) >> 1) * 8192 + (s) * 4096; \
    wl[n] = ldstr(p_); wh[n] = ldstr(p_ + 512);                                                              \
  } while (0)
#define G1_MM(s, n, m)                                                                                       \
  do {                                                                                                       \
    if (SWAP0 && (n) < 2) mfma16a(acc[m][n], xf[s][m], cat8(wl[n], wh[n]));                                   \
    else mfma16a(acc[m][n], cat8(wl[n], wh[n]), xf[s][m]);                                                    \
  } while (0)
#define G1_GRP(s, n, A, B, C) do { G1_MM(s, n, 0); SBAR(); A; SBAR(); G1_MM(s, n, 1); SBAR(); B; SBAR(); G1_MM(s, n, 2); SBAR(); C; SBAR(); G1_MM(s, n, 3); SBAR(); } while (0)
#define G1_WAITBAR() G1_WAITN(0)
#define G1_WAITN(N) asm volatile("s_waitcnt vmcnt(%0) lgkmcnt(0)\n\ts_barrier" ::"i"(N) : "memory")
#define G1_HEAD(st)                                                                                          \
  do {                                                                                                       \
    G1_GRP(0, 0, G1_RW(st, 0, 9), NOP_, NOP_);                                                               \
    G1_GRP(0, 1, G1_RW(st, 1, 0), G1_RX(st, 1, 0), NOP_);                                                    \
    G1_GRP(0, 2, G1_RW(st, 1, 1), G1_RX(st, 1, 1), NOP_);                                                    \
    G1_GRP(0, 3, G1_RW(st, 1, 2), G1_RX(st, 1, 2), NOP_);                                                    \
    G1_GRP(0, 4, G1_RW(st, 1, 3), G1_RX(st, 1, 3), NOP_);                                                    \
    G1_GRP(0, 5, G1_RW(st, 1, 4), NOP_, NOP_);                                                               \
    G1_GRP(0, 6, G1_RW(st, 1, 5), NOP_, G1_WARMW());                                                           \
    G1_GRP(0, 7, G1_RW(st, 1, 6), NOP_, NOP_);                                                               \
    G1_GRP(0, 8, G1_RW(st, 1, 7), NOP_, if (st) G1_WARMD(); else G1_CODE());                                                           \
    G1_GRP(0, 9, G1_RW(st, 1, 8), NOP_, NOP_);                                                               \
    G1_GRP(1, 0, G1_RW(st, 1, 9), NOP_, NOP_);                                                               \
    G1_GRP(1, 1, NOP_, NOP_, NOP_);                                                                          \
    G1_GRP(1, 2, NOP_, NOP_, NOP_);                                                                          \
    G1_WAITN(2);                                 \
  } while (0)
#define G1_TILE(st, DM)                                                                                      \
  do {                                                                                                       \
    G1_HEAD(st);                                                                                             \
    G1_GRP(1, 3, G1_RX(st ^ 1, 0, 0), G1_RW(st ^ 1, 0, 0), if (DM) G1_DX(0, st));                             \
    G1_GRP(1, 4, G1_RX(st ^ 1, 0, 1), G1_RW(st ^ 1, 0, 1), if (DM) G1_DW(0, st));                             \
    G1_GRP(1, 5, G1_RX(st ^ 1, 0, 2), G1_RW(st ^ 1, 0, 2), if (DM) G1_DX(1, st));                             \
    G1_GRP(1, 6, G1_RX(st ^ 1, 0, 3), G1_RW(st ^ 1, 0, 3), if (DM) G1_DW(1, st));                             \
    G1_GRP(1, 7, G1_RW(st ^ 1, 0, 4), G1_RW(st ^ 1, 0, 5), if (DM) { G1_DX(2, st); G1_DW(2, st); });          \
    G1_GRP(1, 8, G1_RW(st ^ 1, 0, 6), G1_RW(st ^ 1, 0, 7), if (DM) { G1_DX(3, st); G1_DW(3, st); });          \
    G1_GRP(1, 9, G1_RW(st ^ 1, 0, 8), NOP_, if (DM) { G1_DW(4, st); G1_ADVX(); G1_ADVW(); });                 \
  } while (0)
  asm volatile("s_waitcnt vmcnt(9)\n\ts_barrier" ::: "memory");
  G1_RX(0, 0, 0); G1_RW(0, 0, 0); G1_RX(0, 0, 1); G1_RX(0, 0, 2); G1_RX(0, 0, 3);
  G1_RW(0, 0, 1); G1_RW(0, 0, 2); G1_RW(0, 0, 3); G1_RW(0, 0, 4); G1_RW(0, 0, 5); G1_RW(0, 0, 6); G1_RW(0, 0, 7); G1_RW(0, 0, 8);
  SBAR();
  STAMP(3);
  for (int t = 0; t < 14; t += 2) { G1_TILE(0, true); G1_TILE(1, true); }
  STAMP(4);
  {
    const srd_t sw = mksrd(ws, WS_END);
    unsigned vq, vk;
    auto store_addr = [&]() { const unsigned l = lane_id(); vq = (l >> 4) * 512u + (l & 15) * 16u; vk = (l >> 4) * 512u + (2 * (l & 8) + (l & 7)) * 16u; };
    const unsigned T32 = mt * 8 + wr * 2;
    auto store_u = [&](int u, int i) {
      const int a = a0 + u; const unsigned head = 2 * (a & 15) + wc; const bool isq = a < 16;
      if (SWAP0) st16_sc1(sw, vk, WS_KV + (head * 64 + T32 + (i >> 1)) * 4096u + 2048u + (i & 1) * 128u, pack44(acc[2 * (i >> 1)][i & 1], acc[2 * (i >> 1) + 1][i & 1], 1.f));
      else st16_sc1(sw, isq ? vq : vk, isq ? WS_Q + (head * 64 + T32 + (i >> 1)) * 2048u + (i & 1) * 256u : WS_KV + (head * 64 + T32 + (i >> 1)) * 4096u + (i & 1) * 128u,
                    pack44(acc[i][2 * u], acc[i][2 * u + 1], isq ? QSCALE : 1.f));
    };
#define G1_HA(k) do { if (!isY) { const f32x2 h_ = glu2(acc[(k) >> 2][2 + (((k) >> 1) & 1)][2 * ((k) & 1)], acc[(k) >> 2][2 + (((k) >> 1) & 1)][2 * ((k) & 1) + 1], acc[(k) >> 2][4 + (((k) >> 1) & 1)][2 * ((k) & 1)], acc[(k) >> 2][4 + (((k) >> 1) & 1)][2 * ((k) & 1) + 1]); \
      unsigned w_ = cvtpk(h_[0], h_[1]); PIN(w_); ha[(k) >> 2][(k) & 3] = w_; } } while (0)
#define G1_SY(u, i) do { if (isY) store_u(u, i); } while (0)
#define G1_RD(d, st, s, n)                                                                                   \
  do {                                                                                                       \
    LAS const char* p_ = LP((st) ? (((n) & 1) ? wa11 : wa10) : (((n) & 1) ? wa01 : wa00)) + ((n) >> 1) * 8192 + (s) * 4096; \
    wl[d] = ldstr(p_); wh[d] = ldstr(p_ + 512);                                                              \
  } while (0)
#define G1_MD(d, s, n, m)                                                                                    \
  do {                                                                                                       \
    if (SWAP0 && (n) < 2) mfma16a(acc[m][n], xf[s][m], cat8(wl[d], wh[d]));                                   \
    else mfma16a(acc[m][n], cat8(wl[d], wh[d]), xf[s][m]);                                                    \
  } while (0)
#define G1_GD(d, s, n, A, B, C) do { G1_MD(d, s, n, 0); SBAR(); A; SBAR(); G1_MD(d, s, n, 1); SBAR(); B; SBAR(); G1_MD(d, s, n, 2); SBAR(); C; SBAR(); G1_MD(d, s, n, 3); SBAR(); } while (0)
    G1_GD(0, 0, 0, G1_RD(9, 0, 1, 0), G1_RX(0, 1, 0), NOP_);
    G1_GD(1, 0, 1, G1_RD(0, 0, 1, 1), G1_RX(0, 1, 1), NOP_);
    G1_GD(2, 0, 2, G1_RD(1, 0, 1, 2), G1_RX(0, 1, 2), NOP_);
    G1_GD(3, 0, 3, G1_RD(2, 0, 1, 3), G1_RX(0, 1, 3), NOP_);
    G1_GD(4, 0, 4, G1_RD(3, 0, 1, 4), NOP_, NOP_);
    G1_GD(5, 0, 5, G1_RD(4, 0, 1, 5), NOP_, NOP_);
    G1_GD(9, 1, 0, NOP_, NOP_, NOP_);
    asm volatile("s_waitcnt vmcnt(0)\n\ts_barrier" ::: "memory");
    G1_GD(0, 1, 1, G1_RX(1, 0, 0), G1_RD(5, 1, 0, 0), G1_RX(1, 0, 1));
    G1_GD(1, 1, 2, G1_RX(1, 0, 2), G1_RD(6, 1, 0, 1), G1_RX(1, 0, 3));
    G1_GD(2, 1, 3, G1_RD(7, 1, 1, 0), G1_RD(8, 1, 1, 1), NOP_);
    G1_GD(3, 1, 4, G1_RD(9, 1, 0, 2), G1_RD(0, 1, 0, 3), NOP_);
    G1_GD(4, 1, 5, G1_RD(1, 1, 1, 2), G1_RD(2, 1, 1, 3), NOP_);
    G1_GD(5, 0, 0, { G1_RX(1, 1, 0); G1_RX(1, 1, 1); }, { G1_RX(1, 1, 2); G1_RX(1, 1, 3); }, G1_RD(3, 1, 0, 4));
    G1_GD(6, 0, 1, G1_RD(4, 1, 0, 5), NOP_, NOP_);
    G1_GD(9, 0, 2, G1_RD(5, 1, 1, 4), NOP_, NOP_);
    G1_GD(7, 1, 0, G1_RD(6, 1, 1, 5), NOP_, NOP_);
    G1_GD(8, 1, 1, G1_RD(9, 1, 0, 6), NOP_, NOP_);
    G1_GD(0, 0, 3, G1_RD(7, 1, 0, 7), NOP_, NOP_);
    G1_GD(1, 1, 2, G1_RD(8, 1, 0, 8), NOP_, store_addr());
    G1_GD(2, 1, 3, G1_RD(0, 1, 0, 9), store_u(0, 0), store_u(0, 1));
    G1_GD(3, 0, 4, G1_RD(1, 1, 1, 6), store_u(0, 2), store_u(0, 3));
    G1_GD(4, 0, 5, G1_RD(2, 1, 1, 7), NOP_, NOP_);
    G1_GD(5, 1, 4, G1_RD(3, 1, 1, 8), G1_SY(1, 0), G1_SY(1, 1));
    G1_GD(6, 1, 5, G1_RD(4, 1, 1, 9), G1_SY(1, 2), G1_SY(1, 3));
    G1_GD(9, 0, 6, G1_RD(5, 0, 0, 6), NOP_, NOP_);
    G1_GD(7, 0, 7, G1_RD(6, 0, 0, 7), NOP_, G1_HA(0));
    G1_GD(8, 0, 8, G1_RD(9, 0, 0, 8), { G1_SY(2, 0); G1_HA(1); }, G1_SY(2, 1));
    G1_GD(0, 0, 9, G1_RD(7, 0, 0, 9), { G1_SY(2, 2); G1_HA(2); }, G1_SY(2, 3));
    G1_GD(1, 1, 6, { G1_RX(0, 0, 0); G1_RX(0, 0, 1); }, G1_RD(8, 0, 1, 6), G1_HA(3));
    G1_GD(2, 1, 7, { G1_RX(0, 0, 2); G1_RX(0, 0, 3); }, G1_RD(0, 0, 1, 7), G1_HA(4));
    G1_GD(3, 1, 8, G1_RD(1, 0, 1, 8), G1_HA(5), G1_HA(6));
    G1_GD(4, 1, 9, G1_RD(2, 0, 1, 9), G1_HA(7), NOP_);
    G1_GD(5, 0, 6, { G1_RX(0, 1, 0); G1_RX(0, 1, 1); }, { G1_RX(0, 1, 2); G1_RX(0, 1, 3); }, G1_HA(8));
    G1_GD(6, 0, 7, G1_HA(9), NOP_, NOP_);
    G1_GD(9, 0, 8, G1_HA(10), NOP_, NOP_);
    G1_GD(7, 0, 9, G1_HA(11), NOP_, NOP_);
    G1_WAITBAR();
    G1_GD(8, 1, 6, if (wid == 7) publish(ws, FLG_F2 + mt * 32 + j, tag, lane_id()), G1_HA(12), NOP_);
#define G1_PF(k) do { if (wc) { const unsigned vw_ = g2_vw(lane_id(), cg); _Pragma("unroll") for (int i = 4 * (k); i < 4 * (k) + 8; ++i) dma16<false>(mksrd(wd, F * D * 2), vw_, (unsigned)(28 * wr) * 65536u + i * 16384u, ldsb + P2_G2RING + wr * 16384u + i * 1024u); } } while (0)
    G1_GD(0, 1, 7, G1_HA(13), G1_PF(0), NOP_);
    G1_GD(1, 1, 8, G1_HA(14), G1_PF(2), NOP_);
    G1_GD(2, 1, 9, G1_HA(15), NOP_, NOP_);
    if (wc == 0) { __builtin_amdgcn_s_setprio(1); dma16<true>(mksrd(ws, WS_XN), att_dep(head, mt, lane_id()) * (8u * FST), 0u, ldsb + LDS_DUMP + wid * 1024u); }
  }
  asm volatile("s_nop 7" ::: "memory"); SBAR();
#pragma unroll
  for (int m = 0; m < 4; ++m)
#pragma unroll
    for (int n = 0; n < 4; ++n) accb[m][n] = acc[m][6 + n];
  STAMP(5);
}

DI void att_slow(const char* kv, const bf16x8 (&qf)[2][2], f32x16 (&o)[2], float (&linv)[2], int lane) {
  __builtin_amdgcn_fence(__ATOMIC_ACQUIRE, "agent");
#pragma unroll
  for (int m = 0; m < 2; ++m) {
    f32x16 oo = f32x16{}; float mref = -INFINITY, l = 0.f;
    for (int kt = 0; kt < 64; ++kt) {
      const char* p = kv + kt * 4096 + lane * 16;
      const bf16x8 k0 = *(const bf16x8*)p, k1 = *(const bf16x8*)(p + 1024), v0 = *(const bf16x8*)(p + 2048), v1 = *(const bf16x8*)(p + 3072);
      f32x16 s = MFMA32(k0, qf[m][0], f32x16{});
      s = MFMA32(k1, qf[m][1], s);
      float mx = s[0];
#pragma unroll
      for (int r = 1; r < 16; ++r) mx = fmaxf(mx, s[r]);
      mx = fmaxf(fmaxf(mx, __shfl_xor(mx, 32)), mref);
      const float f = EX2(mref - mx);
      mref = mx;
      float sum = 0.f;
#pragma unroll
      for (int r = 0; r < 16; ++r) { s[r] = EX2(s[r] - mref); sum += s[r]; oo[r] *= f; }
      l = l * f + sum;
      oo = MFMA32(v0, BF(pack8(s, 0, 1.f)), oo);
      oo = MFMA32(v1, BF(pack8(s, 1, 1.f)), oo);
    }
    o[m] = oo; linv[m] = 1.f / (l + __shfl_xor(l, 32));
  }
}
DI void lds_meet(unsigned v, unsigned need, unsigned addr) {
  unsigned left = SPIN_CAP;
  asm volatile("1:\n\tv_cmp_le_u32 vcc, %2, %0\n\ts_cmp_eq_u64 vcc, exec\n\ts_cbranch_scc1 2f\n\ts_sleep 1\n\tds_read_b32 %0, %3\n\ts_sub_u32 %1, %1, 1\n\ts_waitcnt lgkmcnt(0)\n\ts_cbranch_scc0 1b\n2:"
               : "+v"(v), "+s"(left) : "s"(need), "v"(addr) : "vcc", "scc", "memory");
}
DI void lds_meet_fast(unsigned v, unsigned need, unsigned addr) {
  unsigned left;
  asm volatile("v_cmp_le_u32 vcc, %2, %0\n\ts_cmp_eq_u64 vcc, exec\n\ts_cbranch_scc0 3f\n2:\n\t.subsection 1\n3:\n\ts_mov_b32 %1, 0x100000\n4:\n\ts_sleep 1\n\tds_read_b32 %0, %3\n\ts_waitcnt lgkmcnt(0)\n\t"
               "v_cmp_le_u32 vcc, %2, %0\n\ts_cmp_eq_u64 vcc, exec\n\ts_cbranch_scc1 2b\n\ts_sub_u32 %1, %1, 1\n\ts_cbranch_scc0 4b\n\ts_branch 2b\n\t.subsection 0"
               : "+v"(v), "=&s"(left) : "s"(need), "v"(addr) : "vcc", "scc", "memory");
}
DI void glu_pack(const f32x4 (&a)[4][4], u32x4 (&hp)[4]) {
#pragma unroll
  for (int m = 0; m < 4; ++m) {
#pragma unroll
    for (int k = 0; k < 4; ++k) { const f32x2 h = glu2(a[m][k >> 1][2 * (k & 1)], a[m][k >> 1][2 * (k & 1) + 1], a[m][2 + (k >> 1)][2 * (k & 1)], a[m][2 + (k >> 1)][2 * (k & 1) + 1]); hp[m][k] = cvtpk(h[0], h[1]); }
    PIN(hp[m]);
  }
}

DI void dma16x4_sc1(srd_t srd, unsigned voff, unsigned soff, unsigned m0v) {
  asm volatile("s_mov_b32 m0, %0\n\ts_nop 0\n\tbuffer_load_dwordx4 %1, %2, %3 offen sc1 lds\n\tbuffer_load_dwordx4 %1, %2, %3 offen offset:1024 sc1 lds\n\t"
               "buffer_load_dwordx4 %1, %2, %3 offen offset:2048 sc1 lds\n\tbuffer_load_dwordx4 %1, %2, %3 offen offset:3072 sc1 lds" ::"s"(m0v), "v"(voff), "s"(srd), "s"(soff) : "memory");
}
DI void att_role(const u16* wo, char* ws, LAS char* lds, int head, int qb, int tg, int cg, int wid, u64 tag, const u32x4 (&hp)[4], const u32x4 (&ha)[4]) {
  const int tz = lane_id();
  const int lane = tz & 63;
  const char* kvg = ws + WS_KV + (size_t)head * 262144;
  const srd_t skv = mksrd(kvg, 262144), sw = mksrd(ws, WS_END);
  const unsigned ldsb = (unsigned)(uintptr_t)lds;
  const unsigned voff = lane * 16u;
  volatile LAS unsigned* ctl = (volatile LAS unsigned*)(lds + LDS_CTL);
  const unsigned ctla = ldsb + LDS_CTL + (lane & 7) * 4u;
  {
#pragma unroll
    for (int i = 0; i < 4; ++i) { *(LAS u32x4*)(lds + 131072 + wid * 4096 + i * 1024 + lane * 16) = hp[i]; *(LAS u32x4*)(lds + 49152 + wid * 4096 + i * 1024 + lane * 16) = ha[i]; }
    asm volatile("s_waitcnt lgkmcnt(0)" ::: "memory");
    ctl[12 + wid] = 1u;
  }
#define AT_ARRIVE(k) ctl[wid] = (unsigned)((k) + 2)
#define AT_MEET(v, k) lds_meet(v, (unsigned)((k) + 2), ctla)
  f32x16 o[2] = {f32x16{}, f32x16{}};
  f32x4 ls[2] = {f32x4{}, f32x4{}};
  bf16x8 sel;
  { const short one = ((lane & 1) == ((lane >> 4) & 1) && (lane & 2) == 0) ? (short)0x3f80 : (short)0;
#pragma unroll
    for (int e = 0; e < 8; ++e) sel[e] = one; }
  bf16x8 kf[2][2], vf[2][2], qf[2][2];
  f32x16 sa, sb, negm[2];
  u32x4 pa0, pa1, pb0 = u32x4{}, pb1 = u32x4{};
  vf[1][0] = bf16x8{}; vf[1][1] = bf16x8{};
  PIN(sel); PIN(o[0]); PIN(o[1]); PIN(ls[0]); PIN(ls[1]); PIN(pb0); PIN(pb1); PIN(vf[1][0]); PIN(vf[1][1]);
  const unsigned kp = ldsb + lane * 16u;
  {
    asm volatile("s_waitcnt vmcnt(0)" ::: "memory");
    const u32x2 f = *(volatile LAS const u32x2*)(lds + LDS_DUMP + wid * 1024 + lane * 16);
    if (!__all(f[0] == (unsigned)tag && f[1] == (unsigned)(tag >> 32))) await(ws, att_dep(head, qb, lane), tag);
  }
  STAMP(6);
  dma16x4_sc1(skv, voff, wid * 4096u, ldsb + wid * 4096u);
  dma16x4_sc1(sw, voff, WS_Q + (head * 64 + qb * 8 + wid * 2) * 2048u, ldsb + 32768u + wid * 4096u);
  dma16x4_sc1(skv, voff, 16384u + wid * 4096u, ldsb + 16384u + wid * 4096u);
  asm volatile("s_waitcnt vmcnt(0)" ::: "memory");
  AT_ARRIVE(-1);
#pragma unroll
  for (int i = 0; i < 4; ++i) qf[i >> 1][i & 1] = lds128(LP(kp) + 32768 + wid * 4096 + i * 1024);
  asm volatile("" : "+v"(qf[0][0]), "+v"(qf[0][1]), "+v"(qf[1][0]), "+v"(qf[1][1]));
  lds_meet(ctl[lane & 3], 1u, ldsb + LDS_CTL + (lane & 3) * 4u);
  kf[0][0] = lds128(LP(kp)); kf[0][1] = lds128(LP(kp) + 1024);
  {
    sa = MFMA32(kf[0][0], qf[0][0], f32x16{}); sa = MFMA32(kf[0][1], qf[0][1], sa);
    sb = MFMA32(kf[0][0], qf[1][0], f32x16{}); sb = MFMA32(kf[0][1], qf[1][1], sb);
    dma16x4_sc1(skv, voff, 32768u + wid * 4096u, ldsb + 32768u + wid * 4096u);
    float ma = sa[0], mb = sb[0];
#pragma unroll
    for (int r = 1; r < 16; ++r) { ma = fmaxf(ma, sa[r]); mb = fmaxf(mb, sb[r]); }
    { const auto ra = __builtin_amdgcn_permlane32_swap(__float_as_uint(ma), __float_as_uint(ma), false, false), rb = __builtin_amdgcn_permlane32_swap(__float_as_uint(mb), __float_as_uint(mb), false, false);
      ma = fmaxf(__uint_as_float(ra[0]), __uint_as_float(ra[1])); mb = fmaxf(__uint_as_float(rb[0]), __uint_as_float(rb[1])); }
#pragma unroll
    for (int r = 0; r < 16; ++r) { negm[0][r] = -ma; negm[1][r] = -mb; sa[r] -= ma; }
  }
  PIN(negm[0]); PIN(negm[1]);
#define E1(S_, i) S_[i] = EX2(S_[i])
#define CV(P_, i, S_, r) P_[i] = cvtpk(S_[r], S_[(r) + 1])
#define AT_UNIT(SC, PC0, PC1, SN, kz, mn, PP0, PP1, vz, X0, X1, X2, X3)                                       \
  do {                                                                                                       \
    SN = MFMA32(kf[kz][0], qf[mn][0], negm[mn]); SBAR(); X0;                                                  \
    E1(SC, 0); E1(SC, 1); E1(SC, 2); E1(SC, 3); PIN(SC); SBAR();                                               \
    SN = MFMA32(kf[kz][1], qf[mn][1], SN); SBAR(); X1;                                                        \
    CV(PC0, 0, SC, 0); CV(PC0, 1, SC, 2); E1(SC, 4); E1(SC, 5); E1(SC, 6); PIN(SC); PIN(PC0); SBAR();           \
    o[mn] = MFMA32(vf[vz][0], BF(PP0), o[mn]); SBAR(); X2;                                                    \
    E1(SC, 7); CV(PC0, 2, SC, 4); E1(SC, 8); CV(PC0, 3, SC, 6); E1(SC, 9); PIN(SC); PIN(PC0); SBAR();           \
    ls[mn] = MFMA16(sel, BF(PP0), ls[mn]); SBAR();                                                            \
    E1(SC, 10); E1(SC, 11); PIN(SC); SBAR();                                                                  \
    o[mn] = MFMA32(vf[vz][1], BF(PP1), o[mn]); SBAR(); X3;                                                    \
    CV(PC1, 0, SC, 8); E1(SC, 12); CV(PC1, 1, SC, 10); E1(SC, 13); E1(SC, 14); E1(SC, 15); PIN(SC); PIN(PC1); SBAR(); \
    ls[mn] = MFMA16(sel, BF(PP1), ls[mn]); SBAR();                                                            \
    CV(PC1, 2, SC, 12); CV(PC1, 3, SC, 14); PIN(PC1); SBAR();                                                  \
  } while (0)
#define AT_KT(z, KADDR, VADDR, D0, D1)                                                                        \
  do {                                                                                                       \
    AT_UNIT(sa, pa0, pa1, sb, z, 1, pb0, pb1, z ^ 1, kf[z ^ 1][0] = lds128(KADDR), kf[z ^ 1][1] = lds128((KADDR) + 1024), vf[z][0] = lds128(VADDR), vf[z][1] = lds128((VADDR) + 1024)); \
    asm volatile("" : "+v"(kf[z ^ 1][0]), "+v"(kf[z ^ 1][1]), "+v"(vf[z][0]), "+v"(vf[z][1]));   \
    AT_UNIT(sb, pb0, pb1, sa, z ^ 1, 0, pa0, pa1, z, D0, NOP_, D1, NOP_);                                      \
  } while (0)
  const unsigned vwo = g2_vw(lane, cg), m0b = ldsb + wid * 4096u;
  unsigned arr = 2u, ctlw = ldsb + LDS_CTL + wid * 4u;
  PIN(arr); PIN(ctlw);
#define AT_INT(p, SRD, VO, SO, ST)                                                                            \
  do {                                                                                                       \
    unsigned pv;                                                                                             \
    AT_KT(0, LP(kp) + (p) * 16384 + 4096, LP(kp) + (p) * 16384 + 2048, NOP_, NOP_);                           \
    AT_KT(1, LP(kp) + (p) * 16384 + 8192, LP(kp) + (p) * 16384 + 6144, pv = ctl[lane & 7], NOP_);             \
    lds_meet_fast(pv, 4 * it + (p) + 1, ctla);     \
    SBAR();                                                                                                  \
    AT_KT(0, LP(kp) + (p) * 16384 + 12288, LP(kp) + (p) * 16384 + 10240, dma16<true>(SRD, VO, (SO), m0b + (((p) + 3) & 3) * 16384u), dma16<true>(SRD, VO, (SO) + (ST), m0b + (((p) + 3) & 3) * 16384u + 1024u)); \
    AT_KT(1, LP(kp) + (((p) + 1) & 3) * 16384, LP(kp) + (p) * 16384 + 14336, dma16<true>(SRD, VO, (SO) + 2 * (ST), m0b + (((p) + 3) & 3) * 16384u + 2048u), dma16<true>(SRD, VO, (SO) + 3 * (ST), m0b + (((p) + 3) & 3) * 16384u + 3072u)); \
    asm volatile("s_waitcnt vmcnt(4)\n\tds_write_b32 %1, %0\n\tv_add_u32 %0, 1, %0" : "+v"(arr) : "v"(ctlw) : "memory");     \
  } while (0)
  STAMP(7);
  for (int it = 0; it < 4; ++it) {
    const bool kvq = it < 3;
    const srd_t sq_ = mksrd(kvq ? (const void*)kvg : (const void*)wo, kvq ? 262144u : D * D * 2u);
    const unsigned sk_ = (4 * it + 3) * 16384u + wid * 4096u;
    const unsigned vq_ = kvq ? voff : vwo, so_ = kvq ? sk_ + 16384u : (8 * wid + 5) * 65536u, st_ = kvq ? 1024u : 16384u, sn_ = kvq ? 16384u : 65536u;
    AT_INT(0, skv, voff, sk_, 1024u);
    AT_INT(1, sq_, vq_, so_, st_);
    AT_INT(2, sq_, vq_, so_ + sn_, st_);
    AT_INT(3, sq_, vq_, so_ + 2 * sn_, st_);
  }
  STAMP(8);
  o[1] = MFMA32(vf[1][0], BF(pb0), o[1]); ls[1] = MFMA16(sel, BF(pb0), ls[1]);
  o[1] = MFMA32(vf[1][1], BF(pb1), o[1]); ls[1] = MFMA16(sel, BF(pb1), ls[1]);
  auto store_o = [&](int m, float inv) {
    const unsigned T = qb * 8 + wid * 2 + m;
    const unsigned so = WS_X2 + ((T >> 2) * NKB + 112 + head) * 8192u + (T & 3) * 512u, vo = (lane >> 5) * 2048u + (lane & 31) * 16u;
    st16_sc1(sw, vo, so, pack8(o[m], 0, inv));
    st16_sc1(sw, vo, so + 4096u, pack8(o[m], 1, inv));
  };
  float lt[2];
#pragma unroll
  for (int m = 0; m < 2; ++m) { lt[m] = (lane & 16) ? ls[m][1] : ls[m][0]; store_o(m, __builtin_amdgcn_rcpf(lt[m])); }
  bool bad = false;
#pragma unroll
  for (int m = 0; m < 2; ++m) {
    float chk = lt[m], nanp = lt[m];
#pragma unroll
    for (int r = 0; r < 16; ++r) { chk = fmaxf(chk, fabsf(o[m][r])); nanp += o[m][r]; }
    bad = bad || !(chk < 1e30f) || (nanp != nanp);
  }
  if (__builtin_expect(__any(bad), 0)) {
    float linv[2];
    asm volatile("s_waitcnt vmcnt(0)" ::: "memory");
    att_slow(kvg, qf, o, linv, lane);
    store_o(0, linv[0]); store_o(1, linv[1]);
  }
  asm volatile("s_waitcnt vmcnt(0)" ::: "memory");
  publish_last(ws, ldsb + LDS_CTL + 17 * 4u, 4u, FLG_F4 + qb * 32 + head, tag, lane);
  STAMP(9);
  await(ws, FLG_F4 + (tg >> 1) * 32 + 8 * wid + (lane & 7), tag);
  { unsigned pv = 0; AT_MEET(pv, 15); }
  ctl[8 + wid] = 1u;
  STAMP(10);
}

constexpr int g2_cnt(int m, bool x0, bool w0, bool x1, bool w1, bool w2) {
  return (7 - m) + (w2 ? (8 - m) >> 1 : 0) + (x1 ? 8 : 0) + (w1 ? 4 : 0) + (x0 ? m : 0) + (w0 ? (m + 1) >> 1 : 0);
}
#undef STW
#define STW (kq + 4)
DI void g2_role(const u16* x, u32x4 (&xr)[2], const u16* wo, const u16* wd, char* ws, LAS char* lds, int tg, int cg, int kq, u64 tag, const u32x4 (&hp)[4], const u32x4 (&ha)[4], int mt, int j, srd_t scode) {
  const int tz = lane_id();
  const int lane = tz & 63;
  const srd_t sx2 = mksrd(ws + WS_X2 + (size_t)tg * NKB * 8192, NKB * 8192);
  const srd_t swd = mksrd(wd, F * D * 2), swo = mksrd(wo, D * D * 2);
  const unsigned ldsw = (unsigned)(uintptr_t)lds + P2_G2RING + kq * 16384u;
  const unsigned vw = g2_vw(lane, cg);
  const int t16 = lane & 15, g = lane >> 4, q4 = t16 >> 2, p4 = lane & 3;
  const unsigned vx = g * 2048u + t16 * 16u;
  volatile LAS unsigned* ctl = (volatile LAS unsigned*)(lds + LDS_CTL);
  unsigned wa[4];
  f32x4 acc[8][4];
  {
    u32x4 hq[8];
    const srd_t sw = mksrd(ws, WS_END);
    const unsigned cb = (unsigned)(uintptr_t)lds + LDS_CTL;
    lds_meet(0u, 1u, cb + (12 + kq) * 4u);
#pragma unroll
    for (int i = 0; i < 8; ++i) hq[i] = *(LAS const u32x4*)(lds + (i < 4 ? 131072 : 49152 - 4096) + kq * 4096 + i * 1024 + lane * 16);
#pragma unroll
    for (int i = 0; i < 8; ++i) PIN(hq[i]);
    ctl[4 + kq] = ~0u;
#pragma unroll
    for (int c = 0; c < 4; ++c) { wa[c] = ldsw + (4 * g + q4) * 128 + ((c ^ ((2 * g + (q4 >> 1)) & 3)) << 5) + p4 * 8; PIN(wa[c]); }
#pragma unroll
    for (int m = 0; m < 8; ++m)
#pragma unroll
      for (int c = 0; c < 4; ++c) { acc[m][c] = f32x4{}; PIN(acc[m][c]); }
    lds_meet(0u, 1u, cb + (lane & 3) * 4u);
    const int pair = j < 24 ? 2 * j + 1 : 24 + j;
#pragma unroll
    for (int m = 0; m < 4; ++m) { st16_sc1(sw, vx, h_base(mt, pair, kq, 1) + m * 256u, hp[m]); st16_sc1(sw, vx, h_base(mt, pair, kq, 0) + m * 256u, hq[m]); }
    if (j < 24) {
#pragma unroll
      for (int m = 0; m < 4; ++m) { st16_sc1(sw, vx, h_base(mt, 2 * j, kq, 1) + m * 256u, ha[m]); st16_sc1(sw, vx, h_base(mt, 2 * j, kq, 0) + m * 256u, hq[4 + m]); }
    }
    asm volatile("s_waitcnt vmcnt(0)" ::: "memory");
    publish_last(ws, cb + 16 * 4u, 4u, FLG_F3 + mt * 32 + j, tag, lane);
  }
  if (blockIdx.x == 0 && kq == 0) {
    for (int i = 0; i < 4; ++i) await(ws, FLG_F1 + i * 64 + lane, tag);
    publish(ws, 0, tag, lane);
  }
  await(ws, FLG_F3 + (tg >> 1) * 32 + 7 * kq + (lane < (kq == 3 ? 11 : 7) ? lane : 0), tag);
  STAMP(6);
  bf16x8 xf[2][8];
  unsigned xo = (unsigned)(28 * kq) * 8192u, wo_ = (unsigned)(28 * kq + 4) * 65536u;
#pragma unroll
  for (int z = 0; z < 2; ++z) {
#pragma unroll
    for (int m = 0; m < 8; ++m) ld16<true>(xf[z][m], sx2, vx, xo, m * 256);
    xo += 8192u; }
  s16x4 wl[2][4], wh[2][4];
#define G2_RW(z, off, c) do { LAS const char* p_ = LP(wa[c]) + (off); wl[z][c] = ldstr(p_); wh[z][c] = ldstr(p_ + 2048); } while (0)
#pragma unroll
  for (int c = 0; c < 4; ++c) G2_RW(0, 0, c);
  SBAR();
#define G2_KB(sl, WS_, RD, X0, W0, X1, W1, W2) G2_KBX(sl, WS_, RD, (((sl) + 1) & 3) * 4096, X0, W0, X1, W1, W2, 0, NOP_)
#define G2_KBX(sl, WS_, RD, ROFF, X0, W0, X1, W1, W2, EXTRA, HOOK)                                                                \
  do {                                                                                                       \
    constexpr int z = (sl) & 1;                                                                              \
    _Pragma("unroll") for (int m = 0; m < 8; ++m) {                                                          \
      switch (m) {                                                                                           \
        case 0: mfma16w<g2_cnt(0, X0, W0, X1, W1, W2) + (EXTRA)>(acc[m][0], cat8(wl[z][0], wh[z][0]), xf[z][m]); break; \
        case 1: mfma16w<g2_cnt(1, X0, W0, X1, W1, W2) + (EXTRA)>(acc[m][0], cat8(wl[z][0], wh[z][0]), xf[z][m]); break; \
        case 2: mfma16w<g2_cnt(2, X0, W0, X1, W1, W2) + (EXTRA)>(acc[m][0], cat8(wl[z][0], wh[z][0]), xf[z][m]); break; \
        case 3: mfma16w<g2_cnt(3, X0, W0, X1, W1, W2) + (EXTRA)>(acc[m][0], cat8(wl[z][0], wh[z][0]), xf[z][m]); break; \
        case 4: mfma16w<g2_cnt(4, X0, W0, X1, W1, W2) + (EXTRA)>(acc[m][0], cat8(wl[z][0], wh[z][0]), xf[z][m]); break; \
        case 5: mfma16w<g2_cnt(5, X0, W0, X1, W1, W2) + (EXTRA)>(acc[m][0], cat8(wl[z][0], wh[z][0]), xf[z][m]); break; \
        case 6: mfma16w<g2_cnt(6, X0, W0, X1, W1, W2) + (EXTRA)>(acc[m][0], cat8(wl[z][0], wh[z][0]), xf[z][m]); break; \
        default: mfma16w<g2_cnt(7, X0, W0, X1, W1, W2) + (EXTRA)>(acc[m][0], cat8(wl[z][0], wh[z][0]), xf[z][m]); break; \
      }                                                                                                      \
      SBAR();                                                                                                \
      if (RD && m >= 2 && m < 6) G2_RW(z ^ 1, ROFF, m - 2);                                \
      SBAR();                                                                                                \
      mfma16a(acc[m][1], cat8(wl[z][1], wh[z][1]), xf[z][m]);                                                 \
      mfma16a(acc[m][2], cat8(wl[z][2], wh[z][2]), xf[z][m]);                                                 \
      mfma16a(acc[m][3], cat8(wl[z][3], wh[z][3]), xf[z][m]); SBAR();                                         \
      HOOK;                                                                                                  \
      if (X0) ld16_inplace(xf[z][m], sx2, vx, xo, m * 256);                                                  \
      if (W0 && !(m & 1)) dma16<false>(WS_, vw, wo_ + (m >> 1) * 16384u, ldsw + (sl) * 4096u + (m >> 1) * 1024u); \
      SBAR();                                                                                                \
    }                                                                                                        \
    xo += 8192u; wo_ += 65536u;                                                                              \
  } while (0)
#define YES true
#define NO false
  STAMP(7);
  G2_KB(0, swd, YES, YES, YES, YES, NO, NO); G2_KB(1, swd, YES, YES, YES, YES, YES, NO); G2_KB(2, swd, YES, YES, YES, YES, YES, YES); G2_KB(3, swd, YES, YES, YES, YES, YES, YES);
  for (int i = 4; i < 24; i += 4) {
    dma16<false>(scode, vx, (unsigned)((mt * 8192) + ((j >> 3) & 1) * 1024 + kq * 256), (unsigned)(uintptr_t)lds + LDS_DUMP + (4 + kq) * 1024u);
    G2_KB(0, swd, YES, YES, YES, YES, YES, YES); G2_KB(1, swd, YES, YES, YES, YES, YES, YES); G2_KB(2, swd, YES, YES, YES, YES, YES, YES); G2_KB(3, swd, YES, YES, YES, YES, YES, YES);
  }
  wo_ = (unsigned)(8 * kq) * 65536u;
  G2_KB(0, swo, YES, YES, YES, YES, YES, YES); G2_KB(1, swo, YES, YES, YES, YES, YES, YES);
  G2_KB(2, swo, YES, NO, YES, YES, YES, YES); G2_KB(3, swo, YES, NO, YES, NO, YES, YES);
  asm volatile("" : "+v"(wl[0][0]), "+v"(wh[0][0]), "+v"(wl[0][1]), "+v"(wh[0][1]), "+v"(wl[0][2]), "+v"(wh[0][2]), "+v"(wl[0][3]), "+v"(wh[0][3]));
#pragma unroll
  for (int p = 0; p < 4; ++p) dma16<false>(swo, vw, wo_ + p * 16384u, ldsw + p * 1024u);
  STAMP(8);
  lds_meet(0u, 1u, (unsigned)(uintptr_t)lds + LDS_CTL + (8 + kq) * 4u);
  STAMP(9);
  xo = (unsigned)(112 + 8 * kq) * 8192u;
#pragma unroll
  for (int z = 0; z < 2; ++z) {
#pragma unroll
    for (int m = 0; m < 8; ++m) ld16_inplace(xf[z][m], sx2, vx, xo, m * 256);
    xo += 8192u; }
  G2_KB(0, swo, YES, YES, NO, YES, NO, NO); G2_KB(1, swo, YES, YES, NO, YES, NO, NO); G2_KB(2, swo, YES, YES, NO, YES, NO, NO); G2_KB(3, swo, YES, YES, NO, YES, NO, NO);
#pragma unroll
  for (int c = 0; c < 4; ++c) wa[c] -= P2_G2RING + kq * 12288u;
  G2_KBX(0, swo, YES, 0, YES, NO, YES, NO, NO, 0, NOP_); G2_KBX(1, swo, YES, 16384, YES, NO, YES, NO, NO, 0, NOP_);
  G2_KBX(2, swo, YES, 32768, NO, NO, YES, NO, NO, 0, NOP_);
  const int ty = lane_id();
  { const srd_t sxi = mksrd(x, S * D * 2);
    const unsigned xv = (unsigned)((ty & 63) >> 3) * 2048u + (ty & 7) * 16u, xs = (unsigned)(tg * 128 + (4 + kq) * 16) * 2048u + cg * 128u;
    ld16<false>(xr[0], sxi, xv, xs, 0); ld16<false>(xr[1], sxi, xv, xs + 16384u, 0); }
  unsigned pa[4];
#pragma unroll
  for (int c = 0; c < 4; ++c) pa[c] = (unsigned)(uintptr_t)lds + kq * 4096u + (ty & 15) * 256u + (((4 * c + ((ty >> 4) & 3)) ^ (ty & 15)) << 4);
  const unsigned pown = P2_G2RING + kq * 12288u;
#define G2_PUT(mm) do { _Pragma("unroll") for (int c = 0; c < 4; ++c) *(LAS f32x4*)((LAS char*)(uintptr_t)((mm) < 4 ? pa[c] : pa[c] + pown) + ((mm) < 4 ? (mm) * 16384 : ((mm) - 4) * 4096)) = acc[mm][c]; } while (0)
  G2_KBX(3, swo, NO, 0, NO, NO, NO, NO, NO, 2, if (m >= 2) G2_PUT(m >= 2 ? m - 2 : 0));
  asm volatile("s_nop 7" ::: "memory");
  G2_PUT(6); G2_PUT(7);
  asm volatile("s_waitcnt vmcnt(0)" : "+v"(xr[0]), "+v"(xr[1]));
  STAMP(10);
}

#undef STW
#define STW wid
__global__ __launch_bounds__(512, 2) void fused_kernel(const u16* x, const u16* wqkv, const u16* wo, const u16* wg, const u16* wu, const u16* wd, char* ws, u16* out) {
  extern __shared__ __attribute__((aligned(16))) char smem[];
  LAS char* lds = (LAS char*)smem;
  const int b = blockIdx.x, xcd = b & 7, r = b >> 3, wid = rfl(threadIdx.x >> 6);
  u64 tag = *(const __attribute__((address_space(4))) u64*)(ws + WS_FLG);
  ((LAS unsigned*)(lds + LDS_CTL))[threadIdx.x & 31] = 0u;
  const int tg = (xcd >> 1) * 4 + (r & 3), cg = (xcd & 1) * 8 + (r >> 2);
  const int mt = r & 7, j = xcd + 8 * (r >> 3);
  srd_t scode;
  { u64 pc; unsigned len;
    asm volatile("s_getpc_b64 %0\n1:\n\ts_mov_b32 %1, .Lfk_code_end-1b" : "=s"(pc), "=s"(len));
    scode = mksrd((const void*)(pc & ~127ull), len); }
  STAMP(0);
  ln_row(x, ws, mt * 256 + j * 8 + wid, scode, (unsigned)((r * 8 + wid) & 127) * 1024u, (unsigned)(uintptr_t)lds + LDS_DUMP + wid * 1024u);
  asm volatile("s_waitcnt vmcnt(0)" : "+s"(tag)::"memory");
  tag = tag * 6364136223846793005ull + 1442695040888963407ull;
  STAMP(1);
  asm volatile("s_barrier" ::: "memory");
  if (wid == 0) publish(ws, FLG_F1 + mt * 32 + j, tag, lane_id());
  const int head = xcd * 4 + (r >> 3);
  f32x4 accb[4][4];
  u32x4 ha[4];
  if (j < 16) g1_body<0>(head, wo, wd, cg, wqkv, wg, wu, ws, lds, wid, mt, j, scode, tag, accb, ha);
  else if (j < 24) g1_body<1>(head, wo, wd, cg, wqkv, wg, wu, ws, lds, wid, mt, j, scode, tag, accb, ha);
  else g1_body<2>(head, wo, wd, cg, wqkv, wg, wu, ws, lds, wid, mt, j, scode, tag, accb, ha);
  u32x4 hp[4];
  glu_pack(accb, hp);
  u32x4 xr[2];
  if (wid >= 4) {
    g2_role(x, xr, wo, wd, ws, lds, tg, cg, wid - 4, tag, hp, ha, mt, j, scode);
  } else {
    att_role(wo, ws, lds, head, mt, tg, cg, wid, tag, hp, ha);
    const int ty = lane_id();
#pragma unroll
    for (int e = 0; e < 2; ++e) xr[e] = *(const u32x4*)(x + (size_t)(tg * 128 + wid * 16 + e * 8 + ((ty & 63) >> 3)) * D + cg * 64 + (ty & 7) * 8);
  }
  const int ln = lane_id();
  unsigned ra[4][4], vo = (unsigned)((tg * 128 + wid * 16 + (ln >> 3)) * D + cg * 64 + (ln & 7) * 8) * 2u;
  f32x4 sum[4];
#pragma unroll
  for (int i = 0; i < 4; ++i) {
    const unsigned tk = (i >> 1) * 8 + (ln >> 3);
#pragma unroll
    for (int k = 0; k < 4; ++k) { ra[i][k] = (unsigned)(uintptr_t)lds + (wid < 4 ? wid * 16384 + k * 4096 : P2_G2RING + (wid - 4) * 4096 + k * 16384) + tk * 256 + (((2 * (ln & 7) + (i & 1)) ^ tk) << 4); PIN(ra[i][k]); }
    sum[i] = f32x4{blo(xr[i >> 1][2 * (i & 1)]), bhi(xr[i >> 1][2 * (i & 1)]), blo(xr[i >> 1][2 * (i & 1) + 1]), bhi(xr[i >> 1][2 * (i & 1) + 1])};
    PIN(sum[i]);
  }
  PIN(vo);
  asm volatile("s_waitcnt lgkmcnt(0)\n\ts_barrier" ::: "memory");
  STAMP(11);
  f32x4 part[4][4];
#pragma unroll
  for (int i = 0; i < 4; ++i)
#pragma unroll
    for (int k = 0; k < 4; ++k) part[i][k] = *(LAS const f32x4*)LP(ra[i][k]);
  SBAR();
#pragma unroll
  for (int i = 0; i < 4; ++i) sum[i] += (part[i][0] + part[i][1]) + (part[i][2] + part[i][3]);
  st16_sc1(mksrd(out, S * D * 2), vo, 0u, pack44(sum[0], sum[1], 1.f));
  st16_sc1(mksrd(out, S * D * 2), vo, 8u * D * 2u, pack44(sum[2], sum[3], 1.f));
  STAMP(12);
  asm volatile(".Lfk_code_end:");
}

extern "C" void kernel_launch(void* const* d_in, const int* in_sizes, int n_in, void* d_out, int out_size, void* d_ws, size_t ws_size, hipStream_t stream) {
  (void)hipFuncSetAttribute((const void*)fused_kernel, hipFuncAttributeMaxDynamicSharedMemorySize, LDS_BYTES);
  fused_kernel<<<256, 512, LDS_BYTES, stream>>>((const u16*)d_in[0], (const u16*)d_in[1], (const u16*)d_in[2], (const u16*)d_in[4], (const u16*)d_in[5], (const u16*)d_in[6], (char*)d_ws, (u16*)d_out);
}
```

```cpp
#include <hip/hip_runtime.h>
#include <hip/hip_bf16.h>
#include <stdint.h>

typedef unsigned short u16;
typedef unsigned long long u64;
typedef short bf16x8 __attribute__((ext_vector_type(8)));
typedef short s16x4 __attribute__((ext_vector_type(4)));
typedef float f32x16 __attribute__((ext_vector_type(16)));
typedef float f32x4 __attribute__((ext_vector_type(4)));
typedef float f32x2 __attribute__((ext_vector_type(2)));
typedef unsigned u32x4 __attribute__((ext_vector_type(4)));
typedef unsigned u32x2 __attribute__((ext_vector_type(2)));
typedef __bf16 bf16x2_t __attribute__((ext_vector_type(2)));
typedef short v4i16_t __attribute__((ext_vector_type(4)));
typedef __amdgpu_buffer_rsrc_t srd_t;
typedef __attribute__((address_space(1))) u64 gu64;
#define LAS __attribute__((address_space(3)))
#define DI __device__ __forceinline__
#define SBAR() __builtin_amdgcn_sched_barrier(0)
#define PIN(x) asm volatile("" : "+v"(x))
#define NOP_ ((void)0)
#define LP(a) ((LAS const char*)(uintptr_t)(a))
#define MFMA32(a, b, c) __builtin_amdgcn_mfma_f32_32x32x16_bf16((a), (b), (c), 0, 0, 0)
#define MFMA16(a, b, c) __builtin_amdgcn_mfma_f32_16x16x32_bf16((a), (b), (c), 0, 0, 0)
#define EX2(v) __builtin_amdgcn_exp2f(v)
#define BF(x) __builtin_bit_cast(bf16x8, x)
#define RLX_AGENT __ATOMIC_RELAXED, __HIP_MEMORY_SCOPE_AGENT

constexpr int S = 2048, D = 1024, F = 3584;
constexpr int NKB = (F + D) / 32;
constexpr float LOG2E = 1.4426950408889634f;
constexpr float QSCALE = 0.17677669529663687f * LOG2E;

constexpr unsigned WS_FLG = 0;
constexpr unsigned FLG_F1 = 256, FLG_F2 = FLG_F1 + 2048, FLG_F3 = FLG_F2 + 2048, FLG_F4 = FLG_F3 + 2048;
constexpr unsigned FST = 16;
constexpr unsigned WS_XN = 2097152;
constexpr unsigned WS_Q = WS_XN + S * D * 2;
constexpr unsigned WS_KV = WS_Q + S * D * 2;
constexpr unsigned WS_X2 = WS_KV + S * D * 4;
constexpr unsigned WS_END = WS_X2 + S * (F + D) * 2;
constexpr int G1_XS = 32768, G1_WB = 65536, G1_WS = 40960;
constexpr int P2_G2RING = 65536;
constexpr int LDS_CTL = G1_WB + 2 * G1_WS, LDS_DUMP = LDS_CTL + 1024, LDS_BYTES = LDS_DUMP + 8192;
constexpr unsigned PFD = 4;
constexpr unsigned SPIN_CAP = 1u << 20;

DI unsigned rfl(unsigned x) { return __builtin_amdgcn_readfirstlane(x); }
DI int lane_id() { int l; asm volatile("v_mbcnt_lo_u32_b32 %0, -1, 0\n\tv_mbcnt_hi_u32_b32 %0, -1, %0" : "=v"(l)); return l; }
DI unsigned cvtpk(float lo, float hi) { f32x2 v = {lo, hi}; bf16x2_t b = __builtin_convertvector(v, bf16x2_t); return __builtin_bit_cast(unsigned, b); }
DI float rbf(float f) { return __uint_as_float(cvtpk(f, f) << 16); }
DI float blo(unsigned u) { return __uint_as_float(u << 16); }
DI float bhi(unsigned u) { return __uint_as_float(u & 0xffff0000u); }
DI srd_t mksrd(const void* p, unsigned bytes) { return __builtin_amdgcn_make_buffer_rsrc((void*)p, 0, bytes, 0x00020000); }
template <bool SC1> DI void dma16(srd_t srd, unsigned voff, unsigned soff, unsigned m0v) {
  if (SC1) asm volatile("s_mov_b32 m0, %0\n\ts_nop 0\n\tbuffer_load_dwordx4 %1, %2, %3 offen sc1 lds" ::"s"(m0v), "v"(voff), "s"(srd), "s"(soff) : "memory");
  else asm volatile("s_mov_b32 m0, %0\n\ts_nop 0\n\tbuffer_load_dwordx4 %1, %2, %3 offen lds" ::"s"(m0v), "v"(voff), "s"(srd), "s"(soff) : "memory");
}
template <bool SC1, int IMM> DI void dma16i(srd_t srd, unsigned voff, unsigned soff, unsigned m0b) {
  if (SC1) asm volatile("s_add_u32 m0, %0, %4\n\ts_nop 0\n\tbuffer_load_dwordx4 %1, %2, %3 offen sc1 lds" ::"s"(m0b), "v"(voff), "s"(srd), "s"(soff), "i"(IMM) : "scc", "memory");
  else asm volatile("s_add_u32 m0, %0, %4\n\ts_nop 0\n\tbuffer_load_dwordx4 %1, %2, %3 offen lds" ::"s"(m0b), "v"(voff), "s"(srd), "s"(soff), "i"(IMM) : "scc", "memory");
}
template <bool SC1, class T> DI void ld16(T& d, srd_t srd, unsigned voff, unsigned soff, int imm) {
  if (SC1) asm volatile("buffer_load_dwordx4 %0, %1, %2, %3 offen offset:%4 sc1" : "=v"(d) : "v"(voff), "s"(srd), "s"(soff), "i"(imm) : "memory");
  else asm volatile("buffer_load_dwordx4 %0, %1, %2, %3 offen offset:%4" : "=v"(d) : "v"(voff), "s"(srd), "s"(soff), "i"(imm) : "memory");
}
template <class T> DI void ld16_inplace(T& d, srd_t srd, unsigned voff, unsigned soff, int imm) {
  asm volatile("buffer_load_dwordx4 %0, %1, %2, %3 offen offset:%4 sc1" : "+v"(d) : "v"(voff), "s"(srd), "s"(soff), "i"(imm) : "memory");
}
DI void st16_sc1(srd_t srd, unsigned voff, unsigned soff, u32x4 v) {
  asm volatile("buffer_store_dwordx4 %0, %1, %2, %3 offen sc1\n\ts_nop 1" ::"v"(v), "v"(voff), "s"(srd), "s"(soff) : "memory");
}
DI s16x4 ldstr(LAS const char* p) { return __builtin_bit_cast(s16x4, __builtin_amdgcn_ds_read_tr16_b64_v4i16((LAS v4i16_t*)p)); }
DI bf16x8 lds128(LAS const char* p) { return *(LAS const bf16x8*)p; }
DI bf16x8 cat8(s16x4 lo, s16x4 hi) { return __builtin_shufflevector(lo, hi, 0, 1, 2, 3, 4, 5, 6, 7); }
DI u32x4 pack8(const f32x16& v, int s, float sc) {
  u32x4 w;
  w[0] = cvtpk(v[8 * s + 0] * sc, v[8 * s + 1] * sc); w[1] = cvtpk(v[8 * s + 2] * sc, v[8 * s + 3] * sc);
  w[2] = cvtpk(v[8 * s + 4] * sc, v[8 * s + 5] * sc); w[3] = cvtpk(v[8 * s + 6] * sc, v[8 * s + 7] * sc);
  return w;
}
DI u32x4 pack44(const f32x4& a, const f32x4& b, float sc) {
  u32x4 w; w[0] = cvtpk(a[0] * sc, a[1] * sc); w[1] = cvtpk(a[2] * sc, a[3] * sc); w[2] = cvtpk(b[0] * sc, b[1] * sc); w[3] = cvtpk(b[2] * sc, b[3] * sc); return w;
}
DI void mfma16a(f32x4& c, const bf16x8& a, const bf16x8& b) { asm volatile("v_mfma_f32_16x16x32_bf16 %0, %1, %2, %0" : "+v"(c) : "v"(a), "v"(b)); }
template <int N> DI void mfma16w(f32x4& c, const bf16x8& a, const bf16x8& b) { asm volatile("s_waitcnt vmcnt(%3)\n\tv_mfma_f32_16x16x32_bf16 %0, %1, %2, %0" : "+v"(c) : "v"(a), "v"(b), "i"(N)); }
template <int CTRL, int RM> DI float dppadd(float v) { return v + __builtin_bit_cast(float, __builtin_amdgcn_update_dpp(0, __builtin_bit_cast(int, v), CTRL, RM, 0xf, true)); }
DI float wave_sum(float v) {
  v = dppadd<0x111, 0xf>(v); v = dppadd<0x112, 0xf>(v); v = dppadd<0x114, 0xf>(v); v = dppadd<0x118, 0xf>(v);
  v = dppadd<0x142, 0xa>(v); v = dppadd<0x143, 0xc>(v);
  return __builtin_bit_cast(float, __builtin_amdgcn_readlane(__builtin_bit_cast(int, v), 63));
}
DI void publish(char* ws, unsigned idx, u64 tag, int lane) {
  const u32x2 tv = {(unsigned)tag, (unsigned)(tag >> 32)};
  asm volatile("buffer_store_dwordx2 %0, %1, %2, %3 offen sc1" ::"v"(tv), "v"(lane == 0 ? 0u : 0x7ff00000u), "s"(mksrd(ws + WS_FLG, WS_XN)), "s"(idx * (8u * FST)) : "memory");
}
DI void publish_last(char* ws, unsigned cnt_addr, unsigned n, unsigned idx, u64 tag, int lane) {
  unsigned old;
  asm volatile("ds_add_rtn_u32 %0, %1, %2\n\ts_waitcnt lgkmcnt(0)" : "=v"(old) : "v"(cnt_addr), "v"(lane == 0 ? 1u : 0u) : "memory");
  if (rfl(old) == n - 1) publish(ws, idx, tag, lane);
}
DI void await(char* ws, unsigned idx, u64 tag) {
  gu64* p = (gu64*)(ws + WS_FLG) + idx * FST;
#pragma nounroll
  for (unsigned spins = 0; spins < SPIN_CAP; ++spins) {
    if (__all(__hip_atomic_load(p, RLX_AGENT) == tag)) break;
    __builtin_amdgcn_s_sleep(1);
  }
  asm volatile("" ::: "memory");
}

DI unsigned g2_vw(int lane, int cg) { const unsigned l3 = lane >> 3, l7 = lane & 7; return l3 * 2048u + ((l7 ^ (((l3 >> 1) & 3) << 1)) << 4) + cg * 128u; }

DI void ln_row(const u16* x, char* ws, int row, srd_t scode, unsigned code_off, unsigned dump) {
  const int lane = lane_id();
  const srd_t sxi = mksrd(x, S * D * 2), sx = mksrd(ws + WS_XN, S * D * 2);
  u32x4 a, b;
  ld16<false>(a, sxi, lane * 16u, row * 2048u, 0); ld16<false>(b, sxi, lane * 16u, row * 2048u, 1024);
  dma16<false>(scode, lane * 16u, code_off, dump);
  asm volatile("s_waitcnt vmcnt(1)" : "+v"(a), "+v"(b));
  f32x2 v[8];
#pragma unroll
  for (int i = 0; i < 4; ++i) { v[i] = f32x2{blo(a[i]), bhi(a[i])}; v[4 + i] = f32x2{blo(b[i]), bhi(b[i])}; }
  const f32x2 s2 = ((v[0] + v[1]) + (v[2] + v[3])) + ((v[4] + v[5]) + (v[6] + v[7]));
  const float mean = wave_sum(s2[0] + s2[1]) * (1.f / D);
  f32x2 q[4];
#pragma unroll
  for (int i = 0; i < 4; ++i) { const f32x2 d0 = v[i] - mean, d1 = v[4 + i] - mean; q[i] = __builtin_elementwise_fma(d1, d1, d0 * d0); }
  const f32x2 q2 = (q[0] + q[1]) + (q[2] + q[3]);
  const float mb = rbf(mean);
  unsigned dn[8];
#pragma unroll
  for (int i = 0; i < 8; ++i) { const f32x2 d = v[i] - mb; dn[i] = cvtpk(d[0], d[1]); }
  const float vb = rbf(wave_sum(q2[0] + q2[1]) * (1.f / D));
  const float inv = __builtin_amdgcn_rcpf(rbf(__builtin_amdgcn_sqrtf(rbf(vb + 1e-5f))));
  u32x4 oa, ob;
#pragma unroll
  for (int i = 0; i < 4; ++i) { oa[i] = cvtpk(blo(dn[i]) * inv, bhi(dn[i]) * inv); ob[i] = cvtpk(blo(dn[4 + i]) * inv, bhi(dn[4 + i]) * inv); }
  st16_sc1(sx, lane * 16u, row * 2048u, oa); st16_sc1(sx, lane * 16u + 1024u, row * 2048u, ob);
}

DI f32x2 glu2(float g0, float g1, float u0, float u1) { return f32x2{u0 * __builtin_amdgcn_rcpf(1.f + EX2(g0 * -LOG2E)), u1 * __builtin_amdgcn_rcpf(1.f + EX2(g1 * -LOG2E))}; }
DI unsigned h_base(int mt, int pair, int wr, int wc) { return WS_X2 + ((mt * 2 + (wr >> 1)) * NKB + 2 * pair + wc) * 8192u + (wr & 1) * 1024u; }
DI unsigned att_dep(int head, int qb, int lane) {
  const unsigned u = head >> 1, jy = 24 + ((u + 16) * 43 >> 7) - 8, jq = u < 8 ? 16 + u : jy, jk = 24 + ((u + 8) * 43 >> 7), jv = u;
  unsigned ikv = (lane & 7) * 32 + ((lane & 8) ? jk : jv); PIN(ikv);
  return FLG_F2 + (lane < 16 ? ikv : qb * 32 + jq);
}
template <int KIND>
DI void g1_body(int head, const u16* wo, const u16* wd, int cg, const u16* wqkv, const u16* wg, const u16* wu, char* ws, LAS char* lds, int wid, int mt, int j, srd_t scode, u64 tag, f32x4 (&accb)[4][4], u32x4 (&ha)[4]) {
  const int lane = lane_id(), wr = wid & 3, wc = wid >> 2;
  constexpr bool SWAP0 = KIND == 0, isY = KIND == 2;
  const int a0 = KIND == 0 ? 32 + j : (KIND == 1 ? j - 16 : 3 * j - 64);
  const srd_t sx = mksrd(ws + WS_XN, S * D * 2), sq = mksrd(wqkv, D * 3 * D * 2), sg = mksrd(wg, D * F * 2), su = mksrd(wu, D * F * 2);
  const srd_t sa = isY ? sq : sg, sb = isY ? sq : su;
  const unsigned ld1 = isY ? 6144u : 7168u;
  const unsigned c0 = a0 * 128u, c1 = isY ? c0 + 128u : j * 256u, c2 = isY ? c0 + 256u : j * 256u, c3 = isY ? (24 + j) * 128u : j * 256u + 128u;
  const unsigned l3 = lane >> 3, l7 = lane & 7, l4 = lane >> 4;
  const unsigned vx0 = l3 * 2048u + ((l7 ^ l4) << 4), vx1 = vx0 ^ 64u;
  const unsigned wsw = (l7 ^ (((l4 & 1) | ((wid & 1) << 1)) << 1)) << 4;
  const unsigned vw0 = l3 * 6144u + wsw, vw2 = l3 * 7168u + wsw;
  const unsigned vw1 = isY ? vw0 : vw2;
  const unsigned ldsb = (unsigned)(uintptr_t)lds;
  unsigned sox = (unsigned)(mt * 256 + wid * 32) * 2048u;
  unsigned so0 = wid * 8 * 6144u + c0, so1 = wid * 8 * ld1, so2 = wid * 8 * 7168u + c3;
  const unsigned m0x = ldsb + wid * 4096u, m0w = ldsb + G1_WB + wid * 1024u;
#define G1_DX(i, st) dma16i<true, (st) * G1_XS + (i) * 1024>(sx, ((i) & 1) ? vx1 : vx0, sox + (i) * 16384u, m0x)
#define G1_DW(i, st)                                                                             \
  do {                                                                                           \
    if ((i) == 0) dma16i<false, (st) * G1_WS>(sq, vw0, so0, m0w);                                 \
    if ((i) == 1) dma16i<false, (st) * G1_WS + 8192>(sa, vw1, so1 + c1, m0w);                    \
    if ((i) == 2) dma16i<false, (st) * G1_WS + 16384>(sb, vw1, so1 + c2, m0w);                   \
    if ((i) == 3) dma16i<false, (st) * G1_WS + 24576>(sg, vw2, so2, m0w);                        \
    if ((i) == 4) dma16i<false, (st) * G1_WS + 32768>(su, vw2, so2, m0w);                        \
  } while (0)
#define G1_ADVX() do { sox += 128u; } while (0)
#define G1_ADVW() do { so0 += 64u * 6144u; so1 += 64u * ld1; so2 += 64u * 7168u; } while (0)
#define G1_CODE() dma16<false>(scode, vx0, (unsigned)(mt * 16384 + ((j >> 3) & 1) * 1024 + wid * 128), ldsb + LDS_DUMP + wid * 1024u)
  const srd_t sp = wid == 0 ? sq : (wid == 1 ? sa : (wid == 2 ? sb : (wid == 3 ? sg : su)));
  const unsigned ldp = wid == 0 ? 6144u : (wid < 3 ? ld1 : 7168u), cp = wid == 0 ? c0 : (wid == 1 ? c1 : (wid == 2 ? c2 : (wid < 5 ? c3 : 0x7ff00000u)));
  const unsigned vwp = ldp == 6144u ? vw0 : vw2, idp = (mt * 32 + j) * 8 + wid;
  unsigned sop = cp + (PFD * 64u + mt * 8u) * ldp, rowd = (idp >> 4) * 8u;
#define G1_WARMW() do { dma16<false>(sp, vwp, sop, ldsb + LDS_DUMP + wid * 1024u); sop += 64u * ldp; } while (0)
#define G1_WARMD() do { const bool d_ = rowd < (unsigned)F; dma16<false>(mksrd(d_ ? wd : wo, d_ ? F * D * 2 : D * D * 2), vx0, (d_ ? rowd : rowd - F) * 2048u + (idp & 15) * 128u, ldsb + LDS_DUMP + wid * 1024u); rowd += 1024u; } while (0)
  f32x4 acc[4][10];
  G1_DW(0, 0); G1_DW(1, 0); G1_DW(2, 0); G1_DW(3, 0); G1_DW(4, 0); G1_ADVW();
  asm volatile("s_mov_b32 m0, %0\n\ts_nop 0\n\tbuffer_load_dword %1, %2, %3 offen lds" ::"s"(ldsb + LDS_DUMP + wid * 1024u), "v"(l3 + 1u < PFD ? ((l3 + 1u) * 64u + mt * 8u + l7) * ldp : 0x7ff00000u), "s"(sp), "s"(cp) : "memory");
#pragma unroll
  for (int m = 0; m < 4; ++m)
#pragma unroll
    for (int n = 0; n < 10; ++n) { acc[m][n] = f32x4{}; PIN(acc[m][n]); }
  await(ws, FLG_F1 + mt * 32 + wid * 4 + (lane & 3), tag);
  G1_DX(0, 0); G1_DX(1, 0); G1_DX(2, 0); G1_DX(3, 0); G1_ADVX();
  G1_DX(0, 1); G1_DX(1, 1); G1_DX(2, 1); G1_DX(3, 1); G1_ADVX();
  G1_DW(0, 1); G1_DW(1, 1); G1_DW(2, 1); G1_DW(3, 1); G1_DW(4, 1); G1_ADVW();
  const int t16 = lane & 15, g = lane >> 4, q4 = t16 >> 2, p4 = lane & 3;
  unsigned xa0 = ldsb + (wr * 64 + t16) * 128 + ((g ^ ((t16 >> 1) & 7)) << 4), xa1 = xa0 ^ 64u;
  unsigned wa00 = ldsb + G1_WB + (8 * g + q4) * 128 + (((2 * wc) ^ (((q4 >> 1) & 1) | ((g & 1) << 1))) << 5) + p4 * 8;
  unsigned wa01 = wa00 ^ 32u, wa10 = wa00 + G1_WS, wa11 = wa01 + G1_WS;
  asm volatile("" : "+v"(xa0), "+v"(xa1), "+v"(wa00), "+v"(wa01), "+v"(wa10), "+v"(wa11));
  bf16x8 xf[2][4];
  s16x4 wl[10], wh[10];
#define G1_RX(st, s, m) xf[s][m] = lds128(LP((s) ? xa1 : xa0) + (st) * G1_XS + (m) * 2048)
#define G1_RW(st, s, n)                                                                                      \
  do {                                                                                                       \
    LAS const char* p_ = LP((st) ? (((n) & 1) ? wa11 : wa10) : (((n) & 1) ? wa01 : wa00)) + ((n) >> 1) * 8192 + (s) * 4096; \
    wl[n] = ldstr(p_); wh[n] = ldstr(p_ + 512);                                                              \
  } while (0)
#define G1_MM(s, n, m)                                                                                       \
  do {                                                                                                       \
    if (SWAP0 && (n) < 2) mfma16a(acc[m][n], xf[s][m], cat8(wl[n], wh[n]));                                   \
    else mfma16a(acc[m][n], cat8(wl[n], wh[n]), xf[s][m]);                                                    \
  } while (0)
#define G1_GRP(s, n, A, B, C) do { G1_MM(s, n, 0); SBAR(); A; SBAR(); G1_MM(s, n, 1); SBAR(); B; SBAR(); G1_MM(s, n, 2); SBAR(); C; SBAR(); G1_MM(s, n, 3); SBAR(); } while (0)
#define G1_WAITBAR() G1_WAITN(0)
#define G1_WAITN(N) asm volatile("s_waitcnt vmcnt(%0) lgkmcnt(0)\n\ts_barrier" ::"i"(N) : "memory")
#define G1_HEAD(st)                                                                                          \
  do {                                                                                                       \
    G1_GRP(0, 0, G1_RW(st, 0, 9), NOP_, NOP_);                                                               \
    G1_GRP(0, 1, G1_RW(st, 1, 0), G1_RX(st, 1, 0), NOP_);                                                    \
    G1_GRP(0, 2, G1_RW(st, 1, 1), G1_RX(st, 1, 1), NOP_);                                                    \
    G1_GRP(0, 3, G1_RW(st, 1, 2), G1_RX(st, 1, 2), NOP_);                                                    \
    G1_GRP(0, 4, G1_RW(st, 1, 3), G1_RX(st, 1, 3), NOP_);                                                    \
    G1_GRP(0, 5, G1_RW(st, 1, 4), NOP_, NOP_);                                                               \
    G1_GRP(0, 6, G1_RW(st, 1, 5), NOP_, G1_WARMW());                                                           \
    G1_GRP(0, 7, G1_RW(st, 1, 6), NOP_, NOP_);                                                               \
    G1_GRP(0, 8, G1_RW(st, 1, 7), NOP_, if (st) G1_WARMD(); else G1_CODE());                                                           \
    G1_GRP(0, 9, G1_RW(st, 1, 8), NOP_, NOP_);                                                               \
    G1_GRP(1, 0, G1_RW(st, 1, 9), NOP_, NOP_);                                                               \
    G1_GRP(1, 1, NOP_, NOP_, NOP_);                                                                          \
    G1_GRP(1, 2, NOP_, NOP_, NOP_);                                                                          \
    G1_WAITN(2);                                 \
  } while (0)
#define G1_TILE(st, DM)                                                                                      \
  do {                                                                                                       \
    G1_HEAD(st);                                                                                             \
    G1_GRP(1, 3, G1_RX(st ^ 1, 0, 0), G1_RW(st ^ 1, 0, 0), if (DM) G1_DX(0, st));                             \
    G1_GRP(1, 4, G1_RX(st ^ 1, 0, 1), G1_RW(st ^ 1, 0, 1), if (DM) G1_DW(0, st));                             \
    G1_GRP(1, 5, G1_RX(st ^ 1, 0, 2), G1_RW(st ^ 1, 0, 2), if (DM) G1_DX(1, st));                             \
    G1_GRP(1, 6, G1_RX(st ^ 1, 0, 3), G1_RW(st ^ 1, 0, 3), if (DM) G1_DW(1, st));                             \
    G1_GRP(1, 7, G1_RW(st ^ 1, 0, 4), G1_RW(st ^ 1, 0, 5), if (DM) { G1_DX(2, st); G1_DW(2, st); });          \
    G1_GRP(1, 8, G1_RW(st ^ 1, 0, 6), G1_RW(st ^ 1, 0, 7), if (DM) { G1_DX(3, st); G1_DW(3, st); });          \
    G1_GRP(1, 9, G1_RW(st ^ 1, 0, 8), NOP_, if (DM) { G1_DW(4, st); G1_ADVX(); G1_ADVW(); });                 \
  } while (0)
  asm volatile("s_waitcnt vmcnt(9)\n\ts_barrier" ::: "memory");
  G1_RX(0, 0, 0); G1_RW(0, 0, 0); G1_RX(0, 0, 1); G1_RX(0, 0, 2); G1_RX(0, 0, 3);
  G1_RW(0, 0, 1); G1_RW(0, 0, 2); G1_RW(0, 0, 3); G1_RW(0, 0, 4); G1_RW(0, 0, 5); G1_RW(0, 0, 6); G1_RW(0, 0, 7); G1_RW(0, 0, 8);
  SBAR();
  for (int t = 0; t < 14; t += 2) { G1_TILE(0, true); G1_TILE(1, true); }
  {
    const srd_t sw = mksrd(ws, WS_END);
    unsigned vq, vk;
    auto store_addr = [&]() { const unsigned l = lane_id(); vq = (l >> 4) * 512u + (l & 15) * 16u; vk = (l >> 4) * 512u + (2 * (l & 8) + (l & 7)) * 16u; };
    const unsigned T32 = mt * 8 + wr * 2;
    auto store_u = [&](int u, int i) {
      const int a = a0 + u; const unsigned head = 2 * (a & 15) + wc; const bool isq = a < 16;
      if (SWAP0) st16_sc1(sw, vk, WS_KV + (head * 64 + T32 + (i >> 1)) * 4096u + 2048u + (i & 1) * 128u, pack44(acc[2 * (i >> 1)][i & 1], acc[2 * (i >> 1) + 1][i & 1], 1.f));
      else st16_sc1(sw, isq ? vq : vk, isq ? WS_Q + (head * 64 + T32 + (i >> 1)) * 2048u + (i & 1) * 256u : WS_KV + (head * 64 + T32 + (i >> 1)) * 4096u + (i & 1) * 128u,
                    pack44(acc[i][2 * u], acc[i][2 * u + 1], isq ? QSCALE : 1.f));
    };
#define G1_HA(k) do { if (!isY) { const f32x2 h_ = glu2(acc[(k) >> 2][2 + (((k) >> 1) & 1)][2 * ((k) & 1)], acc[(k) >> 2][2 + (((k) >> 1) & 1)][2 * ((k) & 1) + 1], acc[(k) >> 2][4 + (((k) >> 1) & 1)][2 * ((k) & 1)], acc[(k) >> 2][4 + (((k) >> 1) & 1)][2 * ((k) & 1) + 1]); \
      unsigned w_ = cvtpk(h_[0], h_[1]); PIN(w_); ha[(k) >> 2][(k) & 3] = w_; } } while (0)
#define G1_SY(u, i) do { if (isY) store_u(u, i); } while (0)
#define G1_RD(d, st, s, n)                                                                                   \
  do {                                                                                                       \
    LAS const char* p_ = LP((st) ? (((n) & 1) ? wa11 : wa10) : (((n) & 1) ? wa01 : wa00)) + ((n) >> 1) * 8192 + (s) * 4096; \
    wl[d] = ldstr(p_); wh[d] = ldstr(p_ + 512);                                                              \
  } while (0)
#define G1_MD(d, s, n, m)                                                                                    \
  do {                                                                                                       \
    if (SWAP0 && (n) < 2) mfma16a(acc[m][n], xf[s][m], cat8(wl[d], wh[d]));                                   \
    else mfma16a(acc[m][n], cat8(wl[d], wh[d]), xf[s][m]);                                                    \
  } while (0)
#define G1_GD(d, s, n, A, B, C) do { G1_MD(d, s, n, 0); SBAR(); A; SBAR(); G1_MD(d, s, n, 1); SBAR(); B; SBAR(); G1_MD(d, s, n, 2); SBAR(); C; SBAR(); G1_MD(d, s, n, 3); SBAR(); } while (0)
    G1_GD(0, 0, 0, G1_RD(9, 0, 1, 0), G1_RX(0, 1, 0), NOP_);
    G1_GD(1, 0, 1, G1_RD(0, 0, 1, 1), G1_RX(0, 1, 1), NOP_);
    G1_GD(2, 0, 2, G1_RD(1, 0, 1, 2), G1_RX(0, 1, 2), NOP_);
    G1_GD(3, 0, 3, G1_RD(2, 0, 1, 3), G1_RX(0, 1, 3), NOP_);
    G1_GD(4, 0, 4, G1_RD(3, 0, 1, 4), NOP_, NOP_);
    G1_GD(5, 0, 5, G1_RD(4, 0, 1, 5), NOP_, NOP_);
    G1_GD(9, 1, 0, NOP_, NOP_, NOP_);
    asm volatile("s_waitcnt vmcnt(0)\n\ts_barrier" ::: "memory");
    G1_GD(0, 1, 1, G1_RX(1, 0, 0), G1_RD(5, 1, 0, 0), G1_RX(1, 0, 1));
    G1_GD(1, 1, 2, G1_RX(1, 0, 2), G1_RD(6, 1, 0, 1), G1_RX(1, 0, 3));
    G1_GD(2, 1, 3, G1_RD(7, 1, 1, 0), G1_RD(8, 1, 1, 1), NOP_);
    G1_GD(3, 1, 4, G1_RD(9, 1, 0, 2), G1_RD(0, 1, 0, 3), NOP_);
    G1_GD(4, 1, 5, G1_RD(1, 1, 1, 2), G1_RD(2, 1, 1, 3), NOP_);
    G1_GD(5, 0, 0, { G1_RX(1, 1, 0); G1_RX(1, 1, 1); }, { G1_RX(1, 1, 2); G1_RX(1, 1, 3); }, G1_RD(3, 1, 0, 4));
    G1_GD(6, 0, 1, G1_RD(4, 1, 0, 5), NOP_, NOP_);
    G1_GD(9, 0, 2, G1_RD(5, 1, 1, 4), NOP_, NOP_);
    G1_GD(7, 1, 0, G1_RD(6, 1, 1, 5), NOP_, NOP_);
    G1_GD(8, 1, 1, G1_RD(9, 1, 0, 6), NOP_, NOP_);
    G1_GD(0, 0, 3, G1_RD(7, 1, 0, 7), NOP_, NOP_);
    G1_GD(1, 1, 2, G1_RD(8, 1, 0, 8), NOP_, store_addr());
    G1_GD(2, 1, 3, G1_RD(0, 1, 0, 9), store_u(0, 0), store_u(0, 1));
    G1_GD(3, 0, 4, G1_RD(1, 1, 1, 6), store_u(0, 2), store_u(0, 3));
    G1_GD(4, 0, 5, G1_RD(2, 1, 1, 7), NOP_, NOP_);
    G1_GD(5, 1, 4, G1_RD(3, 1, 1, 8), G1_SY(1, 0), G1_SY(1, 1));
    G1_GD(6, 1, 5, G1_RD(4, 1, 1, 9), G1_SY(1, 2), G1_SY(1, 3));
    G1_GD(9, 0, 6, G1_RD(5, 0, 0, 6), NOP_, NOP_);
    G1_GD(7, 0, 7, G1_RD(6, 0, 0, 7), NOP_, G1_HA(0));
    G1_GD(8, 0, 8, G1_RD(9, 0, 0, 8), { G1_SY(2, 0); G1_HA(1); }, G1_SY(2, 1));
    G1_GD(0, 0, 9, G1_RD(7, 0, 0, 9), { G1_SY(2, 2); G1_HA(2); }, G1_SY(2, 3));
    G1_GD(1, 1, 6, { G1_RX(0, 0, 0); G1_RX(0, 0, 1); }, G1_RD(8, 0, 1, 6), G1_HA(3));
    G1_GD(2, 1, 7, { G1_RX(0, 0, 2); G1_RX(0, 0, 3); }, G1_RD(0, 0, 1, 7), G1_HA(4));
    G1_GD(3, 1, 8, G1_RD(1, 0, 1, 8), G1_HA(5), G1_HA(6));
    G1_GD(4, 1, 9, G1_RD(2, 0, 1, 9), G1_HA(7), NOP_);
    G1_GD(5, 0, 6, { G1_RX(0, 1, 0); G1_RX(0, 1, 1); }, { G1_RX(0, 1, 2); G1_RX(0, 1, 3); }, G1_HA(8));
    G1_GD(6, 0, 7, G1_HA(9), NOP_, NOP_);
    G1_GD(9, 0, 8, G1_HA(10), NOP_, NOP_);
    G1_GD(7, 0, 9, G1_HA(11), NOP_, NOP_);
    G1_WAITBAR();
    G1_GD(8, 1, 6, if (wid == 7) publish(ws, FLG_F2 + mt * 32 + j, tag, lane_id()), G1_HA(12), NOP_);
#define G1_PF(k) do { if (wc) { const unsigned vw_ = g2_vw(lane_id(), cg); _Pragma("unroll") for (int i = 4 * (k); i < 4 * (k) + 8; ++i) dma16<false>(mksrd(wd, F * D * 2), vw_, (unsigned)(28 * wr) * 65536u + i * 16384u, ldsb + P2_G2RING + wr * 16384u + i * 1024u); } } while (0)
    G1_GD(0, 1, 7, G1_HA(13), G1_PF(0), NOP_);
    G1_GD(1, 1, 8, G1_HA(14), G1_PF(2), NOP_);
    G1_GD(2, 1, 9, G1_HA(15), NOP_, NOP_);
    if (wc == 0) { __builtin_amdgcn_s_setprio(1); dma16<true>(mksrd(ws, WS_XN), att_dep(head, mt, lane_id()) * (8u * FST), 0u, ldsb + LDS_DUMP + wid * 1024u); }
  }
  asm volatile("s_nop 7" ::: "memory"); SBAR();
#pragma unroll
  for (int m = 0; m < 4; ++m)
#pragma unroll
    for (int n = 0; n < 4; ++n) accb[m][n] = acc[m][6 + n];
}

DI void att_slow(const char* kv, const bf16x8 (&qf)[2][2], f32x16 (&o)[2], float (&linv)[2], int lane) {
  __builtin_amdgcn_fence(__ATOMIC_ACQUIRE, "agent");
#pragma unroll
  for (int m = 0; m < 2; ++m) {
    f32x16 oo = f32x16{}; float mref = -INFINITY, l = 0.f;
    for (int kt = 0; kt < 64; ++kt) {
      const char* p = kv + kt * 4096 + lane * 16;
      const bf16x8 k0 = *(const bf16x8*)p, k1 = *(const bf16x8*)(p + 1024), v0 = *(const bf16x8*)(p + 2048), v1 = *(const bf16x8*)(p + 3072);
      f32x16 s = MFMA32(k0, qf[m][0], f32x16{});
      s = MFMA32(k1, qf[m][1], s);
      float mx = s[0];
#pragma unroll
      for (int r = 1; r < 16; ++r) mx = fmaxf(mx, s[r]);
      mx = fmaxf(fmaxf(mx, __shfl_xor(mx, 32)), mref);
      const float f = EX2(mref - mx);
      mref = mx;
      float sum = 0.f;
#pragma unroll
      for (int r = 0; r < 16; ++r) { s[r] = EX2(s[r] - mref); sum += s[r]; oo[r] *= f; }
      l = l * f + sum;
      oo = MFMA32(v0, BF(pack8(s, 0, 1.f)), oo);
      oo = MFMA32(v1, BF(pack8(s, 1, 1.f)), oo);
    }
    o[m] = oo; linv[m] = 1.f / (l + __shfl_xor(l, 32));
  }
}
DI void lds_meet(unsigned v, unsigned need, unsigned addr) {
  unsigned left = SPIN_CAP;
  asm volatile("1:\n\tv_cmp_le_u32 vcc, %2, %0\n\ts_cmp_eq_u64 vcc, exec\n\ts_cbranch_scc1 2f\n\ts_sleep 1\n\tds_read_b32 %0, %3\n\ts_sub_u32 %1, %1, 1\n\ts_waitcnt lgkmcnt(0)\n\ts_cbranch_scc0 1b\n2:"
               : "+v"(v), "+s"(left) : "s"(need), "v"(addr) : "vcc", "scc", "memory");
}
DI void lds_meet_fast(unsigned v, unsigned need, unsigned addr) {
  unsigned left;
  asm volatile("v_cmp_le_u32 vcc, %2, %0\n\ts_cmp_eq_u64 vcc, exec\n\ts_cbranch_scc0 3f\n2:\n\t.subsection 1\n3:\n\ts_mov_b32 %1, 0x100000\n4:\n\ts_sleep 1\n\tds_read_b32 %0, %3\n\ts_waitcnt lgkmcnt(0)\n\t"
               "v_cmp_le_u32 vcc, %2, %0\n\ts_cmp_eq_u64 vcc, exec\n\ts_cbranch_scc1 2b\n\ts_sub_u32 %1, %1, 1\n\ts_cbranch_scc0 4b\n\ts_branch 2b\n\t.subsection 0"
               : "+v"(v), "=&s"(left) : "s"(need), "v"(addr) : "vcc", "scc", "memory");
}
DI void glu_pack(const f32x4 (&a)[4][4], u32x4 (&hp)[4]) {
#pragma unroll
  for (int m = 0; m < 4; ++m) {
#pragma unroll
    for (int k = 0; k < 4; ++k) { const f32x2 h = glu2(a[m][k >> 1][2 * (k & 1)], a[m][k >> 1][2 * (k & 1) + 1], a[m][2 + (k >> 1)][2 * (k & 1)], a[m][2 + (k >> 1)][2 * (k & 1) + 1]); hp[m][k] = cvtpk(h[0], h[1]); }
    PIN(hp[m]);
  }
}

DI void dma16x4_sc1(srd_t srd, unsigned voff, unsigned soff, unsigned m0v) {
  asm volatile("s_mov_b32 m0, %0\n\ts_nop 0\n\tbuffer_load_dwordx4 %1, %2, %3 offen sc1 lds\n\tbuffer_load_dwordx4 %1, %2, %3 offen offset:1024 sc1 lds\n\t"
               "buffer_load_dwordx4 %1, %2, %3 offen offset:2048 sc1 lds\n\tbuffer_load_dwordx4 %1, %2, %3 offen offset:3072 sc1 lds" ::"s"(m0v), "v"(voff), "s"(srd), "s"(soff) : "memory");
}
DI void att_role(const u16* wo, char* ws, LAS char* lds, int head, int qb, int tg, int cg, int wid, u64 tag, const u32x4 (&hp)[4], const u32x4 (&ha)[4]) {
  const int tz = lane_id();
  const int lane = tz & 63;
  const char* kvg = ws + WS_KV + (size_t)head * 262144;
  const srd_t skv = mksrd(kvg, 262144), sw = mksrd(ws, WS_END);
  const unsigned ldsb = (unsigned)(uintptr_t)lds;
  const unsigned voff = lane * 16u;
  volatile LAS unsigned* ctl = (volatile LAS unsigned*)(lds + LDS_CTL);
  const unsigned ctla = ldsb + LDS_CTL + (lane & 7) * 4u;
  {
#pragma unroll
    for (int i = 0; i < 4; ++i) { *(LAS u32x4*)(lds + 131072 + wid * 4096 + i * 1024 + lane * 16) = hp[i]; *(LAS u32x4*)(lds + 49152 + wid * 4096 + i * 1024 + lane * 16) = ha[i]; }
    asm volatile("s_waitcnt lgkmcnt(0)" ::: "memory");
    ctl[12 + wid] = 1u;
  }
#define AT_ARRIVE(k) ctl[wid] = (unsigned)((k) + 2)
#define AT_MEET(v, k) lds_meet(v, (unsigned)((k) + 2), ctla)
  f32x16 o[2] = {f32x16{}, f32x16{}};
  f32x4 ls[2] = {f32x4{}, f32x4{}};
  bf16x8 sel;
  { const short one = ((lane & 1) == ((lane >> 4) & 1) && (lane & 2) == 0) ? (short)0x3f80 : (short)0;
#pragma unroll
    for (int e = 0; e < 8; ++e) sel[e] = one; }
  bf16x8 kf[2][2], vf[2][2], qf[2][2];
  f32x16 sa, sb, negm[2];
  u32x4 pa0, pa1, pb0 = u32x4{}, pb1 = u32x4{};
  vf[1][0] = bf16x8{}; vf[1][1] = bf16x8{};
  PIN(sel); PIN(o[0]); PIN(o[1]); PIN(ls[0]); PIN(ls[1]); PIN(pb0); PIN(pb1); PIN(vf[1][0]); PIN(vf[1][1]);
  const unsigned kp = ldsb + lane * 16u;
  {
    asm volatile("s_waitcnt vmcnt(0)" ::: "memory");
    const u32x2 f = *(volatile LAS const u32x2*)(lds + LDS_DUMP + wid * 1024 + lane * 16);
    if (!__all(f[0] == (unsigned)tag && f[1] == (unsigned)(tag >> 32))) await(ws, att_dep(head, qb, lane), tag);
  }
  dma16x4_sc1(skv, voff, wid * 4096u, ldsb + wid * 4096u);
  dma16x4_sc1(sw, voff, WS_Q + (head * 64 + qb * 8 + wid * 2) * 2048u, ldsb + 32768u + wid * 4096u);
  dma16x4_sc1(skv, voff, 16384u + wid * 4096u, ldsb + 16384u + wid * 4096u);
  asm volatile("s_waitcnt vmcnt(0)" ::: "memory");
  AT_ARRIVE(-1);
#pragma unroll
  for (int i = 0; i < 4; ++i) qf[i >> 1][i & 1] = lds128(LP(kp) + 32768 + wid * 4096 + i * 1024);
  asm volatile("" : "+v"(qf[0][0]), "+v"(qf[0][1]), "+v"(qf[1][0]), "+v"(qf[1][1]));
  lds_meet(ctl[lane & 3], 1u, ldsb + LDS_CTL + (lane & 3) * 4u);
  kf[0][0] = lds128(LP(kp)); kf[0][1] = lds128(LP(kp) + 1024);
  {
    sa = MFMA32(kf[0][0], qf[0][0], f32x16{}); sa = MFMA32(kf[0][1], qf[0][1], sa);
    sb = MFMA32(kf[0][0], qf[1][0], f32x16{}); sb = MFMA32(kf[0][1], qf[1][1], sb);
    dma16x4_sc1(skv, voff, 32768u + wid * 4096u, ldsb + 32768u + wid * 4096u);
    float ma = sa[0], mb = sb[0];
#pragma unroll
    for (int r = 1; r < 16; ++r) { ma = fmaxf(ma, sa[r]); mb = fmaxf(mb, sb[r]); }
    { const auto ra = __builtin_amdgcn_permlane32_swap(__float_as_uint(ma), __float_as_uint(ma), false, false), rb = __builtin_amdgcn_permlane32_swap(__float_as_uint(mb), __float_as_uint(mb), false, false);
      ma = fmaxf(__uint_as_float(ra[0]), __uint_as_float(ra[1])); mb = fmaxf(__uint_as_float(rb[0]), __uint_as_float(rb[1])); }
#pragma unroll
    for (int r = 0; r < 16; ++r) { negm[0][r] = -ma; negm[1][r] = -mb; sa[r] -= ma; }
  }
  PIN(negm[0]); PIN(negm[1]);
#define E1(S_, i) S_[i] = EX2(S_[i])
#define CV(P_, i, S_, r) P_[i] = cvtpk(S_[r], S_[(r) + 1])
#define AT_UNIT(SC, PC0, PC1, SN, kz, mn, PP0, PP1, vz, X0, X1, X2, X3)                                       \
  do {                                                                                                       \
    SN = MFMA32(kf[kz][0], qf[mn][0], negm[mn]); SBAR(); X0;                                                  \
    E1(SC, 0); E1(SC, 1); E1(SC, 2); E1(SC, 3); PIN(SC); SBAR();                                               \
    SN = MFMA32(kf[kz][1], qf[mn][1], SN); SBAR(); X1;                                                        \
    CV(PC0, 0, SC, 0); CV(PC0, 1, SC, 2); E1(SC, 4); E1(SC, 5); E1(SC, 6); PIN(SC); PIN(PC0); SBAR();           \
    o[mn] = MFMA32(vf[vz][0], BF(PP0), o[mn]); SBAR(); X2;                                                    \
    E1(SC, 7); CV(PC0, 2, SC, 4); E1(SC, 8); CV(PC0, 3, SC, 6); E1(SC, 9); PIN(SC); PIN(PC0); SBAR();           \
    ls[mn] = MFMA16(sel, BF(PP0), ls[mn]); SBAR();                                                            \
    E1(SC, 10); E1(SC, 11); PIN(SC); SBAR();                                                                  \
    o[mn] = MFMA32(vf[vz][1], BF(PP1), o[mn]); SBAR(); X3;                                                    \
    CV(PC1, 0, SC, 8); E1(SC, 12); CV(PC1, 1, SC, 10); E1(SC, 13); E1(SC, 14); E1(SC, 15); PIN(SC); PIN(PC1); SBAR(); \
    ls[mn] = MFMA16(sel, BF(PP1), ls[mn]); SBAR();                                                            \
    CV(PC1, 2, SC, 12); CV(PC1, 3, SC, 14); PIN(PC1); SBAR();                                                  \
  } while (0)
#define AT_KT(z, KADDR, VADDR, D0, D1)                                                                        \
  do {                                                                                                       \
    AT_UNIT(sa, pa0, pa1, sb, z, 1, pb0, pb1, z ^ 1, kf[z ^ 1][0] = lds128(KADDR), kf[z ^ 1][1] = lds128((KADDR) + 1024), vf[z][0] = lds128(VADDR), vf[z][1] = lds128((VADDR) + 1024)); \
    asm volatile("" : "+v"(kf[z ^ 1][0]), "+v"(kf[z ^ 1][1]), "+v"(vf[z][0]), "+v"(vf[z][1]));   \
    AT_UNIT(sb, pb0, pb1, sa, z ^ 1, 0, pa0, pa1, z, D0, NOP_, D1, NOP_);                                      \
  } while (0)
  const unsigned vwo = g2_vw(lane, cg), m0b = ldsb + wid * 4096u;
  unsigned arr = 2u, ctlw = ldsb + LDS_CTL + wid * 4u;
  PIN(arr); PIN(ctlw);
#define AT_INT(p, SRD, VO, SO, ST)                                                                            \
  do {                                                                                                       \
    unsigned pv;                                                                                             \
    AT_KT(0, LP(kp) + (p) * 16384 + 4096, LP(kp) + (p) * 16384 + 2048, NOP_, NOP_);                           \
    AT_KT(1, LP(kp) + (p) * 16384 + 8192, LP(kp) + (p) * 16384 + 6144, pv = ctl[lane & 7], NOP_);             \
    lds_meet_fast(pv, 4 * it + (p) + 1, ctla);     \
    SBAR();                                                                                                  \
    AT_KT(0, LP(kp) + (p) * 16384 + 12288, LP(kp) + (p) * 16384 + 10240, dma16<true>(SRD, VO, (SO), m0b + (((p) + 3) & 3) * 16384u), dma16<true>(SRD, VO, (SO) + (ST), m0b + (((p) + 3) & 3) * 16384u + 1024u)); \
    AT_KT(1, LP(kp) + (((p) + 1) & 3) * 16384, LP(kp) + (p) * 16384 + 14336, dma16<true>(SRD, VO, (SO) + 2 * (ST), m0b + (((p) + 3) & 3) * 16384u + 2048u), dma16<true>(SRD, VO, (SO) + 3 * (ST), m0b + (((p) + 3) & 3) * 16384u + 3072u)); \
    asm volatile("s_waitcnt vmcnt(4)\n\tds_write_b32 %1, %0\n\tv_add_u32 %0, 1, %0" : "+v"(arr) : "v"(ctlw) : "memory");     \
  } while (0)
  for (int it = 0; it < 4; ++it) {
    const bool kvq = it < 3;
    const srd_t sq_ = mksrd(kvq ? (const void*)kvg : (const void*)wo, kvq ? 262144u : D * D * 2u);
    const unsigned sk_ = (4 * it + 3) * 16384u + wid * 4096u;
    const unsigned vq_ = kvq ? voff : vwo, so_ = kvq ? sk_ + 16384u : (8 * wid + 5) * 65536u, st_ = kvq ? 1024u : 16384u, sn_ = kvq ? 16384u : 65536u;
    AT_INT(0, skv, voff, sk_, 1024u);
    AT_INT(1, sq_, vq_, so_, st_);
    AT_INT(2, sq_, vq_, so_ + sn_, st_);
    AT_INT(3, sq_, vq_, so_ + 2 * sn_, st_);
  }
  o[1] = MFMA32(vf[1][0], BF(pb0), o[1]); ls[1] = MFMA16(sel, BF(pb0), ls[1]);
  o[1] = MFMA32(vf[1][1], BF(pb1), o[1]); ls[1] = MFMA16(sel, BF(pb1), ls[1]);
  auto store_o = [&](int m, float inv) {
    const unsigned T = qb * 8 + wid * 2 + m;
    const unsigned so = WS_X2 + ((T >> 2) * NKB + 112 + head) * 8192u + (T & 3) * 512u, vo = (lane >> 5) * 2048u + (lane & 31) * 16u;
    st16_sc1(sw, vo, so, pack8(o[m], 0, inv));
    st16_sc1(sw, vo, so + 4096u, pack8(o[m], 1, inv));
  };
  float lt[2];
#pragma unroll
  for (int m = 0; m < 2; ++m) { lt[m] = (lane & 16) ? ls[m][1] : ls[m][0]; store_o(m, __builtin_amdgcn_rcpf(lt[m])); }
  bool bad = false;
#pragma unroll
  for (int m = 0; m < 2; ++m) {
    float chk = lt[m], nanp = lt[m];
#pragma unroll
    for (int r = 0; r < 16; ++r) { chk = fmaxf(chk, fabsf(o[m][r])); nanp += o[m][r]; }
    bad = bad || !(chk < 1e30f) || (nanp != nanp);
  }
  if (__builtin_expect(__any(bad), 0)) {
    float linv[2];
    asm volatile("s_waitcnt vmcnt(0)" ::: "memory");
    att_slow(kvg, qf, o, linv, lane);
    store_o(0, linv[0]); store_o(1, linv[1]);
  }
  asm volatile("s_waitcnt vmcnt(0)" ::: "memory");
  publish_last(ws, ldsb + LDS_CTL + 17 * 4u, 4u, FLG_F4 + qb * 32 + head, tag, lane);
  await(ws, FLG_F4 + (tg >> 1) * 32 + 8 * wid + (lane & 7), tag);
  { unsigned pv = 0; AT_MEET(pv, 15); }
  ctl[8 + wid] = 1u;
}

constexpr int g2_cnt(int m, bool x0, bool w0, bool x1, bool w1, bool w2) {
  return (7 - m) + (w2 ? (8 - m) >> 1 : 0) + (x1 ? 8 : 0) + (w1 ? 4 : 0) + (x0 ? m : 0) + (w0 ? (m + 1) >> 1 : 0);
}
DI void g2_role(const u16* x, u32x4 (&xr)[2], const u16* wo, const u16* wd, char* ws, LAS char* lds, int tg, int cg, int kq, u64 tag, const u32x4 (&hp)[4], const u32x4 (&ha)[4], int mt, int j, srd_t scode) {
  const int tz = lane_id();
  const int lane = tz & 63;
  const srd_t sx2 = mksrd(ws + WS_X2 + (size_t)tg * NKB * 8192, NKB * 8192);
  const srd_t swd = mksrd(wd, F * D * 2), swo = mksrd(wo, D * D * 2);
  const unsigned ldsw = (unsigned)(uintptr_t)lds + P2_G2RING + kq * 16384u;
  const unsigned vw = g2_vw(lane, cg);
  const int t16 = lane & 15, g = lane >> 4, q4 = t16 >> 2, p4 = lane & 3;
  const unsigned vx = g * 2048u + t16 * 16u;
  volatile LAS unsigned* ctl = (volatile LAS unsigned*)(lds + LDS_CTL);
  unsigned wa[4];
  f32x4 acc[8][4];
  {
    u32x4 hq[8];
    const srd_t sw = mksrd(ws, WS_END);
    const unsigned cb = (unsigned)(uintptr_t)lds + LDS_CTL;
    lds_meet(0u, 1u, cb + (12 + kq) * 4u);
#pragma unroll
    for (int i = 0; i < 8; ++i) hq[i] = *(LAS const u32x4*)(lds + (i < 4 ? 131072 : 49152 - 4096) + kq * 4096 + i * 1024 + lane * 16);
#pragma unroll
    for (int i = 0; i < 8; ++i) PIN(hq[i]);
    ctl[4 + kq] = ~0u;
#pragma unroll
    for (int c = 0; c < 4; ++c) { wa[c] = ldsw + (4 * g + q4) * 128 + ((c ^ ((2 * g + (q4 >> 1)) & 3)) << 5) + p4 * 8; PIN(wa[c]); }
#pragma unroll
    for (int m = 0; m < 8; ++m)
#pragma unroll
      for (int c = 0; c < 4; ++c) { acc[m][c] = f32x4{}; PIN(acc[m][c]); }
    lds_meet(0u, 1u, cb + (lane & 3) * 4u);
    const int pair = j < 24 ? 2 * j + 1 : 24 + j;
#pragma unroll
    for (int m = 0; m < 4; ++m) { st16_sc1(sw, vx, h_base(mt, pair, kq, 1) + m * 256u, hp[m]); st16_sc1(sw, vx, h_base(mt, pair, kq, 0) + m * 256u, hq[m]); }
    if (j < 24) {
#pragma unroll
      for (int m = 0; m < 4; ++m) { st16_sc1(sw, vx, h_base(mt, 2 * j, kq, 1) + m * 256u, ha[m]); st16_sc1(sw, vx, h_base(mt, 2 * j, kq, 0) + m * 256u, hq[4 + m]); }
    }
    asm volatile("s_waitcnt vmcnt(0)" ::: "memory");
    publish_last(ws, cb + 16 * 4u, 4u, FLG_F3 + mt * 32 + j, tag, lane);
  }
  if (blockIdx.x == 0 && kq == 0) {
#pragma nounroll
    for (int i = 0; i < 4; ++i) await(ws, FLG_F1 + i * 64 + lane, tag);
    publish(ws, 0, tag, lane);
  }
  await(ws, FLG_F3 + (tg >> 1) * 32 + 7 * kq + (lane < (kq == 3 ? 11 : 7) ? lane : 0), tag);
  bf16x8 xf[2][8];
  unsigned xo = (unsigned)(28 * kq) * 8192u, wo_ = (unsigned)(28 * kq + 4) * 65536u;
#pragma unroll
  for (int z = 0; z < 2; ++z) {
#pragma unroll
    for (int m = 0; m < 8; ++m) ld16<true>(xf[z][m], sx2, vx, xo, m * 256);
    xo += 8192u; }
  s16x4 wl[2][4], wh[2][4];
#define G2_RW(z, off, c) do { LAS const char* p_ = LP(wa[c]) + (off); wl[z][c] = ldstr(p_); wh[z][c] = ldstr(p_ + 2048); } while (0)
#pragma unroll
  for (int c = 0; c < 4; ++c) G2_RW(0, 0, c);
  SBAR();
#define G2_KB(sl, WS_, RD, X0, W0, X1, W1, W2) G2_KBX(sl, WS_, RD, (((sl) + 1) & 3) * 4096, X0, W0, X1, W1, W2, 0, NOP_)
#define G2_KBX(sl, WS_, RD, ROFF, X0, W0, X1, W1, W2, EXTRA, HOOK)                                                                \
  do {                                                                                                       \
    constexpr int z = (sl) & 1;                                                                              \
    _Pragma("unroll") for (int m = 0; m < 8; ++m) {                                                          \
      switch (m) {                                                                                           \
        case 0: mfma16w<g2_cnt(0, X0, W0, X1, W1, W2) + (EXTRA)>(acc[m][0], cat8(wl[z][0], wh[z][0]), xf[z][m]); break; \
        case 1: mfma16w<g2_cnt(1, X0, W0, X1, W1, W2) + (EXTRA)>(acc[m][0], cat8(wl[z][0], wh[z][0]), xf[z][m]); break; \
        case 2: mfma16w<g2_cnt(2, X0, W0, X1, W1, W2) + (EXTRA)>(acc[m][0], cat8(wl[z][0], wh[z][0]), xf[z][m]); break; \
        case 3: mfma16w<g2_cnt(3, X0, W0, X1, W1, W2) + (EXTRA)>(acc[m][0], cat8(wl[z][0], wh[z][0]), xf[z][m]); break; \
        case 4: mfma16w<g2_cnt(4, X0, W0, X1, W1, W2) + (EXTRA)>(acc[m][0], cat8(wl[z][0], wh[z][0]), xf[z][m]); break; \
        case 5: mfma16w<g2_cnt(5, X0, W0, X1, W1, W2) + (EXTRA)>(acc[m][0], cat8(wl[z][0], wh[z][0]), xf[z][m]); break; \
        case 6: mfma16w<g2_cnt(6, X0, W0, X1, W1, W2) + (EXTRA)>(acc[m][0], cat8(wl[z][0], wh[z][0]), xf[z][m]); break; \
        default: mfma16w<g2_cnt(7, X0, W0, X1, W1, W2) + (EXTRA)>(acc[m][0], cat8(wl[z][0], wh[z][0]), xf[z][m]); break; \
      }                                                                                                      \
      SBAR();                                                                                                \
      if (RD && m >= 2 && m < 6) G2_RW(z ^ 1, ROFF, m - 2);                                \
      SBAR();                                                                                                \
      mfma16a(acc[m][1], cat8(wl[z][1], wh[z][1]), xf[z][m]);                                                 \
      mfma16a(acc[m][2], cat8(wl[z][2], wh[z][2]), xf[z][m]);                                                 \
      mfma16a(acc[m][3], cat8(wl[z][3], wh[z][3]), xf[z][m]); SBAR();                                         \
      HOOK;                                                                                                  \
      if (X0) ld16_inplace(xf[z][m], sx2, vx, xo, m * 256);                                                  \
      if (W0 && !(m & 1)) dma16<false>(WS_, vw, wo_ + (m >> 1) * 16384u, ldsw + (sl) * 4096u + (m >> 1) * 1024u); \
      SBAR();                                                                                                \
    }                                                                                                        \
    xo += 8192u; wo_ += 65536u;                                                                              \
  } while (0)
#define YES true
#define NO false
  G2_KB(0, swd, YES, YES, YES, YES, NO, NO); G2_KB(1, swd, YES, YES, YES, YES, YES, NO); G2_KB(2, swd, YES, YES, YES, YES, YES, YES); G2_KB(3, swd, YES, YES, YES, YES, YES, YES);
  for (int i = 4; i < 24; i += 4) {
    dma16<false>(scode, vx, (unsigned)((mt * 8192) + ((j >> 3) & 1) * 1024 + kq * 256), (unsigned)(uintptr_t)lds + LDS_DUMP + (4 + kq) * 1024u);
    G2_KB(0, swd, YES, YES, YES, YES, YES, YES); G2_KB(1, swd, YES, YES, YES, YES, YES, YES); G2_KB(2, swd, YES, YES, YES, YES, YES, YES); G2_KB(3, swd, YES, YES, YES, YES, YES, YES);
  }
  wo_ = (unsigned)(8 * kq) * 65536u;
  G2_KB(0, swo, YES, YES, YES, YES, YES, YES); G2_KB(1, swo, YES, YES, YES, YES, YES, YES);
  G2_KB(2, swo, YES, NO, YES, YES, YES, YES); G2_KB(3, swo, YES, NO, YES, NO, YES, YES);
  asm volatile("" : "+v"(wl[0][0]), "+v"(wh[0][0]), "+v"(wl[0][1]), "+v"(wh[0][1]), "+v"(wl[0][2]), "+v"(wh[0][2]), "+v"(wl[0][3]), "+v"(wh[0][3]));
#pragma unroll
  for (int p = 0; p < 4; ++p) dma16<false>(swo, vw, wo_ + p * 16384u, ldsw + p * 1024u);
  lds_meet(0u, 1u, (unsigned)(uintptr_t)lds + LDS_CTL + (8 + kq) * 4u);
  xo = (unsigned)(112 + 8 * kq) * 8192u;
#pragma unroll
  for (int z = 0; z < 2; ++z) {
#pragma unroll
    for (int m = 0; m < 8; ++m) ld16_inplace(xf[z][m], sx2, vx, xo, m * 256);
    xo += 8192u; }
  G2_KB(0, swo, YES, YES, NO, YES, NO, NO); G2_KB(1, swo, YES, YES, NO, YES, NO, NO); G2_KB(2, swo, YES, YES, NO, YES, NO, NO); G2_KB(3, swo, YES, YES, NO, YES, NO, NO);
#pragma unroll
  for (int c = 0; c < 4; ++c) wa[c] -= P2_G2RING + kq * 12288u;
  G2_KBX(0, swo, YES, 0, YES, NO, YES, NO, NO, 0, NOP_); G2_KBX(1, swo, YES, 16384, YES, NO, YES, NO, NO, 0, NOP_);
  G2_KBX(2, swo, YES, 32768, NO, NO, YES, NO, NO, 0, NOP_);
  const int ty = lane_id();
  { const srd_t sxi = mksrd(x, S * D * 2);
    const unsigned xv = (unsigned)((ty & 63) >> 3) * 2048u + (ty & 7) * 16u, xs = (unsigned)(tg * 128 + (4 + kq) * 16) * 2048u + cg * 128u;
    ld16<false>(xr[0], sxi, xv, xs, 0); ld16<false>(xr[1], sxi, xv, xs + 16384u, 0); }
  unsigned pa[4];
#pragma unroll
  for (int c = 0; c < 4; ++c) pa[c] = (unsigned)(uintptr_t)lds + kq * 4096u + (ty & 15) * 256u + (((4 * c + ((ty >> 4) & 3)) ^ (ty & 15)) << 4);
  const unsigned pown = P2_G2RING + kq * 12288u;
#define G2_PUT(mm) do { _Pragma("unroll") for (int c = 0; c < 4; ++c) *(LAS f32x4*)((LAS char*)(uintptr_t)((mm) < 4 ? pa[c] : pa[c] + pown) + ((mm) < 4 ? (mm) * 16384 : ((mm) - 4) * 4096)) = acc[mm][c]; } while (0)
  G2_KBX(3, swo, NO, 0, NO, NO, NO, NO, NO, 2, if (m >= 2) G2_PUT(m >= 2 ? m - 2 : 0));
  asm volatile("s_nop 7" ::: "memory");
  G2_PUT(6); G2_PUT(7);
  asm volatile("s_waitcnt vmcnt(0)" : "+v"(xr[0]), "+v"(xr[1]));
}

__global__ __launch_bounds__(512, 2) void fused_kernel(const u16* x, const u16* wqkv, const u16* wo, const u16* wg, const u16* wu, const u16* wd, char* ws, u16* out) {
  extern __shared__ __attribute__((aligned(16))) char smem[];
  LAS char* lds = (LAS char*)smem;
  const int b = blockIdx.x, xcd = b & 7, r = b >> 3, wid = rfl(threadIdx.x >> 6);
  u64 tag = *(const __attribute__((address_space(4))) u64*)(ws + WS_FLG);
  ((LAS unsigned*)(lds + LDS_CTL))[threadIdx.x & 31] = 0u;
  const int tg = (xcd >> 1) * 4 + (r & 3), cg = (xcd & 1) * 8 + (r >> 2);
  const int mt = r & 7, j = xcd + 8 * (r >> 3);
  srd_t scode;
  { u64 pc; unsigned len;
    asm volatile("s_getpc_b64 %0\n1:\n\ts_mov_b32 %1, .Lfk_code_end-1b" : "=s"(pc), "=s"(len));
    scode = mksrd((const void*)(pc & ~127ull), len); }
  ln_row(x, ws, mt * 256 + j * 8 + wid, scode, (unsigned)((r * 8 + wid) & 127) * 1024u, (unsigned)(uintptr_t)lds + LDS_DUMP + wid * 1024u);
  asm volatile("s_waitcnt vmcnt(0)" : "+s"(tag)::"memory");
  tag = tag * 6364136223846793005ull + 1442695040888963407ull;
  asm volatile("s_barrier" ::: "memory");
  if (wid == 0) publish(ws, FLG_F1 + mt * 32 + j, tag, lane_id());
  const int head = xcd * 4 + (r >> 3);
  f32x4 accb[4][4];
  u32x4 ha[4];
  if (j < 16) g1_body<0>(head, wo, wd, cg, wqkv, wg, wu, ws, lds, wid, mt, j, scode, tag, accb, ha);
  else if (j < 24) g1_body<1>(head, wo, wd, cg, wqkv, wg, wu, ws, lds, wid, mt, j, scode, tag, accb, ha);
  else g1_body<2>(head, wo, wd, cg, wqkv, wg, wu, ws, lds, wid, mt, j, scode, tag, accb, ha);
  u32x4 hp[4];
  glu_pack(accb, hp);
  u32x4 xr[2];
  if (wid >= 4) {
    g2_role(x, xr, wo, wd, ws, lds, tg, cg, wid - 4, tag, hp, ha, mt, j, scode);
  } else {
    att_role(wo, ws, lds, head, mt, tg, cg, wid, tag, hp, ha);
    const int ty = lane_id();
#pragma unroll
    for (int e = 0; e < 2; ++e) xr[e] = *(const u32x4*)(x + (size_t)(tg * 128 + wid * 16 + e * 8 + ((ty & 63) >> 3)) * D + cg * 64 + (ty & 7) * 8);
  }
  const int ln = lane_id();
  unsigned ra[4][4], vo = (unsigned)((tg * 128 + wid * 16 + (ln >> 3)) * D + cg * 64 + (ln & 7) * 8) * 2u;
  f32x4 sum[4];
#pragma unroll
  for (int i = 0; i < 4; ++i) {
    const unsigned tk = (i >> 1) * 8 + (ln >> 3);
#pragma unroll
    for (int k = 0; k < 4; ++k) { ra[i][k] = (unsigned)(uintptr_t)lds + (wid < 4 ? wid * 16384 + k * 4096 : P2_G2RING + (wid - 4) * 4096 + k * 16384) + tk * 256 + (((2 * (ln & 7) + (i & 1)) ^ tk) << 4); PIN(ra[i][k]); }
    sum[i] = f32x4{blo(xr[i >> 1][2 * (i & 1)]), bhi(xr[i >> 1][2 * (i & 1)]), blo(xr[i >> 1][2 * (i & 1) + 1]), bhi(xr[i >> 1][2 * (i & 1) + 1])};
    PIN(sum[i]);
  }
  PIN(vo);
  asm volatile("s_waitcnt lgkmcnt(0)\n\ts_barrier" ::: "memory");
  f32x4 part[4][4];
#pragma unroll
  for (int i = 0; i < 4; ++i)
#pragma unroll
    for (int k = 0; k < 4; ++k) part[i][k] = *(LAS const f32x4*)LP(ra[i][k]);
  SBAR();
#pragma unroll
  for (int i = 0; i < 4; ++i) sum[i] += (part[i][0] + part[i][1]) + (part[i][2] + part[i][3]);
  st16_sc1(mksrd(out, S * D * 2), vo, 0u, pack44(sum[0], sum[1], 1.f));
  st16_sc1(mksrd(out, S * D * 2), vo, 8u * D * 2u, pack44(sum[2], sum[3], 1.f));
  asm volatile(".Lfk_code_end:");
}

extern "C" void kernel_launch(void* const* d_in, const int* in_sizes, int n_in, void* d_out, int out_size, void* d_ws, size_t ws_size, hipStream_t stream) {
  (void)hipFuncSetAttribute((const void*)fused_kernel, hipFuncAttributeMaxDynamicSharedMemorySize, LDS_BYTES);
  fused_kernel<<<256, 512, LDS_BYTES, stream>>>((const u16*)d_in[0], (const u16*)d_in[1], (const u16*)d_in[2], (const u16*)d_in[4], (const u16*)d_in[5], (const u16*)d_in[6], (char*)d_ws, (u16*)d_out);
}
```

```cpp
#include <hip/hip_runtime.h>
#include <hip/hip_bf16.h>
#include <stdint.h>

typedef unsigned short u16;
typedef unsigned long long u64;
typedef short bf16x8 __attribute__((ext_vector_type(8)));
typedef short s16x4 __attribute__((ext_vector_type(4)));
typedef float f32x16 __attribute__((ext_vector_type(16)));
typedef float f32x4 __attribute__((ext_vector_type(4)));
typedef float f32x2 __attribute__((ext_vector_type(2)));
typedef unsigned u32x4 __attribute__((ext_vector_type(4)));
typedef unsigned u32x2 __attribute__((ext_vector_type(2)));
typedef __bf16 bf16x2_t __attribute__((ext_vector_type(2)));
typedef short v4i16_t __attribute__((ext_vector_type(4)));
typedef __amdgpu_buffer_rsrc_t srd_t;
typedef __attribute__((address_space(1))) u64 gu64;
#define LAS __attribute__((address_space(3)))
#define DI __device__ __forceinline__
#define SBAR() __builtin_amdgcn_sched_barrier(0)
#define PIN(x) asm volatile("" : "+v"(x))
#define NOP_ ((void)0)
#define LP(a) ((LAS const char*)(uintptr_t)(a))
#define MFMA32(a, b, c) __builtin_amdgcn_mfma_f32_32x32x16_bf16((a), (b), (c), 0, 0, 0)
#define MFMA16(a, b, c) __builtin_amdgcn_mfma_f32_16x16x32_bf16((a), (b), (c), 0, 0, 0)
#define EX2(v) __builtin_amdgcn_exp2f(v)
#define BF(x) __builtin_bit_cast(bf16x8, x)
#define RLX_AGENT __ATOMIC_RELAXED, __HIP_MEMORY_SCOPE_AGENT

constexpr int S = 2048, D = 1024, F = 3584;
constexpr int NKB = (F + D) / 32;
constexpr float LOG2E = 1.4426950408889634f;
constexpr float QSCALE = 0.17677669529663687f * LOG2E;

constexpr unsigned WS_FLG = 0;
constexpr unsigned FLG_F1 = 256, FLG_F2 = FLG_F1 + 2048, FLG_F3 = FLG_F2 + 2048, FLG_F4 = FLG_F3 + 2048;
constexpr unsigned FST = 16;
constexpr unsigned WS_XN = 2097152;
constexpr unsigned WS_Q = WS_XN + S * D * 2;
constexpr unsigned WS_KV = WS_Q + S * D * 2;
constexpr unsigned WS_X2 = WS_KV + S * D * 4;
constexpr unsigned WS_END = WS_X2 + S * (F + D) * 2;
constexpr int G1_XS = 32768, G1_WB = 65536, G1_WS = 40960;
constexpr int P2_G2RING = 65536;
constexpr int LDS_CTL = G1_WB + 2 * G1_WS, LDS_DUMP = LDS_CTL + 1024, LDS_BYTES = LDS_DUMP + 8192;
constexpr unsigned PFD = 4;
constexpr unsigned SPIN_CAP = 1u << 20;

DI unsigned rfl(unsigned x) { return __builtin_amdgcn_readfirstlane(x); }
DI int lane_id() { int l; asm volatile("v_mbcnt_lo_u32_b32 %0, -1, 0\n\tv_mbcnt_hi_u32_b32 %0, -1, %0" : "=v"(l)); return l; }
DI unsigned cvtpk(float lo, float hi) { f32x2 v = {lo, hi}; bf16x2_t b = __builtin_convertvector(v, bf16x2_t); return __builtin_bit_cast(unsigned, b); }
DI float rbf(float f) { return __uint_as_float(cvtpk(f, f) << 16); }
DI float blo(unsigned u) { return __uint_as_float(u << 16); }
DI float bhi(unsigned u) { return __uint_as_float(u & 0xffff0000u); }
DI srd_t mksrd(const void* p, unsigned bytes) { return __builtin_amdgcn_make_buffer_rsrc((void*)p, 0, bytes, 0x00020000); }
template <bool SC1> DI void dma16(srd_t srd, unsigned voff, unsigned soff, unsigned m0v) {
  if (SC1) asm volatile("s_mov_b32 m0, %0\n\ts_nop 0\n\tbuffer_load_dwordx4 %1, %2, %3 offen sc1 lds" ::"s"(m0v), "v"(voff), "s"(srd), "s"(soff) : "memory");
  else asm volatile("s_mov_b32 m0, %0\n\ts_nop 0\n\tbuffer_load_dwordx4 %1, %2, %3 offen lds" ::"s"(m0v), "v"(voff), "s"(srd), "s"(soff) : "memory");
}
template <bool SC1, int IMM> DI void dma16i(srd_t srd, unsigned voff, unsigned soff, unsigned m0b) {
  if (SC1) asm volatile("s_add_u32 m0, %0, %4\n\ts_nop 0\n\tbuffer_load_dwordx4 %1, %2, %3 offen sc1 lds" ::"s"(m0b), "v"(voff), "s"(srd), "s"(soff), "i"(IMM) : "scc", "memory");
  else asm volatile("s_add_u32 m0, %0, %4\n\ts_nop 0\n\tbuffer_load_dwordx4 %1, %2, %3 offen lds" ::"s"(m0b), "v"(voff), "s"(srd), "s"(soff), "i"(IMM) : "scc", "memory");
}
template <bool SC1, class T> DI void ld16(T& d, srd_t srd, unsigned voff, unsigned soff, int imm) {
  if (SC1) asm volatile("buffer_load_dwordx4 %0, %1, %2, %3 offen offset:%4 sc1" : "=v"(d) : "v"(voff), "s"(srd), "s"(soff), "i"(imm) : "memory");
  else asm volatile("buffer_load_dwordx4 %0, %1, %2, %3 offen offset:%4" : "=v"(d) : "v"(voff), "s"(srd), "s"(soff), "i"(imm) : "memory");
}
template <class T> DI void ld16_inplace(T& d, srd_t srd, unsigned voff, unsigned soff, int imm) {
  asm volatile("buffer_load_dwordx4 %0, %1, %2, %3 offen offset:%4 sc1" : "+v"(d) : "v"(voff), "s"(srd), "s"(soff), "i"(imm) : "memory");
}
DI void st16_sc1(srd_t srd, unsigned voff, unsigned soff, u32x4 v) {
  asm volatile("buffer_store_dwordx4 %0, %1, %2, %3 offen sc1\n\ts_nop 1" ::"v"(v), "v"(voff), "s"(srd), "s"(soff) : "memory");
}
DI s16x4 ldstr(LAS const char* p) { return __builtin_bit_cast(s16x4, __builtin_amdgcn_ds_read_tr16_b64_v4i16((LAS v4i16_t*)p)); }
DI bf16x8 lds128(LAS const char* p) { return *(LAS const bf16x8*)p; }
DI bf16x8 cat8(s16x4 lo, s16x4 hi) { return __builtin_shufflevector(lo, hi, 0, 1, 2, 3, 4, 5, 6, 7); }
DI u32x4 pack8(const f32x16& v, int s, float sc) {
  u32x4 w;
  w[0] = cvtpk(v[8 * s + 0] * sc, v[8 * s + 1] * sc); w[1] = cvtpk(v[8 * s + 2] * sc, v[8 * s + 3] * sc);
  w[2] = cvtpk(v[8 * s + 4] * sc, v[8 * s + 5] * sc); w[3] = cvtpk(v[8 * s + 6] * sc, v[8 * s + 7] * sc);
  return w;
}
DI u32x4 pack44(const f32x4& a, const f32x4& b, float sc) {
  u32x4 w; w[0] = cvtpk(a[0] * sc, a[1] * sc); w[1] = cvtpk(a[2] * sc, a[3] * sc); w[2] = cvtpk(b[0] * sc, b[1] * sc); w[3] = cvtpk(b[2] * sc, b[3] * sc); return w;
}
DI void mfma16a(f32x4& c, const bf16x8& a, const bf16x8& b) { asm volatile("v_mfma_f32_16x16x32_bf16 %0, %1, %2, %0" : "+v"(c) : "v"(a), "v"(b)); }
template <int N> DI void mfma16w(f32x4& c, const bf16x8& a, const bf16x8& b) { asm volatile("s_waitcnt vmcnt(%3)\n\tv_mfma_f32_16x16x32_bf16 %0, %1, %2, %0" : "+v"(c) : "v"(a), "v"(b), "i"(N)); }
template <int CTRL, int RM> DI float dppadd(float v) { return v + __builtin_bit_cast(float, __builtin_amdgcn_update_dpp(0, __builtin_bit_cast(int, v), CTRL, RM, 0xf, true)); }
DI float wave_sum(float v) {
  v = dppadd<0x111, 0xf>(v); v = dppadd<0x112, 0xf>(v); v = dppadd<0x114, 0xf>(v); v = dppadd<0x118, 0xf>(v);
  v = dppadd<0x142, 0xa>(v); v = dppadd<0x143, 0xc>(v);
  return __builtin_bit_cast(float, __builtin_amdgcn_readlane(__builtin_bit_cast(int, v), 63));
}
DI void publish(char* ws, unsigned idx, u64 tag, int lane) {
  const u32x2 tv = {(unsigned)tag, (unsigned)(tag >> 32)};
  asm volatile("buffer_store_dwordx2 %0, %1, %2, %3 offen sc1" ::"v"(tv), "v"(lane == 0 ? 0u : 0x7ff00000u), "s"(mksrd(ws + WS_FLG, WS_XN)), "s"(idx * (8u * FST)) : "memory");
}
DI void publish_last(char* ws, unsigned cnt_addr, unsigned n, unsigned idx, u64 tag, int lane) {
  unsigned old;
  asm volatile("ds_add_rtn_u32 %0, %1, %2\n\ts_waitcnt lgkmcnt(0)" : "=v"(old) : "v"(cnt_addr), "v"(lane == 0 ? 1u : 0u) : "memory");
  if (rfl(old) == n - 1) publish(ws, idx, tag, lane);
}
DI void await(char* ws, unsigned idx, u64 tag) {
  gu64* p = (gu64*)(ws + WS_FLG) + idx * FST;
#pragma nounroll
  for (unsigned spins = 0; spins < SPIN_CAP; ++spins) {
    if (__all(__hip_atomic_load(p, RLX_AGENT) == tag)) break;
    __builtin_amdgcn_s_sleep(1);
  }
  asm volatile("" ::: "memory");
}

DI unsigned g2_vw(int lane, int cg) { const unsigned l3 = lane >> 3, l7 = lane & 7; return l3 * 2048u + ((l7 ^ (((l3 >> 1) & 3) << 1)) << 4) + cg * 128u; }

DI void ln_row(const u16* x, char* ws, int row, srd_t scode, unsigned code_off, unsigned dump) {
  const int lane = lane_id();
  const srd_t sxi = mksrd(x, S * D * 2), sx = mksrd(ws + WS_XN, S * D * 2);
  u32x4 a, b;
  ld16<false>(a, sxi, lane * 16u, row * 2048u, 0); ld16<false>(b, sxi, lane * 16u, row * 2048u, 1024);
  dma16<false>(scode, lane * 16u, code_off, dump);
  asm volatile("s_waitcnt vmcnt(1)" : "+v"(a), "+v"(b));
  f32x2 v[8];
#pragma unroll
  for (int i = 0; i < 4; ++i) { v[i] = f32x2{blo(a[i]), bhi(a[i])}; v[4 + i] = f32x2{blo(b[i]), bhi(b[i])}; }
  const f32x2 s2 = ((v[0] + v[1]) + (v[2] + v[3])) + ((v[4] + v[5]) + (v[6] + v[7]));
  const float mean = wave_sum(s2[0] + s2[1]) * (1.f / D);
  f32x2 q[4];
#pragma unroll
  for (int i = 0; i < 4; ++i) { const f32x2 d0 = v[i] - mean, d1 = v[4 + i] - mean; q[i] = __builtin_elementwise_fma(d1, d1, d0 * d0); }
  const f32x2 q2 = (q[0] + q[1]) + (q[2] + q[3]);
  const float mb = rbf(mean);
  unsigned dn[8];
#pragma unroll
  for (int i = 0; i < 8; ++i) { const f32x2 d = v[i] - mb; dn[i] = cvtpk(d[0], d[1]); }
  const float vb = rbf(wave_sum(q2[0] + q2[1]) * (1.f / D));
  const float inv = __builtin_amdgcn_rcpf(rbf(__builtin_amdgcn_sqrtf(rbf(vb + 1e-5f))));
  u32x4 oa, ob;
#pragma unroll
  for (int i = 0; i < 4; ++i) { oa[i] = cvtpk(blo(dn[i]) * inv, bhi(dn[i]) * inv); ob[i] = cvtpk(blo(dn[4 + i]) * inv, bhi(dn[4 + i]) * inv); }
  st16_sc1(sx, lane * 16u, row * 2048u, oa); st16_sc1(sx, lane * 16u + 1024u, row * 2048u, ob);
}

DI f32x2 glu2(float g0, float g1, float u0, float u1) { return f32x2{u0 * __builtin_amdgcn_rcpf(1.f + EX2(g0 * -LOG2E)), u1 * __builtin_amdgcn_rcpf(1.f + EX2(g1 * -LOG2E))}; }
DI unsigned h_base(int mt, int pair, int wr, int wc) { return WS_X2 + ((mt * 2 + (wr >> 1)) * NKB + 2 * pair + wc) * 8192u + (wr & 1) * 1024u; }
DI unsigned att_dep(int head, int qb, int lane) {
  const unsigned u = head >> 1, jy = 24 + ((u + 16) * 43 >> 7) - 8, jq = u < 8 ? 16 + u : jy, jk = 24 + ((u + 8) * 43 >> 7), jv = u;
  unsigned ikv = (lane & 7) * 32 + ((lane & 8) ? jk : jv); PIN(ikv);
  return FLG_F2 + (lane < 16 ? ikv : qb * 32 + jq);
}
template <int KIND>
DI void g1_body(int head, const u16* wo, const u16* wd, int cg, const u16* wqkv, const u16* wg, const u16* wu, char* ws, LAS char* lds, int wid, int mt, int j, srd_t scode, u64 tag, f32x4 (&accb)[4][4], u32x4 (&ha)[4]) {
  const int lane = lane_id(), wr = wid & 3, wc = wid >> 2;
  constexpr bool SWAP0 = KIND == 0, isY = KIND == 2;
  const int a0 = KIND == 0 ? 32 + j : (KIND == 1 ? j - 16 : 3 * j - 64);
  const srd_t sx = mksrd(ws + WS_XN, S * D * 2), sq = mksrd(wqkv, D * 3 * D * 2), sg = mksrd(wg, D * F * 2), su = mksrd(wu, D * F * 2);
  const srd_t sa = isY ? sq : sg, sb = isY ? sq : su;
  const unsigned ld1 = isY ? 6144u : 7168u;
  const unsigned c0 = a0 * 128u, c1 = isY ? c0 + 128u : j * 256u, c2 = isY ? c0 + 256u : j * 256u, c3 = isY ? (24 + j) * 128u : j * 256u + 128u;
  const unsigned l3 = lane >> 3, l7 = lane & 7, l4 = lane >> 4;
  const unsigned vx0 = l3 * 2048u + ((l7 ^ l4) << 4), vx1 = vx0 ^ 64u;
  const unsigned wsw = (l7 ^ (((l4 & 1) | ((wid & 1) << 1)) << 1)) << 4;
  const unsigned vw0 = l3 * 6144u + wsw, vw2 = l3 * 7168u + wsw;
  const unsigned vw1 = isY ? vw0 : vw2;
  const unsigned ldsb = (unsigned)(uintptr_t)lds;
  unsigned sox = (unsigned)(mt * 256 + wid * 32) * 2048u;
  unsigned so0 = wid * 8 * 6144u + c0, so1 = wid * 8 * ld1, so2 = wid * 8 * 7168u + c3;
  const unsigned m0x = ldsb + wid * 4096u, m0w = ldsb + G1_WB + wid * 1024u;
#define G1_DX(i, st) dma16i<true, (st) * G1_XS + (i) * 1024>(sx, ((i) & 1) ? vx1 : vx0, sox + (i) * 16384u, m0x)
#define G1_DW(i, st)                                                                             \
  do {                                                                                           \
    if ((i) == 0) dma16i<false, (st) * G1_WS>(sq, vw0, so0, m0w);                                 \
    if ((i) == 1) dma16i<false, (st) * G1_WS + 8192>(sa, vw1, so1 + c1, m0w);                    \
    if ((i) == 2) dma16i<false, (st) * G1_WS + 16384>(sb, vw1, so1 + c2, m0w);                   \
    if ((i) == 3) dma16i<false, (st) * G1_WS + 24576>(sg, vw2, so2, m0w);                        \
    if ((i) == 4) dma16i<false, (st) * G1_WS + 32768>(su, vw2, so2, m0w);                        \
  } while (0)
#define G1_ADVX() do { sox += 128u; } while (0)
#define G1_ADVW() do { so0 += 64u * 6144u; so1 += 64u * ld1; so2 += 64u * 7168u; } while (0)
#define G1_CODE() dma16<false>(scode, vx0, (unsigned)(mt * 16384 + ((j >> 3) & 1) * 1024 + wid * 128), ldsb + LDS_DUMP + wid * 1024u)
  const srd_t sp = wid == 0 ? sq : (wid == 1 ? sa : (wid == 2 ? sb : (wid == 3 ? sg : su)));
  const unsigned ldp = wid == 0 ? 6144u : (wid < 3 ? ld1 : 7168u), cp = wid == 0 ? c0 : (wid == 1 ? c1 : (wid == 2 ? c2 : (wid < 5 ? c3 : 0x7ff00000u)));
  const unsigned vwp = ldp == 6144u ? vw0 : vw2, idp = (mt * 32 + j) * 8 + wid;
  unsigned sop = cp + (PFD * 64u + mt * 8u) * ldp, rowd = (idp >> 4) * 8u;
#define G1_WARMW() do { dma16<false>(sp, vwp, sop, ldsb + LDS_DUMP + wid * 1024u); sop += 64u * ldp; } while (0)
#define G1_WARMD() do { const bool d_ = rowd < (unsigned)F; dma16<false>(mksrd(d_ ? wd : wo, d_ ? F * D * 2 : D * D * 2), vx0, (d_ ? rowd : rowd - F) * 2048u + (idp & 15) * 128u, ldsb + LDS_DUMP + wid * 1024u); rowd += 1024u; } while (0)
  f32x4 acc[4][10];
  G1_DW(0, 0); G1_DW(1, 0); G1_DW(2, 0); G1_DW(3, 0); G1_DW(4, 0); G1_ADVW();
  asm volatile("s_mov_b32 m0, %0\n\ts_nop 0\n\tbuffer_load_dword %1, %2, %3 offen lds" ::"s"(ldsb + LDS_DUMP + wid * 1024u), "v"(l3 + 1u < PFD ? ((l3 + 1u) * 64u + mt * 8u + l7) * ldp : 0x7ff00000u), "s"(sp), "s"(cp) : "memory");
#pragma unroll
  for (int m = 0; m < 4; ++m)
#pragma unroll
    for (int n = 0; n < 10; ++n) { acc[m][n] = f32x4{}; PIN(acc[m][n]); }
  await(ws, FLG_F1 + mt * 32 + wid * 4 + (lane & 3), tag);
  G1_DX(0, 0); G1_DX(1, 0); G1_DX(2, 0); G1_DX(3, 0); G1_ADVX();
  G1_DX(0, 1); G1_DX(1, 1); G1_DX(2, 1); G1_DX(3, 1); G1_ADVX();
  G1_DW(0, 1); G1_DW(1, 1); G1_DW(2, 1); G1_DW(3, 1); G1_DW(4, 1); G1_ADVW();
  const int t16 = lane & 15, g = lane >> 4, q4 = t16 >> 2, p4 = lane & 3;
  unsigned xa0 = ldsb + (wr * 64 + t16) * 128 + ((g ^ ((t16 >> 1) & 7)) << 4), xa1 = xa0 ^ 64u;
  unsigned wa00 = ldsb + G1_WB + (8 * g + q4) * 128 + (((2 * wc) ^ (((q4 >> 1) & 1) | ((g & 1) << 1))) << 5) + p4 * 8;
  unsigned wa01 = wa00 ^ 32u, wa10 = wa00 + G1_WS, wa11 = wa01 + G1_WS;
  asm volatile("" : "+v"(xa0), "+v"(xa1), "+v"(wa00), "+v"(wa01), "+v"(wa10), "+v"(wa11));
  bf16x8 xf[2][4];
  s16x4 wl[10], wh[10];
#define G1_RX(st, s, m) xf[s][m] = lds128(LP((s) ? xa1 : xa0) + (st) * G1_XS + (m) * 2048)
#define G1_RW(st, s, n)                                                                                      \
  do {                                                                                                       \
    LAS const char* p_ = LP((st) ? (((n) & 1) ? wa11 : wa10) : (((n) & 1) ? wa01 : wa00)) + ((n) >> 1) * 8192 + (s) * 4096; \
    wl[n] = ldstr(p_); wh[n] = ldstr(p_ + 512);                                                              \
  } while (0)
#define G1_MM(s, n, m)                                                                                       \
  do {                                                                                                       \
    if (SWAP0 && (n) < 2) mfma16a(acc[m][n], xf[s][m], cat8(wl[n], wh[n]));                                   \
    else mfma16a(acc[m][n], cat8(wl[n], wh[n]), xf[s][m]);                                                    \
  } while (0)
#define G1_GRP(s, n, A, B, C) do { G1_MM(s, n, 0); SBAR(); A; SBAR(); G1_MM(s, n, 1); SBAR(); B; SBAR(); G1_MM(s, n, 2); SBAR(); C; SBAR(); G1_MM(s, n, 3); SBAR(); } while (0)
#define G1_WAITBAR() G1_WAITN(0)
#define G1_WAITN(N) asm volatile("s_waitcnt vmcnt(%0) lgkmcnt(0)\n\ts_barrier" ::"i"(N) : "memory")
#define G1_HEAD(st)                                                                                          \
  do {                                                                                                       \
    G1_GRP(0, 0, G1_RW(st, 0, 9), NOP_, G1_WARMW());                                                          \
    G1_GRP(0, 1, G1_RW(st, 1, 0), G1_RX(st, 1, 0), if (st) G1_WARMD(); else G1_CODE());                                               \
    G1_GRP(0, 2, G1_RW(st, 1, 1), G1_RX(st, 1, 1), NOP_);                                                    \
    G1_GRP(0, 3, G1_RW(st, 1, 2), G1_RX(st, 1, 2), NOP_);                                                    \
    G1_GRP(0, 4, G1_RW(st, 1, 3), G1_RX(st, 1, 3), NOP_);                                                    \
    G1_GRP(0, 5, G1_RW(st, 1, 4), NOP_, NOP_);                                                               \
    G1_GRP(0, 6, G1_RW(st, 1, 5), NOP_, NOP_);                                                           \
    G1_GRP(0, 7, G1_RW(st, 1, 6), NOP_, NOP_);                                                               \
    G1_GRP(0, 8, G1_RW(st, 1, 7), NOP_, NOP_);                                                           \
    G1_GRP(0, 9, G1_RW(st, 1, 8), NOP_, NOP_);                                                               \
    G1_GRP(1, 0, G1_RW(st, 1, 9), NOP_, NOP_);                                                               \
    G1_GRP(1, 1, NOP_, NOP_, NOP_);                                                                          \
    G1_GRP(1, 2, NOP_, NOP_, NOP_);                                                                          \
    G1_WAITN(2);                                 \
  } while (0)
#define G1_TILE(st, DM)                                                                                      \
  do {                                                                                                       \
    G1_HEAD(st);                                                                                             \
    G1_GRP(1, 3, G1_RX(st ^ 1, 0, 0), G1_RW(st ^ 1, 0, 0), if (DM) G1_DX(0, st));                             \
    G1_GRP(1, 4, G1_RX(st ^ 1, 0, 1), G1_RW(st ^ 1, 0, 1), if (DM) G1_DW(0, st));                             \
    G1_GRP(1, 5, G1_RX(st ^ 1, 0, 2), G1_RW(st ^ 1, 0, 2), if (DM) G1_DX(1, st));                             \
    G1_GRP(1, 6, G1_RX(st ^ 1, 0, 3), G1_RW(st ^ 1, 0, 3), if (DM) G1_DW(1, st));                             \
    G1_GRP(1, 7, G1_RW(st ^ 1, 0, 4), G1_RW(st ^ 1, 0, 5), if (DM) { G1_DX(2, st); G1_DW(2, st); });          \
    G1_GRP(1, 8, G1_RW(st ^ 1, 0, 6), G1_RW(st ^ 1, 0, 7), if (DM) { G1_DX(3, st); G1_DW(3, st); });          \
    G1_GRP(1, 9, G1_RW(st ^ 1, 0, 8), NOP_, if (DM) { G1_DW(4, st); G1_ADVX(); G1_ADVW(); });                 \
  } while (0)
  asm volatile("s_waitcnt vmcnt(9)\n\ts_barrier" ::: "memory");
  G1_RX(0, 0, 0); G1_RW(0, 0, 0); G1_RX(0, 0, 1); G1_RX(0, 0, 2); G1_RX(0, 0, 3);
  G1_RW(0, 0, 1); G1_RW(0, 0, 2); G1_RW(0, 0, 3); G1_RW(0, 0, 4); G1_RW(0, 0, 5); G1_RW(0, 0, 6); G1_RW(0, 0, 7); G1_RW(0, 0, 8);
  SBAR();
  for (int t = 0; t < 14; t += 2) { G1_TILE(0, true); G1_TILE(1, true); }
  {
    const srd_t sw = mksrd(ws, WS_END);
    unsigned vq, vk;
    auto store_addr = [&]() { const unsigned l = lane_id(); vq = (l >> 4) * 512u + (l & 15) * 16u; vk = (l >> 4) * 512u + (2 * (l & 8) + (l & 7)) * 16u; };
    const unsigned T32 = mt * 8 + wr * 2;
    auto store_u = [&](int u, int i) {
      const int a = a0 + u; const unsigned head = 2 * (a & 15) + wc; const bool isq = a < 16;
      if (SWAP0) st16_sc1(sw, vk, WS_KV + (head * 64 + T32 + (i >> 1)) * 4096u + 2048u + (i & 1) * 128u, pack44(acc[2 * (i >> 1)][i & 1], acc[2 * (i >> 1) + 1][i & 1], 1.f));
      else st16_sc1(sw, isq ? vq : vk, isq ? WS_Q + (head * 64 + T32 + (i >> 1)) * 2048u + (i & 1) * 256u : WS_KV + (head * 64 + T32 + (i >> 1)) * 4096u + (i & 1) * 128u,
                    pack44(acc[i][2 * u], acc[i][2 * u + 1], isq ? QSCALE : 1.f));
    };
#define G1_HA(k) do { if (!isY) { const f32x2 h_ = glu2(acc[(k) >> 2][2 + (((k) >> 1) & 1)][2 * ((k) & 1)], acc[(k) >> 2][2 + (((k) >> 1) & 1)][2 * ((k) & 1) + 1], acc[(k) >> 2][4 + (((k) >> 1) & 1)][2 * ((k) & 1)], acc[(k) >> 2][4 + (((k) >> 1) & 1)][2 * ((k) & 1) + 1]); \
      unsigned w_ = cvtpk(h_[0], h_[1]); PIN(w_); ha[(k) >> 2][(k) & 3] = w_; } } while (0)
#define G1_SY(u, i) do { if (isY) store_u(u, i); } while (0)
#define G1_RD(d, st, s, n)                                                                                   \
  do {                                                                                                       \
    LAS const char* p_ = LP((st) ? (((n) & 1) ? wa11 : wa10) : (((n) & 1) ? wa01 : wa00)) + ((n) >> 1) * 8192 + (s) * 4096; \
    wl[d] = ldstr(p_); wh[d] = ldstr(p_ + 512);                                                              \
  } while (0)
#define G1_MD(d, s, n, m)                                                                                    \
  do {                                                                                                       \
    if (SWAP0 && (n) < 2) mfma16a(acc[m][n], xf[s][m], cat8(wl[d], wh[d]));                                   \
    else mfma16a(acc[m][n], cat8(wl[d], wh[d]), xf[s][m]);                                                    \
  } while (0)
#define G1_GD(d, s, n, A, B, C) do { G1_MD(d, s, n, 0); SBAR(); A; SBAR(); G1_MD(d, s, n, 1); SBAR(); B; SBAR(); G1_MD(d, s, n, 2); SBAR(); C; SBAR(); G1_MD(d, s, n, 3); SBAR(); } while (0)
    G1_GD(0, 0, 0, G1_RD(9, 0, 1, 0), G1_RX(0, 1, 0), NOP_);
    G1_GD(1, 0, 1, G1_RD(0, 0, 1, 1), G1_RX(0, 1, 1), NOP_);
    G1_GD(2, 0, 2, G1_RD(1, 0, 1, 2), G1_RX(0, 1, 2), NOP_);
    G1_GD(3, 0, 3, G1_RD(2, 0, 1, 3), G1_RX(0, 1, 3), NOP_);
    G1_GD(4, 0, 4, G1_RD(3, 0, 1, 4), NOP_, NOP_);
    G1_GD(5, 0, 5, G1_RD(4, 0, 1, 5), NOP_, NOP_);
    G1_GD(9, 1, 0, NOP_, NOP_, NOP_);
    asm volatile("s_waitcnt vmcnt(0)\n\ts_barrier" ::: "memory");
    G1_GD(0, 1, 1, G1_RX(1, 0, 0), G1_RD(5, 1, 0, 0), G1_RX(1, 0, 1));
    G1_GD(1, 1, 2, G1_RX(1, 0, 2), G1_RD(6, 1, 0, 1), G1_RX(1, 0, 3));
    G1_GD(2, 1, 3, G1_RD(7, 1, 1, 0), G1_RD(8, 1, 1, 1), NOP_);
    G1_GD(3, 1, 4, G1_RD(9, 1, 0, 2), G1_RD(0, 1, 0, 3), NOP_);
    G1_GD(4, 1, 5, G1_RD(1, 1, 1, 2), G1_RD(2, 1, 1, 3), NOP_);
    G1_GD(5, 0, 0, { G1_RX(1, 1, 0); G1_RX(1, 1, 1); }, { G1_RX(1, 1, 2); G1_RX(1, 1, 3); }, G1_RD(3, 1, 0, 4));
    G1_GD(6, 0, 1, G1_RD(4, 1, 0, 5), NOP_, NOP_);
    G1_GD(9, 0, 2, G1_RD(5, 1, 1, 4), NOP_, NOP_);
    G1_GD(7, 1, 0, G1_RD(6, 1, 1, 5), NOP_, NOP_);
    G1_GD(8, 1, 1, G1_RD(9, 1, 0, 6), NOP_, NOP_);
    G1_GD(0, 0, 3, G1_RD(7, 1, 0, 7), NOP_, NOP_);
    G1_GD(1, 1, 2, G1_RD(8, 1, 0, 8), NOP_, store_addr());
    G1_GD(2, 1, 3, G1_RD(0, 1, 0, 9), store_u(0, 0), store_u(0, 1));
    G1_GD(3, 0, 4, G1_RD(1, 1, 1, 6), store_u(0, 2), store_u(0, 3));
    G1_GD(4, 0, 5, G1_RD(2, 1, 1, 7), NOP_, NOP_);
    G1_GD(5, 1, 4, G1_RD(3, 1, 1, 8), G1_SY(1, 0), G1_SY(1, 1));
    G1_GD(6, 1, 5, G1_RD(4, 1, 1, 9), G1_SY(1, 2), G1_SY(1, 3));
    G1_GD(9, 0, 6, G1_RD(5, 0, 0, 6), NOP_, NOP_);
    G1_GD(7, 0, 7, G1_RD(6, 0, 0, 7), NOP_, G1_HA(0));
    G1_GD(8, 0, 8, G1_RD(9, 0, 0, 8), { G1_SY(2, 0); G1_HA(1); }, G1_SY(2, 1));
    G1_GD(0, 0, 9, G1_RD(7, 0, 0, 9), { G1_SY(2, 2); G1_HA(2); }, G1_SY(2, 3));
    G1_GD(1, 1, 6, { G1_RX(0, 0, 0); G1_RX(0, 0, 1); }, G1_RD(8, 0, 1, 6), G1_HA(3));
    G1_GD(2, 1, 7, { G1_RX(0, 0, 2); G1_RX(0, 0, 3); }, G1_RD(0, 0, 1, 7), G1_HA(4));
    G1_GD(3, 1, 8, G1_RD(1, 0, 1, 8), G1_HA(5), G1_HA(6));
    G1_GD(4, 1, 9, G1_RD(2, 0, 1, 9), G1_HA(7), NOP_);
    G1_GD(5, 0, 6, { G1_RX(0, 1, 0); G1_RX(0, 1, 1); }, { G1_RX(0, 1, 2); G1_RX(0, 1, 3); }, G1_HA(8));
    G1_GD(6, 0, 7, G1_HA(9), NOP_, NOP_);
    G1_GD(9, 0, 8, G1_HA(10), NOP_, NOP_);
    G1_GD(7, 0, 9, G1_HA(11), NOP_, NOP_);
    G1_WAITBAR();
    G1_GD(8, 1, 6, if (wid == 7) publish(ws, FLG_F2 + mt * 32 + j, tag, lane_id()), G1_HA(12), NOP_);
#define G1_PF(k) do { if (wc) { const unsigned vw_ = g2_vw(lane_id(), cg); _Pragma("unroll") for (int i = 4 * (k); i < 4 * (k) + 8; ++i) dma16<false>(mksrd(wd, F * D * 2), vw_, (unsigned)(28 * wr) * 65536u + i * 16384u, ldsb + P2_G2RING + wr * 16384u + i * 1024u); } } while (0)
    G1_GD(0, 1, 7, G1_HA(13), G1_PF(0), NOP_);
    G1_GD(1, 1, 8, G1_HA(14), G1_PF(2), NOP_);
    G1_GD(2, 1, 9, G1_HA(15), NOP_, NOP_);
    if (wc == 0) { __builtin_amdgcn_s_setprio(1); dma16<true>(mksrd(ws, WS_XN), att_dep(head, mt, lane_id()) * (8u * FST), 0u, ldsb + LDS_DUMP + wid * 1024u); }
  }
  asm volatile("s_nop 7" ::: "memory"); SBAR();
#pragma unroll
  for (int m = 0; m < 4; ++m)
#pragma unroll
    for (int n = 0; n < 4; ++n) accb[m][n] = acc[m][6 + n];
}

DI void att_slow(const char* kv, const bf16x8 (&qf)[2][2], f32x16 (&o)[2], float (&linv)[2], int lane) {
  __builtin_amdgcn_fence(__ATOMIC_ACQUIRE, "agent");
#pragma unroll
  for (int m = 0; m < 2; ++m) {
    f32x16 oo = f32x16{}; float mref = -INFINITY, l = 0.f;
    for (int kt = 0; kt < 64; ++kt) {
      const char* p = kv + kt * 4096 + lane * 16;
      const bf16x8 k0 = *(const bf16x8*)p, k1 = *(const bf16x8*)(p + 1024), v0 = *(const bf16x8*)(p + 2048), v1 = *(const bf16x8*)(p + 3072);
      f32x16 s = MFMA32(k0, qf[m][0], f32x16{});
      s = MFMA32(k1, qf[m][1], s);
      float mx = s[0];
#pragma unroll
      for (int r = 1; r < 16; ++r) mx = fmaxf(mx, s[r]);
      mx = fmaxf(fmaxf(mx, __shfl_xor(mx, 32)), mref);
      const float f = EX2(mref - mx);
      mref = mx;
      float sum = 0.f;
#pragma unroll
      for (int r = 0; r < 16; ++r) { s[r] = EX2(s[r] - mref); sum += s[r]; oo[r] *= f; }
      l = l * f + sum;
      oo = MFMA32(v0, BF(pack8(s, 0, 1.f)), oo);
      oo = MFMA32(v1, BF(pack8(s, 1, 1.f)), oo);
    }
    o[m] = oo; linv[m] = 1.f / (l + __shfl_xor(l, 32));
  }
}
DI void lds_meet(unsigned v, unsigned need, unsigned addr) {
  unsigned left = SPIN_CAP;
  asm volatile("1:\n\tv_cmp_le_u32 vcc, %2, %0\n\ts_cmp_eq_u64 vcc, exec\n\ts_cbranch_scc1 2f\n\ts_sleep 1\n\tds_read_b32 %0, %3\n\ts_sub_u32 %1, %1, 1\n\ts_waitcnt lgkmcnt(0)\n\ts_cbranch_scc0 1b\n2:"
               : "+v"(v), "+s"(left) : "s"(need), "v"(addr) : "vcc", "scc", "memory");
}
DI void lds_meet_fast(unsigned v, unsigned need, unsigned addr) {
  unsigned left;
  asm volatile("v_cmp_le_u32 vcc, %2, %0\n\ts_cmp_eq_u64 vcc, exec\n\ts_cbranch_scc0 3f\n2:\n\t.subsection 1\n3:\n\ts_mov_b32 %1, 0x100000\n4:\n\ts_sleep 1\n\tds_read_b32 %0, %3\n\ts_waitcnt lgkmcnt(0)\n\t"
               "v_cmp_le_u32 vcc, %2, %0\n\ts_cmp_eq_u64 vcc, exec\n\ts_cbranch_scc1 2b\n\ts_sub_u32 %1, %1, 1\n\ts_cbranch_scc0 4b\n\ts_branch 2b\n\t.subsection 0"
               : "+v"(v), "=&s"(left) : "s"(need), "v"(addr) : "vcc", "scc", "memory");
}
DI void glu_pack(const f32x4 (&a)[4][4], u32x4 (&hp)[4]) {
#pragma unroll
  for (int m = 0; m < 4; ++m) {
#pragma unroll
    for (int k = 0; k < 4; ++k) { const f32x2 h = glu2(a[m][k >> 1][2 * (k & 1)], a[m][k >> 1][2 * (k & 1) + 1], a[m][2 + (k >> 1)][2 * (k & 1)], a[m][2 + (k >> 1)][2 * (k & 1) + 1]); hp[m][k] = cvtpk(h[0], h[1]); }
    PIN(hp[m]);
  }
}

DI void dma16x4_sc1(srd_t srd, unsigned voff, unsigned soff, unsigned m0v) {
  asm volatile("s_mov_b32 m0, %0\n\ts_nop 0\n\tbuffer_load_dwordx4 %1, %2, %3 offen sc1 lds\n\tbuffer_load_dwordx4 %1, %2, %3 offen offset:1024 sc1 lds\n\t"
               "buffer_load_dwordx4 %1, %2, %3 offen offset:2048 sc1 lds\n\tbuffer_load_dwordx4 %1, %2, %3 offen offset:3072 sc1 lds" ::"s"(m0v), "v"(voff), "s"(srd), "s"(soff) : "memory");
}
DI void att_role(const u16* wo, char* ws, LAS char* lds, int head, int qb, int tg, int cg, int wid, u64 tag, const u32x4 (&hp)[4], const u32x4 (&ha)[4]) {
  const int tz = lane_id();
  const int lane = tz & 63;
  const char* kvg = ws + WS_KV + (size_t)head * 262144;
  const srd_t skv = mksrd(kvg, 262144), sw = mksrd(ws, WS_END);
  const unsigned ldsb = (unsigned)(uintptr_t)lds;
  const unsigned voff = lane * 16u;
  volatile LAS unsigned* ctl = (volatile LAS unsigned*)(lds + LDS_CTL);
  const unsigned ctla = ldsb + LDS_CTL + (lane & 7) * 4u;
  {
#pragma unroll
    for (int i = 0; i < 4; ++i) { *(LAS u32x4*)(lds + 131072 + wid * 4096 + i * 1024 + lane * 16) = hp[i]; *(LAS u32x4*)(lds + 49152 + wid * 4096 + i * 1024 + lane * 16) = ha[i]; }
    asm volatile("s_waitcnt lgkmcnt(0)" ::: "memory");
    ctl[12 + wid] = 1u;
  }
#define AT_ARRIVE(k) ctl[wid] = (unsigned)((k) + 2)
#define AT_MEET(v, k) lds_meet(v, (unsigned)((k) + 2), ctla)
  f32x16 o[2] = {f32x16{}, f32x16{}};
  f32x4 ls[2] = {f32x4{}, f32x4{}};
  bf16x8 sel;
  { const short one = ((lane & 1) == ((lane >> 4) & 1) && (lane & 2) == 0) ? (short)0x3f80 : (short)0;
#pragma unroll
    for (int e = 0; e < 8; ++e) sel[e] = one; }
  bf16x8 kf[2][2], vf[2][2], qf[2][2];
  f32x16 sa, sb, negm[2];
  u32x4 pa0, pa1, pb0 = u32x4{}, pb1 = u32x4{};
  vf[1][0] = bf16x8{}; vf[1][1] = bf16x8{};
  PIN(sel); PIN(o[0]); PIN(o[1]); PIN(ls[0]); PIN(ls[1]); PIN(pb0); PIN(pb1); PIN(vf[1][0]); PIN(vf[1][1]);
  const unsigned kp = ldsb + lane * 16u;
  {
    asm volatile("s_waitcnt vmcnt(0)" ::: "memory");
    const u32x2 f = *(volatile LAS const u32x2*)(lds + LDS_DUMP + wid * 1024 + lane * 16);
    if (!__all(f[0] == (unsigned)tag && f[1] == (unsigned)(tag >> 32))) await(ws, att_dep(head, qb, lane), tag);
  }
  dma16x4_sc1(skv, voff, wid * 4096u, ldsb + wid * 4096u);
  dma16x4_sc1(sw, voff, WS_Q + (head * 64 + qb * 8 + wid * 2) * 2048u, ldsb + 32768u + wid * 4096u);
  dma16x4_sc1(skv, voff, 16384u + wid * 4096u, ldsb + 16384u + wid * 4096u);
  asm volatile("s_waitcnt vmcnt(0)" ::: "memory");
  AT_ARRIVE(-1);
#pragma unroll
  for (int i = 0; i < 4; ++i) qf[i >> 1][i & 1] = lds128(LP(kp) + 32768 + wid * 4096 + i * 1024);
  asm volatile("" : "+v"(qf[0][0]), "+v"(qf[0][1]), "+v"(qf[1][0]), "+v"(qf[1][1]));
  lds_meet(ctl[lane & 3], 1u, ldsb + LDS_CTL + (lane & 3) * 4u);
  kf[0][0] = lds128(LP(kp)); kf[0][1] = lds128(LP(kp) + 1024);
  {
    sa = MFMA32(kf[0][0], qf[0][0], f32x16{}); sa = MFMA32(kf[0][1], qf[0][1], sa);
    sb = MFMA32(kf[0][0], qf[1][0], f32x16{}); sb = MFMA32(kf[0][1], qf[1][1], sb);
    dma16x4_sc1(skv, voff, 32768u + wid * 4096u, ldsb + 32768u + wid * 4096u);
    float ma = sa[0], mb = sb[0];
#pragma unroll
    for (int r = 1; r < 16; ++r) { ma = fmaxf(ma, sa[r]); mb = fmaxf(mb, sb[r]); }
    { const auto ra = __builtin_amdgcn_permlane32_swap(__float_as_uint(ma), __float_as_uint(ma), false, false), rb = __builtin_amdgcn_permlane32_swap(__float_as_uint(mb), __float_as_uint(mb), false, false);
      ma = fmaxf(__uint_as_float(ra[0]), __uint_as_float(ra[1])); mb = fmaxf(__uint_as_float(rb[0]), __uint_as_float(rb[1])); }
#pragma unroll
    for (int r = 0; r < 16; ++r) { negm[0][r] = -ma; negm[1][r] = -mb; sa[r] -= ma; }
  }
  PIN(negm[0]); PIN(negm[1]);
#define E1(S_, i) S_[i] = EX2(S_[i])
#define CV(P_, i, S_, r) P_[i] = cvtpk(S_[r], S_[(r) + 1])
#define AT_UNIT(SC, PC0, PC1, SN, kz, mn, PP0, PP1, vz, X0, X1, X2, X3)                                       \
  do {                                                                                                       \
    SN = MFMA32(kf[kz][0], qf[mn][0], negm[mn]); SBAR(); X0;                                                  \
    E1(SC, 0); E1(SC, 1); E1(SC, 2); E1(SC, 3); PIN(SC); SBAR();                                               \
    SN = MFMA32(kf[kz][1], qf[mn][1], SN); SBAR(); X1;                                                        \
    CV(PC0, 0, SC, 0); CV(PC0, 1, SC, 2); E1(SC, 4); E1(SC, 5); E1(SC, 6); PIN(SC); PIN(PC0); SBAR();           \
    o[mn] = MFMA32(vf[vz][0], BF(PP0), o[mn]); SBAR(); X2;                                                    \
    E1(SC, 7); CV(PC0, 2, SC, 4); E1(SC, 8); CV(PC0, 3, SC, 6); E1(SC, 9); PIN(SC); PIN(PC0); SBAR();           \
    ls[mn] = MFMA16(sel, BF(PP0), ls[mn]); SBAR();                                                            \
    E1(SC, 10); E1(SC, 11); PIN(SC); SBAR();                                                                  \
    o[mn] = MFMA32(vf[vz][1], BF(PP1), o[mn]); SBAR(); X3;                                                    \
    CV(PC1, 0, SC, 8); E1(SC, 12); CV(PC1, 1, SC, 10); E1(SC, 13); E1(SC, 14); E1(SC, 15); PIN(SC); PIN(PC1); SBAR(); \
    ls[mn] = MFMA16(sel, BF(PP1), ls[mn]); SBAR();                                                            \
    CV(PC1, 2, SC, 12); CV(PC1, 3, SC, 14); PIN(PC1); SBAR();                                                  \
  } while (0)
#define AT_KT(z, KADDR, VADDR, D0, D1)                                                                        \
  do {                                                                                                       \
    AT_UNIT(sa, pa0, pa1, sb, z, 1, pb0, pb1, z ^ 1, kf[z ^ 1][0] = lds128(KADDR), kf[z ^ 1][1] = lds128((KADDR) + 1024), vf[z][0] = lds128(VADDR), vf[z][1] = lds128((VADDR) + 1024)); \
    asm volatile("" : "+v"(kf[z ^ 1][0]), "+v"(kf[z ^ 1][1]), "+v"(vf[z][0]), "+v"(vf[z][1]));   \
    AT_UNIT(sb, pb0, pb1, sa, z ^ 1, 0, pa0, pa1, z, D0, NOP_, D1, NOP_);                                      \
  } while (0)
  const unsigned vwo = g2_vw(lane, cg), m0b = ldsb + wid * 4096u;
  unsigned arr = 2u, ctlw = ldsb + LDS_CTL + wid * 4u;
  PIN(arr); PIN(ctlw);
#define AT_INT(p, SRD, VO, SO, ST)                                                                            \
  do {                                                                                                       \
    unsigned pv;                                                                                             \
    AT_KT(0, LP(kp) + (p) * 16384 + 4096, LP(kp) + (p) * 16384 + 2048, NOP_, NOP_);                           \
    AT_KT(1, LP(kp) + (p) * 16384 + 8192, LP(kp) + (p) * 16384 + 6144, pv = ctl[lane & 7], NOP_);             \
    lds_meet_fast(pv, 4 * it + (p) + 1, ctla);     \
    SBAR();                                                                                                  \
    AT_KT(0, LP(kp) + (p) * 16384 + 12288, LP(kp) + (p) * 16384 + 10240, dma16<true>(SRD, VO, (SO), m0b + (((p) + 3) & 3) * 16384u), dma16<true>(SRD, VO, (SO) + (ST), m0b + (((p) + 3) & 3) * 16384u + 1024u)); \
    AT_KT(1, LP(kp) + (((p) + 1) & 3) * 16384, LP(kp) + (p) * 16384 + 14336, dma16<true>(SRD, VO, (SO) + 2 * (ST), m0b + (((p) + 3) & 3) * 16384u + 2048u), dma16<true>(SRD, VO, (SO) + 3 * (ST), m0b + (((p) + 3) & 3) * 16384u + 3072u)); \
    asm volatile("s_waitcnt vmcnt(4)\n\tds_write_b32 %1, %0\n\tv_add_u32 %0, 1, %0" : "+v"(arr) : "v"(ctlw) : "memory");     \
  } while (0)
  for (int it = 0; it < 4; ++it) {
    const bool kvq = it < 3;
    const srd_t sq_ = mksrd(kvq ? (const void*)kvg : (const void*)wo, kvq ? 262144u : D * D * 2u);
    const unsigned sk_ = (4 * it + 3) * 16384u + wid * 4096u;
    const unsigned vq_ = kvq ? voff : vwo, so_ = kvq ? sk_ + 16384u : (8 * wid + 5) * 65536u, st_ = kvq ? 1024u : 16384u, sn_ = kvq ? 16384u : 65536u;
    AT_INT(0, skv, voff, sk_, 1024u);
    AT_INT(1, sq_, vq_, so_, st_);
    AT_INT(2, sq_, vq_, so_ + sn_, st_);
    AT_INT(3, sq_, vq_, so_ + 2 * sn_, st_);
  }
  o[1] = MFMA32(vf[1][0], BF(pb0), o[1]); ls[1] = MFMA16(sel, BF(pb0), ls[1]);
  o[1] = MFMA32(vf[1][1], BF(pb1), o[1]); ls[1] = MFMA16(sel, BF(pb1), ls[1]);
  auto store_o = [&](int m, float inv) {
    const unsigned T = qb * 8 + wid * 2 + m;
    const unsigned so = WS_X2 + ((T >> 2) * NKB + 112 + head) * 8192u + (T & 3) * 512u, vo = (lane >> 5) * 2048u + (lane & 31) * 16u;
    st16_sc1(sw, vo, so, pack8(o[m], 0, inv));
    st16_sc1(sw, vo, so + 4096u, pack8(o[m], 1, inv));
  };
  float lt[2];
#pragma unroll
  for (int m = 0; m < 2; ++m) { lt[m] = (lane & 16) ? ls[m][1] : ls[m][0]; store_o(m, __builtin_amdgcn_rcpf(lt[m])); }
  bool bad = false;
#pragma unroll
  for (int m = 0; m < 2; ++m) {
    float chk = lt[m], nanp = lt[m];
#pragma unroll
    for (int r = 0; r < 16; ++r) { chk = fmaxf(chk, fabsf(o[m][r])); nanp += o[m][r]; }
    bad = bad || !(chk < 1e30f) || (nanp != nanp);
  }
  if (__builtin_expect(__any(bad), 0)) {
    float linv[2];
    asm volatile("s_waitcnt vmcnt(0)" ::: "memory");
    att_slow(kvg, qf, o, linv, lane);
    store_o(0, linv[0]); store_o(1, linv[1]);
  }
  asm volatile("s_waitcnt vmcnt(0)" ::: "memory");
  publish_last(ws, ldsb + LDS_CTL + 17 * 4u, 4u, FLG_F4 + qb * 32 + head, tag, lane);
  await(ws, FLG_F4 + (tg >> 1) * 32 + 8 * wid + (lane & 7), tag);
  { unsigned pv = 0; AT_MEET(pv, 15); }
  ctl[8 + wid] = 1u;
}

constexpr int g2_cnt(int m, bool x0, bool w0, bool x1, bool w1, bool w2) {
  return (7 - m) + (w2 ? (8 - m) >> 1 : 0) + (x1 ? 8 : 0) + (w1 ? 4 : 0) + (x0 ? m : 0) + (w0 ? (m + 1) >> 1 : 0);
}
DI void g2_role(const u16* x, u32x4 (&xr)[2], const u16* wo, const u16* wd, char* ws, LAS char* lds, int tg, int cg, int kq, u64 tag, const u32x4 (&hp)[4], const u32x4 (&ha)[4], int mt, int j, srd_t scode) {
  const int tz = lane_id();
  const int lane = tz & 63;
  const srd_t sx2 = mksrd(ws + WS_X2 + (size_t)tg * NKB * 8192, NKB * 8192);
  const srd_t swd = mksrd(wd, F * D * 2), swo = mksrd(wo, D * D * 2);
  const unsigned ldsw = (unsigned)(uintptr_t)lds + P2_G2RING + kq * 16384u;
  const unsigned vw = g2_vw(lane, cg);
  const int t16 = lane & 15, g = lane >> 4, q4 = t16 >> 2, p4 = lane & 3;
  const unsigned vx = g * 2048u + t16 * 16u;
  volatile LAS unsigned* ctl = (volatile LAS unsigned*)(lds + LDS_CTL);
  unsigned wa[4];
  f32x4 acc[8][4];
  {
    u32x4 hq[8];
    const srd_t sw = mksrd(ws, WS_END);
    const unsigned cb = (unsigned)(uintptr_t)lds + LDS_CTL;
    lds_meet(0u, 1u, cb + (12 + kq) * 4u);
#pragma unroll
    for (int i = 0; i < 8; ++i) hq[i] = *(LAS const u32x4*)(lds + (i < 4 ? 131072 : 49152 - 4096) + kq * 4096 + i * 1024 + lane * 16);
#pragma unroll
    for (int i = 0; i < 8; ++i) PIN(hq[i]);
    ctl[4 + kq] = ~0u;
#pragma unroll
    for (int c = 0; c < 4; ++c) { wa[c] = ldsw + (4 * g + q4) * 128 + ((c ^ ((2 * g + (q4 >> 1)) & 3)) << 5) + p4 * 8; PIN(wa[c]); }
#pragma unroll
    for (int m = 0; m < 8; ++m)
#pragma unroll
      for (int c = 0; c < 4; ++c) { acc[m][c] = f32x4{}; PIN(acc[m][c]); }
    lds_meet(0u, 1u, cb + (lane & 3) * 4u);
    const int pair = j < 24 ? 2 * j + 1 : 24 + j;
#pragma unroll
    for (int m = 0; m < 4; ++m) { st16_sc1(sw, vx, h_base(mt, pair, kq, 1) + m * 256u, hp[m]); st16_sc1(sw, vx, h_base(mt, pair, kq, 0) + m * 256u, hq[m]); }
    if (j < 24) {
#pragma unroll
      for (int m = 0; m < 4; ++m) { st16_sc1(sw, vx, h_base(mt, 2 * j, kq, 1) + m * 256u, ha[m]); st16_sc1(sw, vx, h_base(mt, 2 * j, kq, 0) + m * 256u, hq[4 + m]); }
    }
    asm volatile("s_waitcnt vmcnt(0)" ::: "memory");
    publish_last(ws, cb + 16 * 4u, 4u, FLG_F3 + mt * 32 + j, tag, lane);
  }
  if (blockIdx.x == 0 && kq == 0) {
#pragma nounroll
    for (int i = 0; i < 4; ++i) await(ws, FLG_F1 + i * 64 + lane, tag);
    publish(ws, 0, tag, lane);
  }
  await(ws, FLG_F3 + (tg >> 1) * 32 + 7 * kq + (lane < (kq == 3 ? 11 : 7) ? lane : 0), tag);
  bf16x8 xf[2][8];
  unsigned xo = (unsigned)(28 * kq) * 8192u, wo_ = (unsigned)(28 * kq + 4) * 65536u;
#pragma unroll
  for (int z = 0; z < 2; ++z) {
#pragma unroll
    for (int m = 0; m < 8; ++m) ld16<true>(xf[z][m], sx2, vx, xo, m * 256);
    xo += 8192u; }
  s16x4 wl[2][4], wh[2][4];
#define G2_RW(z, off, c) do { LAS const char* p_ = LP(wa[c]) + (off); wl[z][c] = ldstr(p_); wh[z][c] = ldstr(p_ + 2048); } while (0)
#pragma unroll
  for (int c = 0; c < 4; ++c) G2_RW(0, 0, c);
  SBAR();
#define G2_KB(sl, WS_, RD, X0, W0, X1, W1, W2) G2_KBX(sl, WS_, RD, (((sl) + 1) & 3) * 4096, X0, W0, X1, W1, W2, 0, NOP_)
#define G2_KBX(sl, WS_, RD, ROFF, X0, W0, X1, W1, W2, EXTRA, HOOK)                                                                \
  do {                                                                                                       \
    constexpr int z = (sl) & 1;                                                                              \
    _Pragma("unroll") for (int m = 0; m < 8; ++m) {                                                          \
      switch (m) {                                                                                           \
        case 0: mfma16w<g2_cnt(0, X0, W0, X1, W1, W2) + (EXTRA)>(acc[m][0], cat8(wl[z][0], wh[z][0]), xf[z][m]); break; \
        case 1: mfma16w<g2_cnt(1, X0, W0, X1, W1, W2) + (EXTRA)>(acc[m][0], cat8(wl[z][0], wh[z][0]), xf[z][m]); break; \
        case 2: mfma16w<g2_cnt(2, X0, W0, X1, W1, W2) + (EXTRA)>(acc[m][0], cat8(wl[z][0], wh[z][0]), xf[z][m]); break; \
        case 3: mfma16w<g2_cnt(3, X0, W0, X1, W1, W2) + (EXTRA)>(acc[m][0], cat8(wl[z][0], wh[z][0]), xf[z][m]); break; \
        case 4: mfma16w<g2_cnt(4, X0, W0, X1, W1, W2) + (EXTRA)>(acc[m][0], cat8(wl[z][0], wh[z][0]), xf[z][m]); break; \
        case 5: mfma16w<g2_cnt(5, X0, W0, X1, W1, W2) + (EXTRA)>(acc[m][0], cat8(wl[z][0], wh[z][0]), xf[z][m]); break; \
        case 6: mfma16w<g2_cnt(6, X0, W0, X1, W1, W2) + (EXTRA)>(acc[m][0], cat8(wl[z][0], wh[z][0]), xf[z][m]); break; \
        default: mfma16w<g2_cnt(7, X0, W0, X1, W1, W2) + (EXTRA)>(acc[m][0], cat8(wl[z][0], wh[z][0]), xf[z][m]); break; \
      }                                                                                                      \
      SBAR();                                                                                                \
      if (RD && m >= 2 && m < 6) G2_RW(z ^ 1, ROFF, m - 2);                                \
      SBAR();                                                                                                \
      mfma16a(acc[m][1], cat8(wl[z][1], wh[z][1]), xf[z][m]);                                                 \
      mfma16a(acc[m][2], cat8(wl[z][2], wh[z][2]), xf[z][m]);                                                 \
      mfma16a(acc[m][3], cat8(wl[z][3], wh[z][3]), xf[z][m]); SBAR();                                         \
      HOOK;                                                                                                  \
      if (X0) ld16_inplace(xf[z][m], sx2, vx, xo, m * 256);                                                  \
      if (W0 && !(m & 1)) dma16<false>(WS_, vw, wo_ + (m >> 1) * 16384u, ldsw + (sl) * 4096u + (m >> 1) * 1024u); \
      SBAR();                                                                                                \
    }                                                                                                        \
    xo += 8192u; wo_ += 65536u;                                                                              \
  } while (0)
#define YES true
#define NO false
  G2_KB(0, swd, YES, YES, YES, YES, NO, NO); G2_KB(1, swd, YES, YES, YES, YES, YES, NO); G2_KB(2, swd, YES, YES, YES, YES, YES, YES); G2_KB(3, swd, YES, YES, YES, YES, YES, YES);
  for (int i = 4; i < 24; i += 4) {
    dma16<false>(scode, vx, (unsigned)((mt * 8192) + ((j >> 3) & 1) * 1024 + kq * 256), (unsigned)(uintptr_t)lds + LDS_DUMP + (4 + kq) * 1024u);
    G2_KB(0, swd, YES, YES, YES, YES, YES, YES); G2_KB(1, swd, YES, YES, YES, YES, YES, YES); G2_KB(2, swd, YES, YES, YES, YES, YES, YES); G2_KB(3, swd, YES, YES, YES, YES, YES, YES);
  }
  wo_ = (unsigned)(8 * kq) * 65536u;
  G2_KB(0, swo, YES, YES, YES, YES, YES, YES); G2_KB(1, swo, YES, YES, YES, YES, YES, YES);
  G2_KB(2, swo, YES, NO, YES, YES, YES, YES); G2_KB(3, swo, YES, NO, YES, NO, YES, YES);
  asm volatile("" : "+v"(wl[0][0]), "+v"(wh[0][0]), "+v"(wl[0][1]), "+v"(wh[0][1]), "+v"(wl[0][2]), "+v"(wh[0][2]), "+v"(wl[0][3]), "+v"(wh[0][3]));
#pragma unroll
  for (int p = 0; p < 4; ++p) dma16<false>(swo, vw, wo_ + p * 16384u, ldsw + p * 1024u);
  lds_meet(0u, 1u, (unsigned)(uintptr_t)lds + LDS_CTL + (8 + kq) * 4u);
  xo = (unsigned)(112 + 8 * kq) * 8192u;
#pragma unroll
  for (int z = 0; z < 2; ++z) {
#pragma unroll
    for (int m = 0; m < 8; ++m) ld16_inplace(xf[z][m], sx2, vx, xo, m * 256);
    xo += 8192u; }
  G2_KB(0, swo, YES, YES, NO, YES, NO, NO); G2_KB(1, swo, YES, YES, NO, YES, NO, NO); G2_KB(2, swo, YES, YES, NO, YES, NO, NO); G2_KB(3, swo, YES, YES, NO, YES, NO, NO);
#pragma unroll
  for (int c = 0; c < 4; ++c) wa[c] -= P2_G2RING + kq * 12288u;
  G2_KBX(0, swo, YES, 0, YES, NO, YES, NO, NO, 0, NOP_); G2_KBX(1, swo, YES, 16384, YES, NO, YES, NO, NO, 0, NOP_);
  G2_KBX(2, swo, YES, 32768, NO, NO, YES, NO, NO, 0, NOP_);
  const int ty = lane_id();
  { const srd_t sxi = mksrd(x, S * D * 2);
    const unsigned xv = (unsigned)((ty & 63) >> 3) * 2048u + (ty & 7) * 16u, xs = (unsigned)(tg * 128 + (4 + kq) * 16) * 2048u + cg * 128u;
    ld16<false>(xr[0], sxi, xv, xs, 0); ld16<false>(xr[1], sxi, xv, xs + 16384u, 0); }
  unsigned pa[4];
#pragma unroll
  for (int c = 0; c < 4; ++c) pa[c] = (unsigned)(uintptr_t)lds + kq * 4096u + (ty & 15) * 256u + (((4 * c + ((ty >> 4) & 3)) ^ (ty & 15)) << 4);
  const unsigned pown = P2_G2RING + kq * 12288u;
#define G2_PUT(mm) do { _Pragma("unroll") for (int c = 0; c < 4; ++c) *(LAS f32x4*)((LAS char*)(uintptr_t)((mm) < 4 ? pa[c] : pa[c] + pown) + ((mm) < 4 ? (mm) * 16384 : ((mm) - 4) * 4096)) = acc[mm][c]; } while (0)
  G2_KBX(3, swo, NO, 0, NO, NO, NO, NO, NO, 2, if (m >= 2) G2_PUT(m >= 2 ? m - 2 : 0));
  asm volatile("s_nop 7" ::: "memory");
  G2_PUT(6); G2_PUT(7);
  asm volatile("s_waitcnt vmcnt(0)" : "+v"(xr[0]), "+v"(xr[1]));
}

__global__ __launch_bounds__(512, 2) void fused_kernel(const u16* x, const u16* wqkv, const u16* wo, const u16* wg, const u16* wu, const u16* wd, char* ws, u16* out) {
  extern __shared__ __attribute__((aligned(16))) char smem[];
  LAS char* lds = (LAS char*)smem;
  const int b = blockIdx.x, xcd = b & 7, r = b >> 3, wid = rfl(threadIdx.x >> 6);
  u64 tag = *(const __attribute__((address_space(4))) u64*)(ws + WS_FLG);
  ((LAS unsigned*)(lds + LDS_CTL))[threadIdx.x & 31] = 0u;
  const int tg = (xcd >> 1) * 4 + (r & 3), cg = (xcd & 1) * 8 + (r >> 2);
  const int mt = r & 7, j = xcd + 8 * (r >> 3);
  srd_t scode;
  { u64 pc; unsigned len;
    asm volatile("s_getpc_b64 %0\n1:\n\ts_mov_b32 %1, .Lfk_code_end-1b" : "=s"(pc), "=s"(len));
    scode = mksrd((const void*)(pc & ~127ull), len); }
  ln_row(x, ws, mt * 256 + j * 8 + wid, scode, (unsigned)((r * 8 + wid) & 127) * 1024u, (unsigned)(uintptr_t)lds + LDS_DUMP + wid * 1024u);
  asm volatile("s_waitcnt vmcnt(0)" : "+s"(tag)::"memory");
  tag = tag * 6364136223846793005ull + 1442695040888963407ull;
  asm volatile("s_barrier" ::: "memory");
  if (wid == 0) publish(ws, FLG_F1 + mt * 32 + j, tag, lane_id());
  const int head = xcd * 4 + (r >> 3);
  f32x4 accb[4][4];
  u32x4 ha[4];
  if (j < 16) g1_body<0>(head, wo, wd, cg, wqkv, wg, wu, ws, lds, wid, mt, j, scode, tag, accb, ha);
  else if (j < 24) g1_body<1>(head, wo, wd, cg, wqkv, wg, wu, ws, lds, wid, mt, j, scode, tag, accb, ha);
  else g1_body<2>(head, wo, wd, cg, wqkv, wg, wu, ws, lds, wid, mt, j, scode, tag, accb, ha);
  u32x4 hp[4];
  glu_pack(accb, hp);
  u32x4 xr[2];
  if (wid >= 4) {
    g2_role(x, xr, wo, wd, ws, lds, tg, cg, wid - 4, tag, hp, ha, mt, j, scode);
  } else {
    att_role(wo, ws, lds, head, mt, tg, cg, wid, tag, hp, ha);
    const int ty = lane_id();
#pragma unroll
    for (int e = 0; e < 2; ++e) xr[e] = *(const u32x4*)(x + (size_t)(tg * 128 + wid * 16 + e * 8 + ((ty & 63) >> 3)) * D + cg * 64 + (ty & 7) * 8);
  }
  const int ln = lane_id();
  unsigned ra[4][4], vo = (unsigned)((tg * 128 + wid * 16 + (ln >> 3)) * D + cg * 64 + (ln & 7) * 8) * 2u;
  f32x4 sum[4];
#pragma unroll
  for (int i = 0; i < 4; ++i) {
    const unsigned tk = (i >> 1) * 8 + (ln >> 3);
#pragma unroll
    for (int k = 0; k < 4; ++k) { ra[i][k] = (unsigned)(uintptr_t)lds + (wid < 4 ? wid * 16384 + k * 4096 : P2_G2RING + (wid - 4) * 4096 + k * 16384) + tk * 256 + (((2 * (ln & 7) + (i & 1)) ^ tk) << 4); PIN(ra[i][k]); }
    sum[i] = f32x4{blo(xr[i >> 1][2 * (i & 1)]), bhi(xr[i >> 1][2 * (i & 1)]), blo(xr[i >> 1][2 * (i & 1) + 1]), bhi(xr[i >> 1][2 * (i & 1) + 1])};
    PIN(sum[i]);
  }
  PIN(vo);
  asm volatile("s_waitcnt lgkmcnt(0)\n\ts_barrier" ::: "memory");
  f32x4 part[4][4];
#pragma unroll
  for (int i = 0; i < 4; ++i)
#pragma unroll
    for (int k = 0; k < 4; ++k) part[i][k] = *(LAS const f32x4*)LP(ra[i][k]);
  SBAR();
#pragma unroll
  for (int i = 0; i < 4; ++i) sum[i] += (part[i][0] + part[i][1]) + (part[i][2] + part[i][3]);
  st16_sc1(mksrd(out, S * D * 2), vo, 0u, pack44(sum[0], sum[1], 1.f));
  st16_sc1(mksrd(out, S * D * 2), vo, 8u * D * 2u, pack44(sum[2], sum[3], 1.f));
  asm volatile(".Lfk_code_end:");
}

extern "C" void kernel_launch(void* const* d_in, const int* in_sizes, int n_in, void* d_out, int out_size, void* d_ws, size_t ws_size, hipStream_t stream) {
  (void)hipFuncSetAttribute((const void*)fused_kernel, hipFuncAttributeMaxDynamicSharedMemorySize, LDS_BYTES);
  fused_kernel<<<256, 512, LDS_BYTES, stream>>>((const u16*)d_in[0], (const u16*)d_in[1], (const u16*)d_in[2], (const u16*)d_in[4], (const u16*)d_in[5], (const u16*)d_in[6], (char*)d_ws, (u16*)d_out);
}
```
